```python
import torch

def _softmax(x, axis=-1):
    return torch.softmax(x, dim=axis)
def _gelu(x):
    return torch.nn.functional.gelu(x, approximate="tanh")
def _silu(x):
    return torch.nn.functional.silu(x)
def _sigmoid(x):
    return torch.sigmoid(x)
def _layernorm(x, axis=-1, eps=1e-5):
    m = x.mean(dim=axis, keepdim=True)
    v = x.var(dim=axis, keepdim=True, unbiased=False)
    return (x - m) / torch.sqrt(v + eps)
def _scatter_add(tgt, idx, src, axis=0):
    return tgt.clone().index_add_(axis, idx, src)
def _segment_sum(src, seg, n):
    out = torch.zeros((n,) + src.shape[1:], dtype=src.dtype)
    return out.index_add_(0, seg, src)
def _pad(x, pw):
    flat = tuple(v for pair in reversed(pw) for v in pair)
    return torch.nn.functional.pad(x, flat)

kernel_name = 'gated_mix__linear_attn_block__linear_attn_block__prompt'

def setup_inputs():

    _w = (lambda a: (lambda t: t.to(torch.bfloat16) if t.is_floating_point() else t)(torch.as_tensor(a)))
    def _A_setup():
        B, S, D = 1, 2048, 1024
        return {
            "x": _w((lambda *s: torch.randn(*s, dtype=torch.bfloat16) * 0.05)(B, S, D)),
            "wqkv": _w((lambda *s: torch.randn(*s, dtype=torch.bfloat16) * 0.8)(D, 3 * D)),
            "wo": _w((lambda *s: torch.randn(*s, dtype=torch.bfloat16) * 0.05)(D, D)),
        }
    _A_in = _A_setup()

    def _B_setup():
        B, S, D = 1, 2048, 1024
        return {
            "x": _w((lambda *s: torch.randn(*s, dtype=torch.bfloat16) * 0.05)(B, S, D)),
            "wqkv": _w((lambda *s: torch.randn(*s, dtype=torch.bfloat16) * 0.8)(D, 3 * D)),
            "wo": _w((lambda *s: torch.randn(*s, dtype=torch.bfloat16) * 0.05)(D, D)),
        }
    _B_in = _B_setup()

    return {
        "A_x": _A_in["x"],
        "A_wqkv": _A_in["wqkv"],
        "A_wo": _A_in["wo"],
        "B_x": _B_in["x"],
        "B_wqkv": _B_in["wqkv"],
        "B_wo": _B_in["wo"],
        "gate_w": _w((lambda *s: torch.randn(*s, dtype=torch.bfloat16) * 0.05)(1024) * 16.0),
    }

def reference(A_x, A_wqkv, A_wo, B_x, B_wqkv, B_wo, gate_w):
    def _A_ref(x, wqkv, wo):
        B, S, D = x.shape
        H = 32; Dh = D // H
        qkv = (lambda x, *d: x.permute(*d))((x @ wqkv).reshape(B, S, 3, H, Dh), 2, 0, 3, 1, 4)
        q, k, v = qkv[0], qkv[1], qkv[2]
        phi = lambda t: torch.where(t > 0, t + 1, torch.exp(t)) + 1e-6
        pq, pk = phi(q), phi(k)
        kv = torch.einsum("bhsd,bhse->bhde", pk, v)
        z = torch.einsum("bhsd->bhd", pk)
        num = torch.einsum("bhqd,bhde->bhqe", pq, kv)
        den = torch.einsum("bhqd,bhd->bhq", pq, z)[..., None] + 1e-6
        o = num / den
        return (lambda x, *d: x.permute(*d))(o, 0, 2, 1, 3).reshape(B, S, D) @ wo

    def _B_ref(x, wqkv, wo):
        B, S, D = x.shape
        H = 32; Dh = D // H
        qkv = (lambda x, *d: x.permute(*d))((x @ wqkv).reshape(B, S, 3, H, Dh), 2, 0, 3, 1, 4)
        q, k, v = qkv[0], qkv[1], qkv[2]
        phi = lambda t: torch.where(t > 0, t + 1, torch.exp(t)) + 1e-6
        pq, pk = phi(q), phi(k)
        kv = torch.einsum("bhsd,bhse->bhde", pk, v)
        z = torch.einsum("bhsd->bhd", pk)
        num = torch.einsum("bhqd,bhde->bhqe", pq, kv)
        den = torch.einsum("bhqd,bhd->bhq", pq, z)[..., None] + 1e-6
        o = num / den
        return (lambda x, *d: x.permute(*d))(o, 0, 2, 1, 3).reshape(B, S, D) @ wo

    x = A_x
    g = _sigmoid((x * gate_w).sum(-1, keepdims=True))
    a = _A_ref(x, A_wqkv, A_wo)
    b = _B_ref(x, B_wqkv, B_wo)
    return g * a + (1 - g) * b
```

```cpp
#include <hip/hip_runtime.h>
#include <hip/hip_bf16.h>
typedef unsigned short u16;
__device__ __forceinline__ float bf2f(u16 v){ return __uint_as_float(((unsigned)v)<<16); }
__device__ __forceinline__ u16 f2bf(float f){ __hip_bfloat16 b = __float2bfloat16(f); return *(u16*)&b; }
__device__ __forceinline__ float rbf(float f){ return bf2f(f2bf(f)); }

__global__ void gemm_naive(const u16* A, const u16* B, u16* C, int M, int N, int K){
  __shared__ float As[32][33], Bs[32][33];
  int tx=threadIdx.x, ty=threadIdx.y;
  int row=blockIdx.y*32+ty, col=blockIdx.x*32+tx;
  float acc=0;
  for(int k0=0;k0<K;k0+=32){
    As[ty][tx]=bf2f(A[(size_t)row*K+k0+tx]);
    Bs[ty][tx]=bf2f(B[(size_t)(k0+ty)*N+col]);
    __syncthreads();
    for(int k=0;k<32;k++) acc+=As[ty][k]*Bs[k][tx];
    __syncthreads();
  }
  C[(size_t)row*N+col]=f2bf(acc);
}
__device__ __forceinline__ float phi(float t){ float r = t>0.f ? rbf(t+1.f) : rbf(__expf(t)); return rbf(r+1e-6f); }
__global__ void kv_naive(const u16* qkv, float* kv, float* z){
  int h=blockIdx.x; int d=threadIdx.y, e=threadIdx.x;
  float acc=0, zz=0;
  for(int s=0;s<2048;s++){
    float pk=phi(bf2f(qkv[(size_t)s*3072+1024+h*32+d]));
    float v=bf2f(qkv[(size_t)s*3072+2048+h*32+e]);
    acc+=pk*v; zz+=pk;
  }
  kv[h*1024+d*32+e]=rbf(acc);
  if(e==0) z[h*32+d]=rbf(zz);
}
__global__ void o_naive(const u16* qkv, const float* kv, const float* z, u16* o){
  int s=blockIdx.x; int c=threadIdx.x; int h=c>>5, e=c&31;
  float num=0, den=0;
  for(int d=0;d<32;d++){
    float pq=phi(bf2f(qkv[(size_t)s*3072+h*32+d]));
    num+=pq*kv[h*1024+d*32+e]; den+=pq*z[h*32+d];
  }
  num=rbf(num); den=rbf(rbf(den)+1e-6f);
  o[(size_t)s*1024+c]=f2bf(num/den);
}
__global__ void mix_naive(const u16* x, const u16* gw, const u16* a, const u16* b, u16* out){
  __shared__ float red[1024];
  int s=blockIdx.x, c=threadIdx.x;
  red[c]=rbf(bf2f(x[(size_t)s*1024+c])*bf2f(gw[c]));
  __syncthreads();
  for(int o=512;o>0;o>>=1){ if(c<o) red[c]+=red[c+o]; __syncthreads(); }
  float t=rbf(red[0]);
  float g=rbf(1.f/(1.f+__expf(-t)));
  float r=rbf(g*bf2f(a[(size_t)s*1024+c]))+rbf(rbf(1.f-g)*bf2f(b[(size_t)s*1024+c]));
  out[(size_t)s*1024+c]=f2bf(r);
}
extern "C" void kernel_launch(void* const* d_in, const int* in_sizes, int n_in,
                              void* d_out, int out_size, void* d_ws, size_t ws_size,
                              hipStream_t stream) {
  const u16* x=(const u16*)d_in[0];
  const u16* w[2]={(const u16*)d_in[1],(const u16*)d_in[4]};
  const u16* wo[2]={(const u16*)d_in[2],(const u16*)d_in[5]};
  const u16* gw=(const u16*)d_in[6];
  char* ws=(char*)d_ws;
  u16* ab[2];
  for(int br=0;br<2;br++){
    char* base=ws+(size_t)br*(32<<20);
    u16* qkv=(u16*)base; float* kv=(float*)(base+(13<<20)); float* z=(float*)(base+(14<<20));
    u16* o=(u16*)(base+(16<<20)); u16* a=(u16*)(base+(22<<20)); ab[br]=a;
    gemm_naive<<<dim3(3072/32,2048/32),dim3(32,32),0,stream>>>(x,w[br],qkv,2048,3072,1024);
    kv_naive<<<32,dim3(32,32),0,stream>>>(qkv,kv,z);
    o_naive<<<2048,1024,0,stream>>>(qkv,kv,z,o);
    gemm_naive<<<dim3(1024/32,2048/32),dim3(32,32),0,stream>>>(o,wo[br],a,2048,1024,1024);
  }
  mix_naive<<<2048,1024,0,stream>>>(x,gw,ab[0],ab[1],(u16*)d_out);
}
```

```cpp
#include <hip/hip_runtime.h>

__global__ __launch_bounds__(256, 1) __attribute__((aligned(4096))) void gm_fused(const void* x, const void* wa, const void* woa, const void* wb, const void* wob,
                                                   const void* gw, void* out, void* ws) {
  __shared__ __attribute__((aligned(16))) char lds[163840];
  asm volatile(
      ".Lgm_first:\n"
      "s_load_dwordx16 s[16:31], %0, 0\n"
      "s_mov_b32 s33, %1\n"
      "v_lshrrev_b32 v44, 6, %2\n"
      "v_and_b32 v31, 63, %2\n"
      "v_readfirstlane_b32 s4, v44\n"
      "s_and_b32 s11, s33, 1\n"
      "s_lshr_b32 s12, s33, 6\n"
      "s_lshl2_add_u32 s7, s11, s12\n"
      "s_bfe_u32 s11, s33, 0x20001\n"
      "s_bfe_u32 s12, s33, 0x30003\n"
      "s_lshl3_add_u32 s8, s11, s12\n"
      "s_mov_b32 s97, 0\n"
      "s_bfe_u32 s12, s33, 0x20001\n"
      "s_mul_i32 s12, s12, 4\n"
      "s_add_u32 s97, s97, s12\n"
      "s_bfe_u32 s12, s33, 0x10000\n"
      "s_mul_i32 s12, s12, 8\n"
      "s_add_u32 s97, s97, s12\n"
      "s_and_b32 s5, s4, 1\n"
      "s_lshr_b32 s6, s4, 1\n"
      "s_lshl_b32 s14, s4, 12\n"
      "v_lshrrev_b32 v44, 3, v31\n"
      "v_and_b32 v45, 7, v31\n"
      "v_lshrrev_b32 v46, 4, v31\n"
      "v_xor_b32 v45, v45, v46\n"
      "v_lshlrev_b32 v44, 11, v44\n"
      "v_lshl_add_u32 v1, v45, 4, v44\n"
      "v_xor_b32 v2, 64, v1\n"
      "v_add_u32 v2, 0x3c00, v2\n"
      "v_add_u32 v3, 0x7800, v1\n"
      "v_add_u32 v4, 0x7800, v2\n"
      "v_bfe_u32 v44, v31, 2, 2\n"
      "v_bfe_u32 v45, v31, 4, 1\n"
      "v_lshl_add_u32 v46, v45, 3, v44\n"
      "v_bfe_u32 v47, v31, 1, 1\n"
      "v_xor_b32 v47, v47, v45\n"
      "v_and_b32 v48, 1, v31\n"
      "v_lshlrev_b32 v48, 4, v48\n"
      "v_lshl_or_b32 v48, v47, 5, v48\n"
      "v_lshrrev_b32 v49, 5, v31\n"
      "v_lshl_or_b32 v50, v49, 6, v48\n"
      "v_mul_u32_u24 v46, 0x1800, v46\n"
      "v_add_u32 v5, v46, v50\n"
      "v_add_u32 v6, 0x400, v5\n"
      "v_add_u32 v7, 0x800, v5\n"
      "s_lshl_b32 s11, s7, 19\n"
      "s_lshl_b32 s12, s6, 18\n"
      "s_add_u32 s11, s11, s12\n"
      "s_lshl_b32 s12, s5, 16\n"
      "s_add_u32 s12, s11, s12\n"
      "s_lshl2_add_u32 s15, s6, s5\n"
      "s_mul_i32 s15, s15, 0x6000\n"
      "s_and_b32 s90, s8, 15\n"
      "s_lshl_b32 s90, s90, 7\n"
      "s_add_u32 s15, s15, s90\n"
      "s_mul_i32 s98, s4, 0xc00\n"
      "s_add_u32 s98, s98, 0x4000\n"
      "s_getpc_b64 s[44:45]\n"
      "s_lshr_b32 s90, s33, 3\n"
      "s_lshl2_add_u32 s90, s90, s4\n"
      "s_mul_i32 s90, s90, 0x180\n"
      "v_min_u32 v51, 2, v31\n"
      "v_lshl_add_u32 v51, v51, 7, s90\n"
      "v_min_u32 v51, (.Lgm_last - .Lgm_first - 1024) & 0xffffff80, v51\n"
      "global_load_dword v0, v51, s[44:45] offset:0\n"
      "s_add_u32 s3, s97, 0\n"
      "s_and_b32 s3, s3, 15\n"
      "s_lshl_b32 s1, s3, 7\n"
      "s_add_u32 m0, s14, 0\n"
      "s_waitcnt lgkmcnt(0)\n"
      "s_add_u32 s34, s16, s12\n"
      "s_addc_u32 s35, s17, 0\n"
      "s_add_u32 s38, s34, s1\n"
      "s_addc_u32 s39, s35, 0\n"
      "global_load_lds_dwordx4 v1, s[38:39] offset:0\n"
      "s_cmp_lt_u32 s8, 16\n"
      "s_cselect_b64 s[48:49], s[18:19], s[22:23]\n"
      "s_add_u32 s36, s48, s15\n"
      "s_addc_u32 s37, s49, 0\n"
      "global_load_lds_dwordx4 v2, s[38:39] offset:1024\n"
      "s_mul_i32 s3, s3, 0x60000\n"
      "s_add_u32 s40, s36, s3\n"
      "s_addc_u32 s41, s37, 0\n"
      "global_load_lds_dwordx4 v3, s[38:39] offset:2048\n"
      "global_load_lds_dwordx4 v4, s[38:39] offset:3072\n"
      "s_add_u32 m0, s98, 0\n"
      "s_nop 0\n"
      "global_load_lds_dwordx4 v5, s[40:41] offset:0\n"
      "global_load_lds_dwordx4 v6, s[40:41] offset:1024\n"
      "global_load_lds_dwordx4 v7, s[40:41] offset:2048\n"
      "s_lshl2_add_u32 s90, s33, s4\n"
      "s_lshr_b32 s90, s90, 1\n"
      "s_cmp_eq_u32 s5, 0\n"
      "s_cselect_b64 s[48:49], s[18:19], s[22:23]\n"
      "s_lshr_b32 s91, s48, 7\n"
      "s_sub_u32 s91, 3, s91\n"
      "s_add_u32 s38, s38, 0x20000\n"
      "s_addc_u32 s39, s39, 0\n"
      "s_add_u32 m0, s14, 0x7000\n"
      "s_nop 0\n"
      "global_load_lds_dwordx4 v1, s[38:39] offset:0\n"
      "s_and_b32 s91, s91, 7\n"
      "s_lshl_b32 s91, s91, 7\n"
      "s_mul_i32 s15, s90, 0x3000\n"
      "s_add_u32 s91, s91, s15\n"
      "s_add_u32 s48, s48, s91\n"
      "s_addc_u32 s49, s49, 0\n"
      "global_load_lds_dwordx4 v2, s[38:39] offset:1024\n"
      "v_min_u32 v62, 11, v31\n"
      "v_lshlrev_b32 v62, 10, v62\n"
      "global_load_dword v0, v62, s[48:49] offset:0\n"
      "v_bfe_u32 v52, v31, 2, 3\n"
      "v_lshlrev_b32 v53, 6, v52\n"
      "v_lshl_or_b32 v53, v45, 5, v53\n"
      "s_add_u32 s40, s40, 0x30000\n"
      "s_addc_u32 s41, s41, 0\n"
      "global_load_lds_dwordx4 v3, s[38:39] offset:2048\n"
      "v_and_b32 v54, 3, v31\n"
      "v_lshl_or_b32 v53, v54, 3, v53\n"
      "v_mul_u32_u24 v54, 0x1800, v49\n"
      "s_lshl_b32 s90, s5, 9\n"
      "s_add_u32 s90, s90, 0x4000\n"
      "v_add3_u32 v8, v53, v54, s90\n"
      "global_load_lds_dwordx4 v4, s[38:39] offset:3072\n"
      "v_xor_b32 v9, 32, v8\n"
      "v_and_b32 v44, 15, v31\n"
      "v_lshrrev_b32 v46, 4, v31\n"
      "v_lshrrev_b32 v47, 1, v44\n"
      "v_lshlrev_b32 v28, 4, v46\n"
      "v_xor_b32 v46, v46, v47\n"
      "s_add_u32 m0, s98, 0x7000\n"
      "s_nop 0\n"
      "global_load_lds_dwordx4 v5, s[40:41] offset:0\n"
      "v_lshlrev_b32 v46, 4, v46\n"
      "v_lshl_or_b32 v46, v44, 7, v46\n"
      "s_lshl_b32 s90, s6, 13\n"
      "v_add_u32 v14, s90, v46\n"
      "v_xor_b32 v15, 64, v14\n"
      "v_add_u32 v10, 0xe000, v8\n"
      "global_load_lds_dwordx4 v6, s[40:41] offset:1024\n"
      "v_add_u32 v16, 0xe000, v14\n"
      "v_add_u32 v11, 0xe000, v9\n"
      "v_add_u32 v17, 0xe000, v15\n"
      "v_add_u32 v12, 0x1c000, v8\n"
      "v_add_u32 v18, 0x1c000, v14\n"
      "v_add_u32 v13, 0x1c000, v9\n"
      "global_load_lds_dwordx4 v7, s[40:41] offset:2048\n"
      "s_mov_b64 exec, 0\n"
      "s_cmp_eq_u32 s4, 0\n"
      "s_cbranch_scc1 .Lgm_w1\n"
      "s_cmp_eq_u32 s4, 1\n"
      "s_cbranch_scc1 .Lgm_w2\n"
      "s_cmp_eq_u32 s4, 2\n"
      "s_cbranch_scc1 .Lgm_w3\n"
      "s_cmp_eq_u32 s4, 3\n"
      "s_cbranch_scc1 .Lgm_w4\n"
      ".Lgm_r0:\n"
      "s_mov_b64 exec, -1\n"
      "v_add_u32 v19, 0x1c000, v15\n"
      "v_mov_b32 v20, 0x358637bd\n"
      "v_mov_b32 v21, 0x358637bd\n"
      "v_mov_b32 v22, 0x358637bd\n"
      "v_mov_b32 v23, 0x358637bd\n"
      "v_lshlrev_b32 v24, 4, v31\n"
      "s_add_u32 s3, s97, 1\n"
      "s_and_b32 s3, s3, 15\n"
      "s_lshl_b32 s1, s3, 7\n"
      "s_add_u32 s38, s34, s1\n"
      "s_addc_u32 s39, s35, 0\n"
      "s_add_u32 m0, s14, 0xe000\n"
      "s_nop 0\n"
      "global_load_lds_dwordx4 v1, s[38:39] offset:0\n"
      "s_lshr_b32 s2, s33, 4\n"
      "s_and_b32 s90, s33, 15\n"
      "s_lshl2_add_u32 s90, s90, s4\n"
      "s_lshl_b32 s76, s90, 13\n"
      "s_lshr_b32 s90, s90, 1\n"
      "s_lshl_b32 s77, s90, 13\n"
      "global_load_lds_dwordx4 v2, s[38:39] offset:1024\n"
      "v_lshlrev_b32 v43, 7, v31\n"
      "s_lshl2_add_u32 s90, s33, s4\n"
      "s_lshr_b32 s90, s90, 1\n"
      "s_cmp_eq_u32 s5, 0\n"
      "s_cselect_b64 s[46:47], s[20:21], s[24:25]\n"
      "s_lshl_b32 s91, s90, 12\n"
      "s_mul_i32 s3, s3, 0x60000\n"
      "s_add_u32 s40, s36, s3\n"
      "s_addc_u32 s41, s37, 0\n"
      "global_load_lds_dwordx4 v3, s[38:39] offset:2048\n"
      "s_add_u32 s46, s46, s91\n"
      "s_addc_u32 s47, s47, 0\n"
      "v_and_b32 v63, 15, v31\n"
      "v_lshlrev_b32 v63, 7, v63\n"
      "global_load_lds_dwordx4 v4, s[38:39] offset:3072\n"
      "s_add_u32 m0, s98, 0xe000\n"
      "s_nop 0\n"
      "global_load_lds_dwordx4 v5, s[40:41] offset:0\n"
      "global_load_lds_dwordx4 v6, s[40:41] offset:1024\n"
      "global_load_lds_dwordx4 v7, s[40:41] offset:2048\n"
      "s_mov_b64 exec, 0\n"
      "s_cmp_eq_u32 s4, 0\n"
      "s_cbranch_scc1 .Lgm_w5\n"
      "s_cmp_eq_u32 s4, 1\n"
      "s_cbranch_scc1 .Lgm_w6\n"
      "s_cmp_eq_u32 s4, 2\n"
      "s_cbranch_scc1 .Lgm_w7\n"
      ".Lgm_r1:\n"
      "s_mov_b64 exec, -1\n"
      "s_add_u32 s38, s38, 0x20000\n"
      "s_addc_u32 s39, s39, 0\n"
      "s_add_u32 m0, s14, 0x15000\n"
      "s_nop 0\n"
      "global_load_lds_dwordx4 v1, s[38:39] offset:0\n"
      "global_load_lds_dwordx4 v2, s[38:39] offset:1024\n"
      "s_add_u32 s40, s40, 0x30000\n"
      "s_addc_u32 s41, s41, 0\n"
      "global_load_lds_dwordx4 v3, s[38:39] offset:2048\n"
      "global_load_lds_dwordx4 v4, s[38:39] offset:3072\n"
      "s_add_u32 m0, s98, 0x15000\n"
      "s_nop 0\n"
      "global_load_lds_dwordx4 v5, s[40:41] offset:0\n"
      "global_load_lds_dwordx4 v6, s[40:41] offset:1024\n"
      "global_load_lds_dwordx4 v7, s[40:41] offset:2048\n"
      "s_waitcnt vmcnt(22)\n"
      "s_barrier\n"
      "ds_read_b64_tr_b16 a[80:81], v8 offset:1024\n"
      "ds_read_b64_tr_b16 a[82:83], v8 offset:4096\n"
      "ds_read_b64_tr_b16 a[88:89], v9 offset:1024\n"
      "ds_read_b64_tr_b16 a[90:91], v9 offset:4096\n"
      "ds_read_b128 a[0:3], v14 offset:0\n"
      "ds_read_b128 a[4:7], v15 offset:0\n"
      "ds_read_b128 a[8:11], v14 offset:2048\n"
      "ds_read_b128 a[12:15], v15 offset:2048\n"
      "ds_read_b128 a[16:19], v14 offset:4096\n"
      "ds_read_b128 a[20:23], v15 offset:4096\n"
      "ds_read_b128 a[24:27], v14 offset:6144\n"
      "ds_read_b128 a[28:31], v15 offset:6144\n"
      "ds_read_b64_tr_b16 a[96:97], v8 offset:2048\n"
      "ds_read_b64_tr_b16 a[98:99], v8 offset:5120\n"
      "ds_read_b64_tr_b16 a[104:105], v9 offset:2048\n"
      "ds_read_b64_tr_b16 a[106:107], v9 offset:5120\n"
      "ds_read_b64_tr_b16 a[64:65], v8 offset:0\n"
      "ds_read_b64_tr_b16 a[66:67], v8 offset:3072\n"
      "ds_read_b64_tr_b16 a[72:73], v9 offset:0\n"
      "ds_read_b64_tr_b16 a[74:75], v9 offset:3072\n"
      "s_waitcnt vmcnt(14) lgkmcnt(0)\n"
      "s_barrier\n"
      "ds_read_b64_tr_b16 a[84:85], v8 offset:29696\n"
      "ds_read_b64_tr_b16 a[86:87], v8 offset:32768\n"
      "ds_read_b64_tr_b16 a[92:93], v9 offset:29696\n"
      "ds_read_b64_tr_b16 a[94:95], v9 offset:32768\n"
      "ds_read_b128 a[32:35], v14 offset:28672\n"
      "ds_read_b128 a[36:39], v15 offset:28672\n"
      "ds_read_b128 a[40:43], v14 offset:30720\n"
      "ds_read_b128 a[44:47], v15 offset:30720\n"
      "ds_read_b128 a[48:51], v14 offset:32768\n"
      "ds_read_b128 a[52:55], v15 offset:32768\n"
      "ds_read_b128 a[56:59], v14 offset:34816\n"
      "ds_read_b128 a[60:63], v15 offset:34816\n"
      "ds_read_b64_tr_b16 a[100:101], v8 offset:30720\n"
      "ds_read_b64_tr_b16 a[102:103], v8 offset:33792\n"
      "ds_read_b64_tr_b16 a[108:109], v9 offset:30720\n"
      "ds_read_b64_tr_b16 a[110:111], v9 offset:33792\n"
      "ds_read_b64_tr_b16 a[68:69], v8 offset:28672\n"
      "ds_read_b64_tr_b16 a[70:71], v8 offset:31744\n"
      "ds_read_b64_tr_b16 a[76:77], v9 offset:28672\n"
      "ds_read_b64_tr_b16 a[78:79], v9 offset:31744\n"
      "s_waitcnt vmcnt(7) lgkmcnt(0)\n"
      "v_mfma_f32_16x16x32_bf16 v[128:131], a[0:3], a[80:83], v[20:23]\n"
      "s_barrier\n"
      "v_mov_b32 v27, 0\n"
      "s_add_u32 s3, s97, 2\n"
      "v_mfma_f32_16x16x32_bf16 v[128:131], a[4:7], a[84:87], v[128:131]\n"
      "ds_read_b64_tr_b16 a[192:193], v10 offset:1024\n"
      "s_and_b32 s3, s3, 15\n"
      "v_mfma_f32_16x16x32_bf16 v[160:163], a[0:3], a[88:91], v[20:23]\n"
      "ds_read_b64_tr_b16 a[194:195], v10 offset:4096\n"
      "s_lshl_b32 s1, s3, 7\n"
      "v_mfma_f32_16x16x32_bf16 v[160:163], a[4:7], a[92:95], v[160:163]\n"
      "s_add_u32 s38, s34, s1\n"
      "s_addc_u32 s39, s35, 0\n"
      "v_mfma_f32_16x16x32_bf16 v[164:167], a[8:11], a[88:91], v[20:23]\n"
      "ds_read_b64_tr_b16 a[200:201], v11 offset:1024\n"
      "s_add_u32 m0, s14, 0x1c000\n"
      "v_mfma_f32_16x16x32_bf16 v[164:167], a[12:15], a[92:95], v[164:167]\n"
      "global_load_dword v26, v27, s[30:31] offset:0 sc1\n"
      "v_mfma_f32_16x16x32_bf16 v[132:135], a[8:11], a[80:83], v[20:23]\n"
      "s_bfe_u32 s90, s33, 0x20001\n"
      "s_lshr_b32 s91, s33, 6\n"
      "v_mfma_f32_16x16x32_bf16 v[132:135], a[12:15], a[84:87], v[132:135]\n"
      "ds_read_b64_tr_b16 a[202:203], v11 offset:4096\n"
      "s_lshl2_add_u32 s9, s90, s91\n"
      "v_mfma_f32_16x16x32_bf16 v[136:139], a[16:19], a[80:83], v[20:23]\n"
      "ds_read_b128 a[112:115], v16 offset:0\n"
      "s_and_b32 s90, s33, 1\n"
      "v_mfma_f32_16x16x32_bf16 v[136:139], a[20:23], a[84:87], v[136:139]\n"
      "global_load_lds_dwordx4 v1, s[38:39] offset:0\n"
      "v_mfma_f32_16x16x32_bf16 v[168:171], a[16:19], a[88:91], v[20:23]\n"
      "ds_read_b128 a[116:119], v17 offset:0\n"
      "s_bfe_u32 s91, s33, 0x30003\n"
      "v_mfma_f32_16x16x32_bf16 v[168:171], a[20:23], a[92:95], v[168:171]\n"
      "s_xor_b32 s91, s91, 1\n"
      "s_lshl3_add_u32 s10, s90, s91\n"
      "v_mfma_f32_16x16x32_bf16 v[172:175], a[24:27], a[88:91], v[20:23]\n"
      "s_lshl_b32 s99, s4, 13\n"
      "v_and_b32 v40, 31, v31\n"
      "v_lshlrev_b32 v40, 7, v40\n"
      "v_mfma_f32_16x16x32_bf16 v[172:175], a[28:31], a[92:95], v[172:175]\n"
      "ds_read_b128 a[120:123], v16 offset:2048\n"
      "v_xor_b32 v50, s6, v49\n"
      "v_mfma_f32_16x16x32_bf16 v[140:143], a[24:27], a[80:83], v[20:23]\n"
      "ds_read_b128 a[124:127], v17 offset:2048\n"
      "v_lshl_or_b32 v50, v50, 6, v48\n"
      "v_mfma_f32_16x16x32_bf16 v[140:143], a[28:31], a[84:87], v[140:143]\n"
      "global_load_lds_dwordx4 v2, s[38:39] offset:1024\n"
      "v_mfma_f32_16x16x32_bf16 v[144:147], a[32:35], a[80:83], v[20:23]\n"
      "ds_read_b128 a[128:131], v16 offset:4096\n"
      "s_mul_i32 s3, s3, 0x60000\n"
      "v_mfma_f32_16x16x32_bf16 v[144:147], a[36:39], a[84:87], v[144:147]\n"
      "s_add_u32 s40, s36, s3\n"
      "v_lshl_or_b32 v32, v52, 11, v50\n"
      "v_add_u32 v33, 0x7c00, v32\n"
      "v_mfma_f32_16x16x32_bf16 v[176:179], a[32:35], a[88:91], v[20:23]\n"
      "s_addc_u32 s41, s37, 0\n"
      "v_add_u32 v34, 0x3800, v32\n"
      "v_add_u32 v35, 0xb400, v32\n"
      "v_mfma_f32_16x16x32_bf16 v[176:179], a[36:39], a[92:95], v[176:179]\n"
      "ds_read_b128 a[132:135], v17 offset:4096\n"
      "v_lshl_or_b32 v53, v49, 11, v53\n"
      "v_mfma_f32_16x16x32_bf16 v[180:183], a[40:43], a[88:91], v[20:23]\n"
      "ds_read_b128 a[136:139], v16 offset:6144\n"
      "s_lshl_b32 s90, s6, 14\n"
      "v_mfma_f32_16x16x32_bf16 v[180:183], a[44:47], a[92:95], v[180:183]\n"
      "global_load_lds_dwordx4 v3, s[38:39] offset:2048\n"
      "v_mfma_f32_16x16x32_bf16 v[148:151], a[40:43], a[80:83], v[20:23]\n"
      "ds_read_b128 a[140:143], v17 offset:6144\n"
      "v_add_u32 v56, s90, v53\n"
      "v_mfma_f32_16x16x32_bf16 v[148:151], a[44:47], a[84:87], v[148:151]\n"
      "v_xor_b32 v57, 32, v56\n"
      "v_add_u32 v58, 0x10000, v56\n"
      "v_mfma_f32_16x16x32_bf16 v[152:155], a[48:51], a[80:83], v[20:23]\n"
      "v_add_u32 v59, 0x10000, v57\n"
      "v_add_u32 v60, 0x20000, v56\n"
      "v_mfma_f32_16x16x32_bf16 v[152:155], a[52:55], a[84:87], v[152:155]\n"
      "ds_read_b64_tr_b16 a[208:209], v10 offset:2048\n"
      "v_add_u32 v61, 0x20000, v57\n"
      "v_mfma_f32_16x16x32_bf16 v[184:187], a[48:51], a[88:91], v[20:23]\n"
      "ds_read_b64_tr_b16 a[210:211], v10 offset:5120\n"
      "v_mfma_f32_16x16x32_bf16 v[184:187], a[52:55], a[92:95], v[184:187]\n"
      "global_load_lds_dwordx4 v4, s[38:39] offset:3072\n"
      "v_mfma_f32_16x16x32_bf16 v[188:191], a[56:59], a[88:91], v[20:23]\n"
      "ds_read_b64_tr_b16 a[216:217], v11 offset:2048\n"
      "s_add_u32 m0, s98, 0x1c000\n"
      "v_mfma_f32_16x16x32_bf16 v[188:191], a[60:63], a[92:95], v[188:191]\n"
      "s_cmp_eq_u32 s6, 0\n"
      "s_cselect_b64 s[48:49], s[20:21], s[24:25]\n"
      "v_mfma_f32_16x16x32_bf16 v[156:159], a[56:59], a[80:83], v[20:23]\n"
      "s_lshl_b32 s90, s5, 17\n"
      "s_lshl_b32 s91, s10, 7\n"
      "v_mfma_f32_16x16x32_bf16 v[156:159], a[60:63], a[84:87], v[156:159]\n"
      "ds_read_b64_tr_b16 a[218:219], v11 offset:5120\n"
      "s_add_u32 s90, s90, s91\n"
      "v_mfma_f32_16x16x32_bf16 v[192:195], a[0:3], a[96:99], 0\n"
      "ds_read_b64_tr_b16 a[176:177], v10 offset:0\n"
      "v_mfma_f32_16x16x32_bf16 v[192:195], a[4:7], a[100:103], v[192:195]\n"
      "global_load_lds_dwordx4 v5, s[40:41] offset:0\n"
      "v_mfma_f32_16x16x32_bf16 v[224:227], a[0:3], a[104:107], 0\n"
      "ds_read_b64_tr_b16 a[178:179], v10 offset:3072\n"
      "v_mfma_f32_16x16x32_bf16 v[224:227], a[4:7], a[108:111], v[224:227]\n"
      "s_add_u32 s42, s48, s90\n"
      "s_addc_u32 s43, s49, 0\n"
      "v_mfma_f32_16x16x32_bf16 v[228:231], a[8:11], a[104:107], 0\n"
      "s_mov_b32 s0, 0\n"
      "s_bfe_u32 s90, s33, 0x20006\n"
      "v_mfma_f32_16x16x32_bf16 v[228:231], a[12:15], a[108:111], v[228:231]\n"
      "ds_read_b64_tr_b16 a[184:185], v11 offset:0\n"
      "s_mul_i32 s90, s90, 4\n"
      "v_mfma_f32_16x16x32_bf16 v[196:199], a[8:11], a[96:99], 0\n"
      "ds_read_b64_tr_b16 a[186:187], v11 offset:3072\n"
      "s_add_u32 s0, s0, s90\n"
      "v_mfma_f32_16x16x32_bf16 v[196:199], a[12:15], a[100:103], v[196:199]\n"
      "global_load_lds_dwordx4 v6, s[40:41] offset:1024\n"
      "v_mfma_f32_16x16x32_bf16 v[200:203], a[16:19], a[96:99], 0\n"
      "s_lshl1_add_u32 s12, s8, s5\n"
      "s_and_b32 s15, s12, 31\n"
      "v_mfma_f32_16x16x32_bf16 v[200:203], a[20:23], a[100:103], v[200:203]\n"
      "s_lshl_b32 s90, s15, 13\n"
      "s_add_u32 s90, s90, s11\n"
      "v_mfma_f32_16x16x32_bf16 v[232:235], a[16:19], a[104:107], 0\n"
      "s_add_u32 s91, s90, 0x1000\n"
      "v_mfma_f32_16x16x32_bf16 v[232:235], a[20:23], a[108:111], v[232:235]\n"
      "s_add_u32 s70, s16, s91\n"
      "s_addc_u32 s71, s17, 0\n"
      "v_mfma_f32_16x16x32_bf16 v[236:239], a[24:27], a[104:107], 0\n"
      "s_lshr_b32 s90, s90, 9\n"
      "s_lshr_b32 s91, s8, 4\n"
      "v_mfma_f32_16x16x32_bf16 v[236:239], a[28:31], a[108:111], v[236:239]\n"
      "global_load_lds_dwordx4 v7, s[40:41] offset:2048\n"
      "v_mfma_f32_16x16x32_bf16 v[204:207], a[24:27], a[96:99], 0\n"
      "s_add_u32 s38, s38, 0x20000\n"
      "s_addc_u32 s39, s39, 0\n"
      "v_mfma_f32_16x16x32_bf16 v[204:207], a[28:31], a[100:103], v[204:207]\n"
      "s_add_u32 m0, s14, 0\n"
      "s_xor_b32 s96, s91, 1\n"
      "s_waitcnt vmcnt(8) lgkmcnt(0)\n"
      "v_mfma_f32_16x16x32_bf16 v[208:211], a[32:35], a[96:99], 0\n"
      "s_barrier\n"
      "s_lshl_b32 s96, s96, 31\n"
      "s_or_b32 s96, s96, 0x3fb8aa3b\n"
      "v_mfma_f32_16x16x32_bf16 v[208:211], a[36:39], a[100:103], v[208:211]\n"
      "ds_read_b64_tr_b16 a[196:197], v10 offset:29696\n"
      "s_lshl_b32 s91, s91, 13\n"
      "v_mfma_f32_16x16x32_bf16 v[240:243], a[32:35], a[104:107], 0\n"
      "ds_read_b64_tr_b16 a[198:199], v10 offset:32768\n"
      "s_add_u32 s90, s90, s91\n"
      "v_mfma_f32_16x16x32_bf16 v[240:243], a[36:39], a[108:111], v[240:243]\n"
      "global_load_lds_dwordx4 v1, s[38:39] offset:0\n"
      "v_mfma_f32_16x16x32_bf16 v[244:247], a[40:43], a[104:107], 0\n"
      "ds_read_b64_tr_b16 a[204:205], v11 offset:29696\n"
      "s_add_u32 s90, s90, 0x70000\n"
      "v_mfma_f32_16x16x32_bf16 v[244:247], a[44:47], a[108:111], v[244:247]\n"
      "s_add_u32 s68, s30, s90\n"
      "s_addc_u32 s69, s31, 0\n"
      "v_mfma_f32_16x16x32_bf16 v[212:215], a[40:43], a[96:99], 0\n"
      "s_lshl1_add_u32 s11, s7, s6\n"
      "s_lshl_b32 s90, s11, 6\n"
      "v_mfma_f32_16x16x32_bf16 v[212:215], a[44:47], a[100:103], v[212:215]\n"
      "ds_read_b64_tr_b16 a[206:207], v11 offset:32768\n"
      "s_add_u32 s90, s90, s12\n"
      "v_mfma_f32_16x16x32_bf16 v[216:219], a[48:51], a[96:99], 0\n"
      "ds_read_b128 a[144:147], v16 offset:28672\n"
      "s_lshl_b32 s90, s90, 7\n"
      "v_mfma_f32_16x16x32_bf16 v[216:219], a[52:55], a[100:103], v[216:219]\n"
      "global_load_lds_dwordx4 v2, s[38:39] offset:1024\n"
      "v_mfma_f32_16x16x32_bf16 v[248:251], a[48:51], a[104:107], 0\n"
      "ds_read_b128 a[148:151], v17 offset:28672\n"
      "s_add_u32 s40, s40, 0x30000\n"
      "v_mfma_f32_16x16x32_bf16 v[248:251], a[52:55], a[108:111], v[248:251]\n"
      "s_addc_u32 s41, s41, 0\n"
      "s_add_u32 s90, s90, 0x50000\n"
      "v_mfma_f32_16x16x32_bf16 v[252:255], a[56:59], a[104:107], 0\n"
      "s_add_u32 s60, s30, s90\n"
      "s_addc_u32 s61, s31, 0\n"
      "v_mfma_f32_16x16x32_bf16 v[252:255], a[60:63], a[108:111], v[252:255]\n"
      "ds_read_b128 a[152:155], v16 offset:30720\n"
      "s_lshl4_add_u32 s90, s12, s11\n"
      "v_mfma_f32_16x16x32_bf16 v[220:223], a[56:59], a[96:99], 0\n"
      "ds_read_b128 a[156:159], v17 offset:30720\n"
      "s_lshl_b32 s91, s90, 12\n"
      "v_mfma_f32_16x16x32_bf16 v[220:223], a[60:63], a[100:103], v[220:223]\n"
      "global_load_lds_dwordx4 v3, s[38:39] offset:2048\n"
      "v_mfma_f32_16x16x32_bf16 v[64:67], a[64:67], a[0:3], v[20:23]\n"
      "ds_read_b128 a[160:163], v16 offset:32768\n"
      "s_add_u32 s91, s91, 0x100000\n"
      "v_mfma_f32_16x16x32_bf16 v[64:67], a[68:71], a[4:7], v[64:67]\n"
      "s_add_u32 s50, s30, s91\n"
      "s_addc_u32 s51, s31, 0\n"
      "v_mfma_f32_16x16x32_bf16 v[96:99], a[72:75], a[0:3], v[20:23]\n"
      "s_lshl_b32 s91, s90, 7\n"
      "s_add_u32 s91, s91, 0x80000\n"
      "v_mfma_f32_16x16x32_bf16 v[96:99], a[76:79], a[4:7], v[96:99]\n"
      "ds_read_b128 a[164:167], v17 offset:32768\n"
      "v_mfma_f32_16x16x32_bf16 v[100:103], a[72:75], a[8:11], v[20:23]\n"
      "ds_read_b128 a[168:171], v16 offset:34816\n"
      "v_mfma_f32_16x16x32_bf16 v[100:103], a[76:79], a[12:15], v[100:103]\n"
      "global_load_lds_dwordx4 v4, s[38:39] offset:3072\n"
      "v_mfma_f32_16x16x32_bf16 v[68:71], a[64:67], a[8:11], v[20:23]\n"
      "ds_read_b128 a[172:175], v17 offset:34816\n"
      "s_add_u32 m0, s98, 0\n"
      "v_mfma_f32_16x16x32_bf16 v[68:71], a[68:71], a[12:15], v[68:71]\n"
      "s_add_u32 s52, s30, s91\n"
      "s_addc_u32 s53, s31, 0\n"
      "v_mfma_f32_16x16x32_bf16 v[72:75], a[64:67], a[16:19], v[20:23]\n"
      "s_lshl_b32 s90, s11, 8\n"
      "s_lshl_b32 s91, s12, 16\n"
      "v_mfma_f32_16x16x32_bf16 v[72:75], a[68:71], a[20:23], v[72:75]\n"
      "ds_read_b64_tr_b16 a[212:213], v10 offset:30720\n"
      "s_add_u32 s91, s91, s90\n"
      "v_mfma_f32_16x16x32_bf16 v[104:107], a[72:75], a[16:19], v[20:23]\n"
      "ds_read_b64_tr_b16 a[214:215], v10 offset:33792\n"
      "s_add_u32 s91, s91, 0x100000\n"
      "v_mfma_f32_16x16x32_bf16 v[104:107], a[76:79], a[20:23], v[104:107]\n"
      "global_load_lds_dwordx4 v5, s[40:41] offset:0\n"
      "v_mfma_f32_16x16x32_bf16 v[108:111], a[72:75], a[24:27], v[20:23]\n"
      "ds_read_b64_tr_b16 a[220:221], v11 offset:30720\n"
      "v_mfma_f32_16x16x32_bf16 v[108:111], a[76:79], a[28:31], v[108:111]\n"
      "s_add_u32 s78, s30, s91\n"
      "s_addc_u32 s79, s31, 0\n"
      "v_mfma_f32_16x16x32_bf16 v[76:79], a[64:67], a[24:27], v[20:23]\n"
      "s_add_u32 s80, s78, 0x4000\n"
      "s_addc_u32 s81, s79, 0\n"
      "v_mfma_f32_16x16x32_bf16 v[76:79], a[68:71], a[28:31], v[76:79]\n"
      "ds_read_b64_tr_b16 a[222:223], v11 offset:33792\n"
      "v_mfma_f32_16x16x32_bf16 v[80:83], a[64:67], a[32:35], v[20:23]\n"
      "ds_read_b64_tr_b16 a[180:181], v10 offset:28672\n"
      "v_mfma_f32_16x16x32_bf16 v[80:83], a[68:71], a[36:39], v[80:83]\n"
      "global_load_lds_dwordx4 v6, s[40:41] offset:1024\n"
      "v_mfma_f32_16x16x32_bf16 v[112:115], a[72:75], a[32:35], v[20:23]\n"
      "ds_read_b64_tr_b16 a[182:183], v10 offset:31744\n"
      "v_mfma_f32_16x16x32_bf16 v[112:115], a[76:79], a[36:39], v[112:115]\n"
      "s_add_u32 s82, s78, 0x8000\n"
      "s_addc_u32 s83, s79, 0\n"
      "v_mfma_f32_16x16x32_bf16 v[116:119], a[72:75], a[40:43], v[20:23]\n"
      "s_add_u32 s84, s78, 0xc000\n"
      "s_addc_u32 s85, s79, 0\n"
      "v_mfma_f32_16x16x32_bf16 v[116:119], a[76:79], a[44:47], v[116:119]\n"
      "ds_read_b64_tr_b16 a[188:189], v11 offset:28672\n"
      "s_lshl_b32 s91, s12, 12\n"
      "v_mfma_f32_16x16x32_bf16 v[84:87], a[64:67], a[40:43], v[20:23]\n"
      "ds_read_b64_tr_b16 a[190:191], v11 offset:31744\n"
      "s_add_u32 s91, s91, 0x10000\n"
      "v_mfma_f32_16x16x32_bf16 v[84:87], a[68:71], a[44:47], v[84:87]\n"
      "global_load_lds_dwordx4 v7, s[40:41] offset:2048\n"
      "v_mfma_f32_16x16x32_bf16 v[88:91], a[64:67], a[48:51], v[20:23]\n"
      "s_add_u32 s3, s97, 3\n"
      "s_and_b32 s3, s3, 15\n"
      "v_mfma_f32_16x16x32_bf16 v[88:91], a[68:71], a[52:55], v[88:91]\n"
      "s_lshl_b32 s1, s3, 7\n"
      "s_add_u32 s38, s34, s1\n"
      "v_mfma_f32_16x16x32_bf16 v[120:123], a[72:75], a[48:51], v[20:23]\n"
      "s_addc_u32 s39, s35, 0\n"
      "s_add_u32 m0, s14, 0x7000\n"
      "v_mfma_f32_16x16x32_bf16 v[120:123], a[76:79], a[52:55], v[120:123]\n"
      "s_add_u32 s56, s30, s91\n"
      "s_addc_u32 s57, s31, 0\n"
      "v_lshl_or_b32 v51, v45, 1, v49\n"
      "v_mfma_f32_16x16x32_bf16 v[124:127], a[72:75], a[56:59], v[20:23]\n"
      "v_lshlrev_b32 v54, 4, v44\n"
      "v_lshl_or_b32 v36, v28, 8, v54\n"
      "v_mfma_f32_16x16x32_bf16 v[124:127], a[76:79], a[60:63], v[124:127]\n"
      "global_load_lds_dwordx4 v1, s[38:39] offset:0\n"
      "v_mfma_f32_16x16x32_bf16 v[92:95], a[64:67], a[56:59], v[20:23]\n"
      "v_lshl_or_b32 v54, v51, 2, v54\n"
      "v_add_u32 v38, s90, v54\n"
      "v_mfma_f32_16x16x32_bf16 v[92:95], a[68:71], a[60:63], v[92:95]\n"
      "v_lshlrev_b32 v42, 2, v51\n"
      "s_waitcnt vmcnt(8) lgkmcnt(0)\n"
      "v_mfma_f32_16x16x32_bf16 v[128:131], a[112:115], a[192:195], v[128:131]\n"
      "s_barrier\n"
      "v_and_b32 v54, 3, v31\n"
      "v_lshlrev_b32 v54, 9, v54\n"
      "v_mfma_f32_16x16x32_bf16 v[128:131], a[116:119], a[196:199], v[128:131]\n"
      "ds_read_b64_tr_b16 a[80:81], v12 offset:1024\n"
      "v_lshl_add_u32 v37, v28, 3, v54\n"
      "v_mfma_f32_16x16x32_bf16 v[160:163], a[116:119], a[204:207], v[160:163]\n"
      "ds_read_b64_tr_b16 a[82:83], v12 offset:4096\n"
      "s_lshr_b32 s90, s11, 1\n"
      "v_mfma_f32_16x16x32_bf16 v[160:163], a[112:115], a[200:203], v[160:163]\n"
      "global_load_lds_dwordx4 v2, s[38:39] offset:1024\n"
      "v_mfma_f32_16x16x32_bf16 v[164:167], a[120:123], a[200:203], v[164:167]\n"
      "ds_read_b64_tr_b16 a[88:89], v13 offset:1024\n"
      "s_mul_i32 s3, s3, 0x60000\n"
      "v_mfma_f32_16x16x32_bf16 v[164:167], a[124:127], a[204:207], v[164:167]\n"
      "s_add_u32 s40, s36, s3\n"
      "s_addc_u32 s41, s37, 0\n"
      "v_mfma_f32_16x16x32_bf16 v[132:135], a[124:127], a[196:199], v[132:135]\n"
      "s_lshl_b32 s90, s90, 4\n"
      "s_lshl_b32 s91, s12, 11\n"
      "v_mfma_f32_16x16x32_bf16 v[132:135], a[120:123], a[192:195], v[132:135]\n"
      "ds_read_b64_tr_b16 a[90:91], v13 offset:4096\n"
      "s_add_u32 s91, s91, s90\n"
      "v_mfma_f32_16x16x32_bf16 v[136:139], a[128:131], a[192:195], v[136:139]\n"
      "ds_read_b128 a[0:3], v18 offset:0\n"
      "s_add_u32 s91, s91, 0x80000\n"
      "v_mfma_f32_16x16x32_bf16 v[136:139], a[132:135], a[196:199], v[136:139]\n"
      "global_load_lds_dwordx4 v3, s[38:39] offset:2048\n"
      "v_mfma_f32_16x16x32_bf16 v[168:171], a[132:135], a[204:207], v[168:171]\n"
      "ds_read_b128 a[4:7], v19 offset:0\n"
      "v_mfma_f32_16x16x32_bf16 v[168:171], a[128:131], a[200:203], v[168:171]\n"
      "s_add_u32 s54, s30, s91\n"
      "s_addc_u32 s55, s31, 0\n"
      "v_mfma_f32_16x16x32_bf16 v[172:175], a[136:139], a[200:203], v[172:175]\n"
      "s_lshl_b32 s91, s12, 7\n"
      "s_add_u32 s91, s91, 0xc0000\n"
      "v_mfma_f32_16x16x32_bf16 v[172:175], a[140:143], a[204:207], v[172:175]\n"
      "ds_read_b128 a[8:11], v18 offset:2048\n"
      "v_mfma_f32_16x16x32_bf16 v[140:143], a[140:143], a[196:199], v[140:143]\n"
      "ds_read_b128 a[12:15], v19 offset:2048\n"
      "v_mfma_f32_16x16x32_bf16 v[140:143], a[136:139], a[192:195], v[140:143]\n"
      "global_load_lds_dwordx4 v4, s[38:39] offset:3072\n"
      "v_mfma_f32_16x16x32_bf16 v[144:147], a[144:147], a[192:195], v[144:147]\n"
      "ds_read_b128 a[16:19], v18 offset:4096\n"
      "s_add_u32 m0, s98, 0x7000\n"
      "v_mfma_f32_16x16x32_bf16 v[144:147], a[148:151], a[196:199], v[144:147]\n"
      "s_add_u32 s58, s30, s91\n"
      "s_addc_u32 s59, s31, 0\n"
      "v_add_u32 v39, s90, v42\n"
      "v_mfma_f32_16x16x32_bf16 v[176:179], a[148:151], a[204:207], v[176:179]\n"
      "v_bfe_u32 v54, v31, 3, 1\n"
      "v_lshl_or_b32 v41, v54, 6, v28\n"
      "v_mfma_f32_16x16x32_bf16 v[176:179], a[144:147], a[200:203], v[176:179]\n"
      "ds_read_b128 a[20:23], v19 offset:4096\n"
      "v_mul_u32_u24 v54, 0x3f80, v54\n"
      "v_mfma_f32_16x16x32_bf16 v[180:183], a[152:155], a[200:203], v[180:183]\n"
      "ds_read_b128 a[24:27], v18 offset:6144\n"
      "v_lshl_or_b32 v29, v54, 16, v54\n"
      "v_mfma_f32_16x16x32_bf16 v[180:183], a[156:159], a[204:207], v[180:183]\n"
      "global_load_lds_dwordx4 v5, s[40:41] offset:0\n"
      "v_mfma_f32_16x16x32_bf16 v[148:151], a[156:159], a[196:199], v[148:151]\n"
      "ds_read_b128 a[28:31], v19 offset:6144\n"
      "s_mov_b32 s86, 0xfff\n"
      "v_mfma_f32_16x16x32_bf16 v[148:151], a[152:155], a[192:195], v[148:151]\n"
      "s_mov_b32 s87, 0xffff\n"
      "s_mov_b32 s89, 0x7060302\n"
      "v_mfma_f32_16x16x32_bf16 v[152:155], a[160:163], a[192:195], v[152:155]\n"
      "s_lshl_b32 s90, s11, 19\n"
      "s_lshl_b32 s91, s12, 10\n"
      "v_mfma_f32_16x16x32_bf16 v[152:155], a[164:167], a[196:199], v[152:155]\n"
      "ds_read_b64_tr_b16 a[96:97], v12 offset:2048\n"
      "s_add_u32 s90, s90, s91\n"
      "v_mfma_f32_16x16x32_bf16 v[184:187], a[164:167], a[204:207], v[184:187]\n"
      "ds_read_b64_tr_b16 a[98:99], v12 offset:5120\n"
      "s_add_u32 s90, s90, 0x900000\n"
      "v_mfma_f32_16x16x32_bf16 v[184:187], a[160:163], a[200:203], v[184:187]\n"
      "global_load_lds_dwordx4 v6, s[40:41] offset:1024\n"
      "v_mfma_f32_16x16x32_bf16 v[188:191], a[168:171], a[200:203], v[188:191]\n"
      "ds_read_b64_tr_b16 a[104:105], v13 offset:2048\n"
      "v_mfma_f32_16x16x32_bf16 v[188:191], a[172:175], a[204:207], v[188:191]\n"
      "s_add_u32 s72, s30, s90\n"
      "s_addc_u32 s73, s31, 0\n"
      "v_mfma_f32_16x16x32_bf16 v[156:159], a[172:175], a[196:199], v[156:159]\n"
      "s_lshl1_add_u32 s11, s9, s5\n"
      "s_lshl_b32 s90, s11, 18\n"
      "v_mfma_f32_16x16x32_bf16 v[156:159], a[168:171], a[192:195], v[156:159]\n"
      "ds_read_b64_tr_b16 a[106:107], v13 offset:5120\n"
      "s_lshl_b32 s91, s6, 15\n"
      "v_mfma_f32_16x16x32_bf16 v[192:195], a[112:115], a[208:211], v[192:195]\n"
      "ds_read_b64_tr_b16 a[64:65], v12 offset:0\n"
      "s_add_u32 s90, s90, s91\n"
      "v_mfma_f32_16x16x32_bf16 v[192:195], a[116:119], a[212:215], v[192:195]\n"
      "global_load_lds_dwordx4 v7, s[40:41] offset:2048\n"
      "v_mfma_f32_16x16x32_bf16 v[224:227], a[116:119], a[220:223], v[224:227]\n"
      "ds_read_b64_tr_b16 a[66:67], v12 offset:3072\n"
      "s_add_u32 s38, s38, 0x20000\n"
      "v_mfma_f32_16x16x32_bf16 v[224:227], a[112:115], a[216:219], v[224:227]\n"
      "s_addc_u32 s39, s39, 0\n"
      "s_add_u32 m0, s14, 0xe000\n"
      "v_mfma_f32_16x16x32_bf16 v[228:231], a[120:123], a[216:219], v[228:231]\n"
      "s_add_u32 s90, s90, 0x900000\n"
      ".Lgm_w1:\n"
      "s_cbranch_execz .Lgm_r0\n"
      "v_mfma_f32_16x16x32_bf16 v[228:231], a[124:127], a[220:223], v[228:231]\n"
      "ds_read_b64_tr_b16 a[72:73], v13 offset:0\n"
      "v_mfma_f32_16x16x32_bf16 v[196:199], a[124:127], a[212:215], v[196:199]\n"
      "ds_read_b64_tr_b16 a[74:75], v13 offset:3072\n"
      "v_mfma_f32_16x16x32_bf16 v[196:199], a[120:123], a[208:211], v[196:199]\n"
      "global_load_lds_dwordx4 v1, s[38:39] offset:0\n"
      "v_mfma_f32_16x16x32_bf16 v[200:203], a[128:131], a[208:211], v[200:203]\n"
      "s_add_u32 s44, s30, s90\n"
      "s_addc_u32 s45, s31, 0\n"
      "v_mfma_f32_16x16x32_bf16 v[200:203], a[132:135], a[212:215], v[200:203]\n"
      "s_lshl1_add_u32 s90, s9, s6\n"
      "s_lshl_b32 s90, s90, 12\n"
      "v_mfma_f32_16x16x32_bf16 v[232:235], a[132:135], a[220:223], v[232:235]\n"
      "s_add_u32 s90, s90, 0x50000\n"
      "v_mfma_f32_16x16x32_bf16 v[232:235], a[128:131], a[216:219], v[232:235]\n"
      "s_add_u32 s62, s30, s90\n"
      "s_addc_u32 s63, s31, 0\n"
      "v_mfma_f32_16x16x32_bf16 v[236:239], a[136:139], a[216:219], v[236:239]\n"
      "s_lshl_b32 s90, s11, 8\n"
      "s_lshl_b32 s91, s6, 13\n"
      "v_mfma_f32_16x16x32_bf16 v[236:239], a[140:143], a[220:223], v[236:239]\n"
      "global_load_lds_dwordx4 v2, s[38:39] offset:1024\n"
      "v_mfma_f32_16x16x32_bf16 v[204:207], a[140:143], a[212:215], v[204:207]\n"
      "s_add_u32 s40, s40, 0x30000\n"
      "s_addc_u32 s41, s41, 0\n"
      "v_mfma_f32_16x16x32_bf16 v[204:207], a[136:139], a[208:211], v[204:207]\n"
      "s_add_u32 s90, s90, s91\n"
      "s_add_u32 s90, s90, 0x70000\n"
      "s_waitcnt vmcnt(9) lgkmcnt(0)\n"
      "v_mfma_f32_16x16x32_bf16 v[208:211], a[144:147], a[208:211], v[208:211]\n"
      "s_barrier\n"
      "s_add_u32 s64, s30, s90\n"
      "s_addc_u32 s65, s31, 0\n"
      "v_mfma_f32_16x16x32_bf16 v[208:211], a[148:151], a[212:215], v[208:211]\n"
      "ds_read_b64_tr_b16 a[84:85], v8 offset:1024\n"
      "s_lshl_b32 s90, s11, 17\n"
      "v_mfma_f32_16x16x32_bf16 v[240:243], a[148:151], a[220:223], v[240:243]\n"
      "ds_read_b64_tr_b16 a[86:87], v8 offset:4096\n"
      "s_lshl_b32 s91, s6, 6\n"
      "v_mfma_f32_16x16x32_bf16 v[240:243], a[144:147], a[216:219], v[240:243]\n"
      "global_load_lds_dwordx4 v3, s[38:39] offset:2048\n"
      "v_mfma_f32_16x16x32_bf16 v[244:247], a[152:155], a[216:219], v[244:247]\n"
      "ds_read_b64_tr_b16 a[92:93], v9 offset:1024\n"
      "s_add_u32 s90, s90, s91\n"
      "v_mfma_f32_16x16x32_bf16 v[244:247], a[156:159], a[220:223], v[244:247]\n"
      "s_lshl_b32 s91, s10, 7\n"
      "s_add_u32 s90, s90, s91\n"
      "v_mfma_f32_16x16x32_bf16 v[212:215], a[156:159], a[212:215], v[212:215]\n"
      "s_add_u32 s66, s28, s90\n"
      "s_addc_u32 s67, s29, 0\n"
      "v_mov_b32 v25, 0x3fb8aa3b\n"
      "v_mfma_f32_16x16x32_bf16 v[212:215], a[152:155], a[208:211], v[212:215]\n"
      "ds_read_b64_tr_b16 a[94:95], v9 offset:4096\n"
      "s_mov_b32 s94, 0x358637bd\n"
      "v_mfma_f32_16x16x32_bf16 v[216:219], a[160:163], a[208:211], v[216:219]\n"
      "ds_read_b128 a[32:35], v14 offset:0\n"
      "v_mfma_f32_16x16x32_bf16 v[216:219], a[164:167], a[212:215], v[216:219]\n"
      "global_load_lds_dwordx4 v4, s[38:39] offset:3072\n"
      "v_mfma_f32_16x16x32_bf16 v[248:251], a[164:167], a[220:223], v[248:251]\n"
      "ds_read_b128 a[36:39], v15 offset:0\n"
      "s_add_u32 m0, s98, 0xe000\n"
      "v_mfma_f32_16x16x32_bf16 v[248:251], a[160:163], a[216:219], v[248:251]\n"
      "v_mfma_f32_16x16x32_bf16 v[252:255], a[168:171], a[216:219], v[252:255]\n"
      "v_mfma_f32_16x16x32_bf16 v[252:255], a[172:175], a[220:223], v[252:255]\n"
      "ds_read_b128 a[40:43], v14 offset:2048\n"
      "v_mfma_f32_16x16x32_bf16 v[220:223], a[172:175], a[212:215], v[220:223]\n"
      "ds_read_b128 a[44:47], v15 offset:2048\n"
      "v_mfma_f32_16x16x32_bf16 v[220:223], a[168:171], a[208:211], v[220:223]\n"
      "global_load_lds_dwordx4 v5, s[40:41] offset:0\n"
      "v_mfma_f32_16x16x32_bf16 v[64:67], a[176:179], a[112:115], v[64:67]\n"
      "ds_read_b128 a[48:51], v14 offset:4096\n"
      "v_mfma_f32_16x16x32_bf16 v[64:67], a[180:183], a[116:119], v[64:67]\n"
      "v_mfma_f32_16x16x32_bf16 v[96:99], a[188:191], a[116:119], v[96:99]\n"
      "v_mfma_f32_16x16x32_bf16 v[96:99], a[184:187], a[112:115], v[96:99]\n"
      "ds_read_b128 a[52:55], v15 offset:4096\n"
      "v_mfma_f32_16x16x32_bf16 v[100:103], a[184:187], a[120:123], v[100:103]\n"
      "ds_read_b128 a[56:59], v14 offset:6144\n"
      "v_mfma_f32_16x16x32_bf16 v[100:103], a[188:191], a[124:127], v[100:103]\n"
      "global_load_lds_dwordx4 v6, s[40:41] offset:1024\n"
      "v_mfma_f32_16x16x32_bf16 v[68:71], a[180:183], a[124:127], v[68:71]\n"
      "ds_read_b128 a[60:63], v15 offset:6144\n"
      "v_mfma_f32_16x16x32_bf16 v[68:71], a[176:179], a[120:123], v[68:71]\n"
      "v_mfma_f32_16x16x32_bf16 v[72:75], a[176:179], a[128:131], v[72:75]\n"
      "v_mfma_f32_16x16x32_bf16 v[72:75], a[180:183], a[132:135], v[72:75]\n"
      "ds_read_b64_tr_b16 a[100:101], v8 offset:2048\n"
      "v_mfma_f32_16x16x32_bf16 v[104:107], a[188:191], a[132:135], v[104:107]\n"
      "ds_read_b64_tr_b16 a[102:103], v8 offset:5120\n"
      "v_mfma_f32_16x16x32_bf16 v[104:107], a[184:187], a[128:131], v[104:107]\n"
      "global_load_lds_dwordx4 v7, s[40:41] offset:2048\n"
      "v_mfma_f32_16x16x32_bf16 v[108:111], a[184:187], a[136:139], v[108:111]\n"
      "ds_read_b64_tr_b16 a[108:109], v9 offset:2048\n"
      "s_add_u32 s3, s97, 4\n"
      "v_mfma_f32_16x16x32_bf16 v[108:111], a[188:191], a[140:143], v[108:111]\n"
      "s_and_b32 s3, s3, 15\n"
      "s_lshl_b32 s1, s3, 7\n"
      "v_mfma_f32_16x16x32_bf16 v[76:79], a[180:183], a[140:143], v[76:79]\n"
      "s_add_u32 s38, s34, s1\n"
      "s_addc_u32 s39, s35, 0\n"
      "v_mfma_f32_16x16x32_bf16 v[76:79], a[176:179], a[136:139], v[76:79]\n"
      "ds_read_b64_tr_b16 a[110:111], v9 offset:5120\n"
      "s_add_u32 m0, s14, 0x15000\n"
      "v_mfma_f32_16x16x32_bf16 v[80:83], a[176:179], a[144:147], v[80:83]\n"
      "ds_read_b64_tr_b16 a[68:69], v8 offset:0\n"
      "v_mfma_f32_16x16x32_bf16 v[80:83], a[180:183], a[148:151], v[80:83]\n"
      "global_load_lds_dwordx4 v1, s[38:39] offset:0\n"
      "v_mfma_f32_16x16x32_bf16 v[112:115], a[188:191], a[148:151], v[112:115]\n"
      "ds_read_b64_tr_b16 a[70:71], v8 offset:3072\n"
      "v_mfma_f32_16x16x32_bf16 v[112:115], a[184:187], a[144:147], v[112:115]\n"
      "v_mfma_f32_16x16x32_bf16 v[116:119], a[184:187], a[152:155], v[116:119]\n"
      "v_mfma_f32_16x16x32_bf16 v[116:119], a[188:191], a[156:159], v[116:119]\n"
      "ds_read_b64_tr_b16 a[76:77], v9 offset:0\n"
      "v_mfma_f32_16x16x32_bf16 v[84:87], a[180:183], a[156:159], v[84:87]\n"
      "ds_read_b64_tr_b16 a[78:79], v9 offset:3072\n"
      "v_mfma_f32_16x16x32_bf16 v[84:87], a[176:179], a[152:155], v[84:87]\n"
      "global_load_lds_dwordx4 v2, s[38:39] offset:1024\n"
      "v_mfma_f32_16x16x32_bf16 v[88:91], a[176:179], a[160:163], v[88:91]\n"
      "s_mul_i32 s3, s3, 0x60000\n"
      "s_add_u32 s40, s36, s3\n"
      "v_mfma_f32_16x16x32_bf16 v[88:91], a[180:183], a[164:167], v[88:91]\n"
      "s_addc_u32 s41, s37, 0\n"
      "v_mfma_f32_16x16x32_bf16 v[120:123], a[188:191], a[164:167], v[120:123]\n"
      "v_mfma_f32_16x16x32_bf16 v[120:123], a[184:187], a[160:163], v[120:123]\n"
      "v_mfma_f32_16x16x32_bf16 v[124:127], a[184:187], a[168:171], v[124:127]\n"
      "v_mfma_f32_16x16x32_bf16 v[124:127], a[188:191], a[172:175], v[124:127]\n"
      "global_load_lds_dwordx4 v3, s[38:39] offset:2048\n"
      "v_mfma_f32_16x16x32_bf16 v[92:95], a[180:183], a[172:175], v[92:95]\n"
      "v_mfma_f32_16x16x32_bf16 v[92:95], a[176:179], a[168:171], v[92:95]\n"
      "s_waitcnt vmcnt(10) lgkmcnt(0)\n"
      "v_mfma_f32_16x16x32_bf16 v[128:131], a[0:3], a[80:83], v[128:131]\n"
      "s_barrier\n"
      "v_mfma_f32_16x16x32_bf16 v[128:131], a[4:7], a[84:87], v[128:131]\n"
      "ds_read_b64_tr_b16 a[192:193], v8 offset:29696\n"
      "v_mfma_f32_16x16x32_bf16 v[160:163], a[4:7], a[92:95], v[160:163]\n"
      "ds_read_b64_tr_b16 a[194:195], v8 offset:32768\n"
      "v_mfma_f32_16x16x32_bf16 v[160:163], a[0:3], a[88:91], v[160:163]\n"
      "global_load_lds_dwordx4 v4, s[38:39] offset:3072\n"
      "v_mfma_f32_16x16x32_bf16 v[164:167], a[8:11], a[88:91], v[164:167]\n"
      "ds_read_b64_tr_b16 a[200:201], v9 offset:29696\n"
      "s_add_u32 m0, s98, 0x15000\n"
      "v_mfma_f32_16x16x32_bf16 v[164:167], a[12:15], a[92:95], v[164:167]\n"
      "v_mfma_f32_16x16x32_bf16 v[132:135], a[12:15], a[84:87], v[132:135]\n"
      "v_mfma_f32_16x16x32_bf16 v[132:135], a[8:11], a[80:83], v[132:135]\n"
      "ds_read_b64_tr_b16 a[202:203], v9 offset:32768\n"
      "v_mfma_f32_16x16x32_bf16 v[136:139], a[16:19], a[80:83], v[136:139]\n"
      "ds_read_b128 a[112:115], v14 offset:28672\n"
      "v_mfma_f32_16x16x32_bf16 v[136:139], a[20:23], a[84:87], v[136:139]\n"
      "global_load_lds_dwordx4 v5, s[40:41] offset:0\n"
      "v_mfma_f32_16x16x32_bf16 v[168:171], a[20:23], a[92:95], v[168:171]\n"
      "ds_read_b128 a[116:119], v15 offset:28672\n"
      "v_mfma_f32_16x16x32_bf16 v[168:171], a[16:19], a[88:91], v[168:171]\n"
      "v_mfma_f32_16x16x32_bf16 v[172:175], a[24:27], a[88:91], v[172:175]\n"
      "v_mfma_f32_16x16x32_bf16 v[172:175], a[28:31], a[92:95], v[172:175]\n"
      "ds_read_b128 a[120:123], v14 offset:30720\n"
      "v_mfma_f32_16x16x32_bf16 v[140:143], a[28:31], a[84:87], v[140:143]\n"
      "ds_read_b128 a[124:127], v15 offset:30720\n"
      "v_mfma_f32_16x16x32_bf16 v[140:143], a[24:27], a[80:83], v[140:143]\n"
      "global_load_lds_dwordx4 v6, s[40:41] offset:1024\n"
      "v_mfma_f32_16x16x32_bf16 v[144:147], a[32:35], a[80:83], v[144:147]\n"
      "ds_read_b128 a[128:131], v14 offset:32768\n"
      "v_mfma_f32_16x16x32_bf16 v[144:147], a[36:39], a[84:87], v[144:147]\n"
      "v_mfma_f32_16x16x32_bf16 v[176:179], a[36:39], a[92:95], v[176:179]\n"
      "v_mfma_f32_16x16x32_bf16 v[176:179], a[32:35], a[88:91], v[176:179]\n"
      "ds_read_b128 a[132:135], v15 offset:32768\n"
      "v_mfma_f32_16x16x32_bf16 v[180:183], a[40:43], a[88:91], v[180:183]\n"
      "ds_read_b128 a[136:139], v14 offset:34816\n"
      "v_mfma_f32_16x16x32_bf16 v[180:183], a[44:47], a[92:95], v[180:183]\n"
      "global_load_lds_dwordx4 v7, s[40:41] offset:2048\n"
      "v_mfma_f32_16x16x32_bf16 v[148:151], a[44:47], a[84:87], v[148:151]\n"
      "ds_read_b128 a[140:143], v15 offset:34816\n"
      "s_add_u32 s38, s38, 0x20000\n"
      "v_mfma_f32_16x16x32_bf16 v[148:151], a[40:43], a[80:83], v[148:151]\n"
      "s_addc_u32 s39, s39, 0\n"
      "s_add_u32 m0, s14, 0x1c000\n"
      "v_mfma_f32_16x16x32_bf16 v[152:155], a[48:51], a[80:83], v[152:155]\n"
      "v_mfma_f32_16x16x32_bf16 v[152:155], a[52:55], a[84:87], v[152:155]\n"
      "ds_read_b64_tr_b16 a[208:209], v8 offset:30720\n"
      "v_mfma_f32_16x16x32_bf16 v[184:187], a[52:55], a[92:95], v[184:187]\n"
      "ds_read_b64_tr_b16 a[210:211], v8 offset:33792\n"
      "v_mfma_f32_16x16x32_bf16 v[184:187], a[48:51], a[88:91], v[184:187]\n"
      "global_load_lds_dwordx4 v1, s[38:39] offset:0\n"
      "v_mfma_f32_16x16x32_bf16 v[188:191], a[56:59], a[88:91], v[188:191]\n"
      "ds_read_b64_tr_b16 a[216:217], v9 offset:30720\n"
      "v_mfma_f32_16x16x32_bf16 v[188:191], a[60:63], a[92:95], v[188:191]\n"
      "v_mfma_f32_16x16x32_bf16 v[156:159], a[60:63], a[84:87], v[156:159]\n"
      "v_mfma_f32_16x16x32_bf16 v[156:159], a[56:59], a[80:83], v[156:159]\n"
      "ds_read_b64_tr_b16 a[218:219], v9 offset:33792\n"
      "v_mfma_f32_16x16x32_bf16 v[192:195], a[0:3], a[96:99], v[192:195]\n"
      "ds_read_b64_tr_b16 a[176:177], v8 offset:28672\n"
      "v_mfma_f32_16x16x32_bf16 v[192:195], a[4:7], a[100:103], v[192:195]\n"
      "global_load_lds_dwordx4 v2, s[38:39] offset:1024\n"
      "v_mfma_f32_16x16x32_bf16 v[224:227], a[4:7], a[108:111], v[224:227]\n"
      "ds_read_b64_tr_b16 a[178:179], v8 offset:31744\n"
      "s_add_u32 s40, s40, 0x30000\n"
      "v_mfma_f32_16x16x32_bf16 v[224:227], a[0:3], a[104:107], v[224:227]\n"
      "s_addc_u32 s41, s41, 0\n"
      "v_mfma_f32_16x16x32_bf16 v[228:231], a[8:11], a[104:107], v[228:231]\n"
      "v_mfma_f32_16x16x32_bf16 v[228:231], a[12:15], a[108:111], v[228:231]\n"
      "ds_read_b64_tr_b16 a[184:185], v9 offset:28672\n"
      "v_mfma_f32_16x16x32_bf16 v[196:199], a[12:15], a[100:103], v[196:199]\n"
      "ds_read_b64_tr_b16 a[186:187], v9 offset:31744\n"
      "v_mfma_f32_16x16x32_bf16 v[196:199], a[8:11], a[96:99], v[196:199]\n"
      "global_load_lds_dwordx4 v3, s[38:39] offset:2048\n"
      "v_mfma_f32_16x16x32_bf16 v[200:203], a[16:19], a[96:99], v[200:203]\n"
      "v_mfma_f32_16x16x32_bf16 v[200:203], a[20:23], a[100:103], v[200:203]\n"
      "v_mfma_f32_16x16x32_bf16 v[232:235], a[20:23], a[108:111], v[232:235]\n"
      "v_mfma_f32_16x16x32_bf16 v[232:235], a[16:19], a[104:107], v[232:235]\n"
      "v_mfma_f32_16x16x32_bf16 v[236:239], a[24:27], a[104:107], v[236:239]\n"
      "v_mfma_f32_16x16x32_bf16 v[236:239], a[28:31], a[108:111], v[236:239]\n"
      "global_load_lds_dwordx4 v4, s[38:39] offset:3072\n"
      "v_mfma_f32_16x16x32_bf16 v[204:207], a[28:31], a[100:103], v[204:207]\n"
      "s_add_u32 m0, s98, 0x1c000\n"
      "v_mfma_f32_16x16x32_bf16 v[204:207], a[24:27], a[96:99], v[204:207]\n"
      "s_waitcnt vmcnt(11) lgkmcnt(0)\n"
      "v_mfma_f32_16x16x32_bf16 v[208:211], a[32:35], a[96:99], v[208:211]\n"
      "s_barrier\n"
      "v_mfma_f32_16x16x32_bf16 v[208:211], a[36:39], a[100:103], v[208:211]\n"
      "ds_read_b64_tr_b16 a[196:197], v10 offset:1024\n"
      "v_mfma_f32_16x16x32_bf16 v[240:243], a[36:39], a[108:111], v[240:243]\n"
      "ds_read_b64_tr_b16 a[198:199], v10 offset:4096\n"
      "v_mfma_f32_16x16x32_bf16 v[240:243], a[32:35], a[104:107], v[240:243]\n"
      "global_load_lds_dwordx4 v5, s[40:41] offset:0\n"
      "v_mfma_f32_16x16x32_bf16 v[244:247], a[40:43], a[104:107], v[244:247]\n"
      "ds_read_b64_tr_b16 a[204:205], v11 offset:1024\n"
      "v_mfma_f32_16x16x32_bf16 v[244:247], a[44:47], a[108:111], v[244:247]\n"
      "v_mfma_f32_16x16x32_bf16 v[212:215], a[44:47], a[100:103], v[212:215]\n"
      "v_mfma_f32_16x16x32_bf16 v[212:215], a[40:43], a[96:99], v[212:215]\n"
      "ds_read_b64_tr_b16 a[206:207], v11 offset:4096\n"
      "v_mfma_f32_16x16x32_bf16 v[216:219], a[48:51], a[96:99], v[216:219]\n"
      "ds_read_b128 a[144:147], v16 offset:0\n"
      "v_mfma_f32_16x16x32_bf16 v[216:219], a[52:55], a[100:103], v[216:219]\n"
      "global_load_lds_dwordx4 v6, s[40:41] offset:1024\n"
      "v_mfma_f32_16x16x32_bf16 v[248:251], a[52:55], a[108:111], v[248:251]\n"
      "ds_read_b128 a[148:151], v17 offset:0\n"
      "v_mfma_f32_16x16x32_bf16 v[248:251], a[48:51], a[104:107], v[248:251]\n"
      "v_mfma_f32_16x16x32_bf16 v[252:255], a[56:59], a[104:107], v[252:255]\n"
      "v_mfma_f32_16x16x32_bf16 v[252:255], a[60:63], a[108:111], v[252:255]\n"
      "ds_read_b128 a[152:155], v16 offset:2048\n"
      "v_mfma_f32_16x16x32_bf16 v[220:223], a[60:63], a[100:103], v[220:223]\n"
      "ds_read_b128 a[156:159], v17 offset:2048\n"
      "v_mfma_f32_16x16x32_bf16 v[220:223], a[56:59], a[96:99], v[220:223]\n"
      "global_load_lds_dwordx4 v7, s[40:41] offset:2048\n"
      "v_mfma_f32_16x16x32_bf16 v[64:67], a[64:67], a[0:3], v[64:67]\n"
      "ds_read_b128 a[160:163], v16 offset:4096\n"
      "s_add_u32 s3, s97, 5\n"
      "v_mfma_f32_16x16x32_bf16 v[64:67], a[68:71], a[4:7], v[64:67]\n"
      "s_and_b32 s3, s3, 15\n"
      "s_lshl_b32 s1, s3, 7\n"
      "v_mfma_f32_16x16x32_bf16 v[96:99], a[76:79], a[4:7], v[96:99]\n"
      "s_add_u32 s38, s34, s1\n"
      "s_addc_u32 s39, s35, 0\n"
      "v_mfma_f32_16x16x32_bf16 v[96:99], a[72:75], a[0:3], v[96:99]\n"
      "ds_read_b128 a[164:167], v17 offset:4096\n"
      "s_add_u32 m0, s14, 0\n"
      "v_mfma_f32_16x16x32_bf16 v[100:103], a[72:75], a[8:11], v[100:103]\n"
      "ds_read_b128 a[168:171], v16 offset:6144\n"
      "v_mfma_f32_16x16x32_bf16 v[100:103], a[76:79], a[12:15], v[100:103]\n"
      "global_load_lds_dwordx4 v1, s[38:39] offset:0\n"
      "v_mfma_f32_16x16x32_bf16 v[68:71], a[68:71], a[12:15], v[68:71]\n"
      "ds_read_b128 a[172:175], v17 offset:6144\n"
      "v_add_u32 v26, 0x9e3779b9, v26\n"
      "v_mfma_f32_16x16x32_bf16 v[68:71], a[64:67], a[8:11], v[68:71]\n"
      "v_readfirstlane_b32 s13, v26\n"
      "v_mfma_f32_16x16x32_bf16 v[72:75], a[64:67], a[16:19], v[72:75]\n"
      "s_and_b32 s88, s13, 0xfff\n"
      "v_mfma_f32_16x16x32_bf16 v[72:75], a[68:71], a[20:23], v[72:75]\n"
      "ds_read_b64_tr_b16 a[212:213], v10 offset:2048\n"
      "v_mfma_f32_16x16x32_bf16 v[104:107], a[76:79], a[20:23], v[104:107]\n"
      "ds_read_b64_tr_b16 a[214:215], v10 offset:5120\n"
      "v_mfma_f32_16x16x32_bf16 v[104:107], a[72:75], a[16:19], v[104:107]\n"
      "global_load_lds_dwordx4 v2, s[38:39] offset:1024\n"
      "v_mfma_f32_16x16x32_bf16 v[108:111], a[72:75], a[24:27], v[108:111]\n"
      "ds_read_b64_tr_b16 a[220:221], v11 offset:2048\n"
      "s_mul_i32 s3, s3, 0x60000\n"
      "v_mfma_f32_16x16x32_bf16 v[108:111], a[76:79], a[28:31], v[108:111]\n"
      "s_add_u32 s40, s36, s3\n"
      "s_addc_u32 s41, s37, 0\n"
      "v_mfma_f32_16x16x32_bf16 v[76:79], a[68:71], a[28:31], v[76:79]\n"
      "v_mfma_f32_16x16x32_bf16 v[76:79], a[64:67], a[24:27], v[76:79]\n"
      "ds_read_b64_tr_b16 a[222:223], v11 offset:5120\n"
      "v_mfma_f32_16x16x32_bf16 v[80:83], a[64:67], a[32:35], v[80:83]\n"
      "ds_read_b64_tr_b16 a[180:181], v10 offset:0\n"
      "v_mfma_f32_16x16x32_bf16 v[80:83], a[68:71], a[36:39], v[80:83]\n"
      "global_load_lds_dwordx4 v3, s[38:39] offset:2048\n"
      "v_mfma_f32_16x16x32_bf16 v[112:115], a[76:79], a[36:39], v[112:115]\n"
      "ds_read_b64_tr_b16 a[182:183], v10 offset:3072\n"
      "v_mfma_f32_16x16x32_bf16 v[112:115], a[72:75], a[32:35], v[112:115]\n"
      "v_mfma_f32_16x16x32_bf16 v[116:119], a[72:75], a[40:43], v[116:119]\n"
      "v_mfma_f32_16x16x32_bf16 v[116:119], a[76:79], a[44:47], v[116:119]\n"
      "ds_read_b64_tr_b16 a[188:189], v11 offset:0\n"
      "v_mfma_f32_16x16x32_bf16 v[84:87], a[68:71], a[44:47], v[84:87]\n"
      "ds_read_b64_tr_b16 a[190:191], v11 offset:3072\n"
      "v_mfma_f32_16x16x32_bf16 v[84:87], a[64:67], a[40:43], v[84:87]\n"
      "global_load_lds_dwordx4 v4, s[38:39] offset:3072\n"
      "v_mfma_f32_16x16x32_bf16 v[88:91], a[64:67], a[48:51], v[88:91]\n"
      "s_add_u32 m0, s98, 0\n"
      "v_mfma_f32_16x16x32_bf16 v[88:91], a[68:71], a[52:55], v[88:91]\n"
      "v_mfma_f32_16x16x32_bf16 v[120:123], a[76:79], a[52:55], v[120:123]\n"
      "v_mfma_f32_16x16x32_bf16 v[120:123], a[72:75], a[48:51], v[120:123]\n"
      "v_mfma_f32_16x16x32_bf16 v[124:127], a[72:75], a[56:59], v[124:127]\n"
      "v_mfma_f32_16x16x32_bf16 v[124:127], a[76:79], a[60:63], v[124:127]\n"
      "global_load_lds_dwordx4 v5, s[40:41] offset:0\n"
      "v_mfma_f32_16x16x32_bf16 v[92:95], a[68:71], a[60:63], v[92:95]\n"
      "v_mfma_f32_16x16x32_bf16 v[92:95], a[64:67], a[56:59], v[92:95]\n"
      "s_waitcnt vmcnt(12) lgkmcnt(0)\n"
      "v_mfma_f32_16x16x32_bf16 v[128:131], a[112:115], a[192:195], v[128:131]\n"
      "s_barrier\n"
      "v_mfma_f32_16x16x32_bf16 v[128:131], a[116:119], a[196:199], v[128:131]\n"
      "ds_read_b64_tr_b16 a[80:81], v10 offset:29696\n"
      "v_mfma_f32_16x16x32_bf16 v[160:163], a[116:119], a[204:207], v[160:163]\n"
      "ds_read_b64_tr_b16 a[82:83], v10 offset:32768\n"
      "v_mfma_f32_16x16x32_bf16 v[160:163], a[112:115], a[200:203], v[160:163]\n"
      "global_load_lds_dwordx4 v6, s[40:41] offset:1024\n"
      "v_mfma_f32_16x16x32_bf16 v[164:167], a[120:123], a[200:203], v[164:167]\n"
      "ds_read_b64_tr_b16 a[88:89], v11 offset:29696\n"
      "v_mfma_f32_16x16x32_bf16 v[164:167], a[124:127], a[204:207], v[164:167]\n"
      "v_mfma_f32_16x16x32_bf16 v[132:135], a[124:127], a[196:199], v[132:135]\n"
      "v_mfma_f32_16x16x32_bf16 v[132:135], a[120:123], a[192:195], v[132:135]\n"
      "ds_read_b64_tr_b16 a[90:91], v11 offset:32768\n"
      "v_mfma_f32_16x16x32_bf16 v[136:139], a[128:131], a[192:195], v[136:139]\n"
      "ds_read_b128 a[0:3], v16 offset:28672\n"
      "v_mfma_f32_16x16x32_bf16 v[136:139], a[132:135], a[196:199], v[136:139]\n"
      "global_load_lds_dwordx4 v7, s[40:41] offset:2048\n"
      "v_mfma_f32_16x16x32_bf16 v[168:171], a[132:135], a[204:207], v[168:171]\n"
      "ds_read_b128 a[4:7], v17 offset:28672\n"
      "s_add_u32 s38, s38, 0x20000\n"
      "v_mfma_f32_16x16x32_bf16 v[168:171], a[128:131], a[200:203], v[168:171]\n"
      "s_addc_u32 s39, s39, 0\n"
      "s_add_u32 m0, s14, 0x7000\n"
      "v_mfma_f32_16x16x32_bf16 v[172:175], a[136:139], a[200:203], v[172:175]\n"
      "v_mfma_f32_16x16x32_bf16 v[172:175], a[140:143], a[204:207], v[172:175]\n"
      "ds_read_b128 a[8:11], v16 offset:30720\n"
      "v_mfma_f32_16x16x32_bf16 v[140:143], a[140:143], a[196:199], v[140:143]\n"
      "ds_read_b128 a[12:15], v17 offset:30720\n"
      "v_mfma_f32_16x16x32_bf16 v[140:143], a[136:139], a[192:195], v[140:143]\n"
      "global_load_lds_dwordx4 v1, s[38:39] offset:0\n"
      "v_mfma_f32_16x16x32_bf16 v[144:147], a[144:147], a[192:195], v[144:147]\n"
      "ds_read_b128 a[16:19], v16 offset:32768\n"
      "v_mfma_f32_16x16x32_bf16 v[144:147], a[148:151], a[196:199], v[144:147]\n"
      "v_mfma_f32_16x16x32_bf16 v[176:179], a[148:151], a[204:207], v[176:179]\n"
      "v_mfma_f32_16x16x32_bf16 v[176:179], a[144:147], a[200:203], v[176:179]\n"
      "ds_read_b128 a[20:23], v17 offset:32768\n"
      "v_mfma_f32_16x16x32_bf16 v[180:183], a[152:155], a[200:203], v[180:183]\n"
      "ds_read_b128 a[24:27], v16 offset:34816\n"
      "v_mfma_f32_16x16x32_bf16 v[180:183], a[156:159], a[204:207], v[180:183]\n"
      "global_load_lds_dwordx4 v2, s[38:39] offset:1024\n"
      "v_mfma_f32_16x16x32_bf16 v[148:151], a[156:159], a[196:199], v[148:151]\n"
      "ds_read_b128 a[28:31], v17 offset:34816\n"
      "s_add_u32 s40, s40, 0x30000\n"
      "v_mfma_f32_16x16x32_bf16 v[148:151], a[152:155], a[192:195], v[148:151]\n"
      "s_addc_u32 s41, s41, 0\n"
      "v_mfma_f32_16x16x32_bf16 v[152:155], a[160:163], a[192:195], v[152:155]\n"
      "v_mfma_f32_16x16x32_bf16 v[152:155], a[164:167], a[196:199], v[152:155]\n"
      "ds_read_b64_tr_b16 a[96:97], v10 offset:30720\n"
      "v_mfma_f32_16x16x32_bf16 v[184:187], a[164:167], a[204:207], v[184:187]\n"
      "ds_read_b64_tr_b16 a[98:99], v10 offset:33792\n"
      "v_mfma_f32_16x16x32_bf16 v[184:187], a[160:163], a[200:203], v[184:187]\n"
      "global_load_lds_dwordx4 v3, s[38:39] offset:2048\n"
      "v_mfma_f32_16x16x32_bf16 v[188:191], a[168:171], a[200:203], v[188:191]\n"
      "ds_read_b64_tr_b16 a[104:105], v11 offset:30720\n"
      "v_mfma_f32_16x16x32_bf16 v[188:191], a[172:175], a[204:207], v[188:191]\n"
      "v_mfma_f32_16x16x32_bf16 v[156:159], a[172:175], a[196:199], v[156:159]\n"
      "v_mfma_f32_16x16x32_bf16 v[156:159], a[168:171], a[192:195], v[156:159]\n"
      "ds_read_b64_tr_b16 a[106:107], v11 offset:33792\n"
      "v_mfma_f32_16x16x32_bf16 v[192:195], a[112:115], a[208:211], v[192:195]\n"
      "ds_read_b64_tr_b16 a[64:65], v10 offset:28672\n"
      "v_mfma_f32_16x16x32_bf16 v[192:195], a[116:119], a[212:215], v[192:195]\n"
      "global_load_lds_dwordx4 v4, s[38:39] offset:3072\n"
      "v_mfma_f32_16x16x32_bf16 v[224:227], a[116:119], a[220:223], v[224:227]\n"
      "ds_read_b64_tr_b16 a[66:67], v10 offset:31744\n"
      "s_add_u32 m0, s98, 0x7000\n"
      "v_mfma_f32_16x16x32_bf16 v[224:227], a[112:115], a[216:219], v[224:227]\n"
      "v_mfma_f32_16x16x32_bf16 v[228:231], a[120:123], a[216:219], v[228:231]\n"
      "v_mfma_f32_16x16x32_bf16 v[228:231], a[124:127], a[220:223], v[228:231]\n"
      "ds_read_b64_tr_b16 a[72:73], v11 offset:28672\n"
      "v_mfma_f32_16x16x32_bf16 v[196:199], a[124:127], a[212:215], v[196:199]\n"
      "ds_read_b64_tr_b16 a[74:75], v11 offset:31744\n"
      "v_mfma_f32_16x16x32_bf16 v[196:199], a[120:123], a[208:211], v[196:199]\n"
      "global_load_lds_dwordx4 v5, s[40:41] offset:0\n"
      "v_mfma_f32_16x16x32_bf16 v[200:203], a[128:131], a[208:211], v[200:203]\n"
      "v_mfma_f32_16x16x32_bf16 v[200:203], a[132:135], a[212:215], v[200:203]\n"
      "v_mfma_f32_16x16x32_bf16 v[232:235], a[132:135], a[220:223], v[232:235]\n"
      "v_mfma_f32_16x16x32_bf16 v[232:235], a[128:131], a[216:219], v[232:235]\n"
      "v_mfma_f32_16x16x32_bf16 v[236:239], a[136:139], a[216:219], v[236:239]\n"
      "v_mfma_f32_16x16x32_bf16 v[236:239], a[140:143], a[220:223], v[236:239]\n"
      "global_load_lds_dwordx4 v6, s[40:41] offset:1024\n"
      "v_mfma_f32_16x16x32_bf16 v[204:207], a[140:143], a[212:215], v[204:207]\n"
      "v_mfma_f32_16x16x32_bf16 v[204:207], a[136:139], a[208:211], v[204:207]\n"
      "s_waitcnt vmcnt(13) lgkmcnt(0)\n"
      "v_mfma_f32_16x16x32_bf16 v[208:211], a[144:147], a[208:211], v[208:211]\n"
      "s_barrier\n"
      "v_mfma_f32_16x16x32_bf16 v[208:211], a[148:151], a[212:215], v[208:211]\n"
      "ds_read_b64_tr_b16 a[84:85], v12 offset:1024\n"
      "v_mfma_f32_16x16x32_bf16 v[240:243], a[148:151], a[220:223], v[240:243]\n"
      "ds_read_b64_tr_b16 a[86:87], v12 offset:4096\n"
      "v_mfma_f32_16x16x32_bf16 v[240:243], a[144:147], a[216:219], v[240:243]\n"
      "global_load_lds_dwordx4 v7, s[40:41] offset:2048\n"
      "v_mfma_f32_16x16x32_bf16 v[244:247], a[152:155], a[216:219], v[244:247]\n"
      "ds_read_b64_tr_b16 a[92:93], v13 offset:1024\n"
      "s_add_u32 s3, s97, 6\n"
      "v_mfma_f32_16x16x32_bf16 v[244:247], a[156:159], a[220:223], v[244:247]\n"
      "s_and_b32 s3, s3, 15\n"
      "s_lshl_b32 s1, s3, 7\n"
      "v_mfma_f32_16x16x32_bf16 v[212:215], a[156:159], a[212:215], v[212:215]\n"
      "s_add_u32 s38, s34, s1\n"
      "s_addc_u32 s39, s35, 0\n"
      "v_mfma_f32_16x16x32_bf16 v[212:215], a[152:155], a[208:211], v[212:215]\n"
      "ds_read_b64_tr_b16 a[94:95], v13 offset:4096\n"
      "s_add_u32 m0, s14, 0xe000\n"
      "v_mfma_f32_16x16x32_bf16 v[216:219], a[160:163], a[208:211], v[216:219]\n"
      "ds_read_b128 a[32:35], v18 offset:0\n"
      "v_mfma_f32_16x16x32_bf16 v[216:219], a[164:167], a[212:215], v[216:219]\n"
      "global_load_lds_dwordx4 v1, s[38:39] offset:0\n"
      "v_mfma_f32_16x16x32_bf16 v[248:251], a[164:167], a[220:223], v[248:251]\n"
      "ds_read_b128 a[36:39], v19 offset:0\n"
      "v_mfma_f32_16x16x32_bf16 v[248:251], a[160:163], a[216:219], v[248:251]\n"
      "v_mfma_f32_16x16x32_bf16 v[252:255], a[168:171], a[216:219], v[252:255]\n"
      "v_mfma_f32_16x16x32_bf16 v[252:255], a[172:175], a[220:223], v[252:255]\n"
      "ds_read_b128 a[40:43], v18 offset:2048\n"
      "v_mfma_f32_16x16x32_bf16 v[220:223], a[172:175], a[212:215], v[220:223]\n"
      "ds_read_b128 a[44:47], v19 offset:2048\n"
      "v_mfma_f32_16x16x32_bf16 v[220:223], a[168:171], a[208:211], v[220:223]\n"
      "global_load_lds_dwordx4 v2, s[38:39] offset:1024\n"
      "v_mfma_f32_16x16x32_bf16 v[64:67], a[176:179], a[112:115], v[64:67]\n"
      "ds_read_b128 a[48:51], v18 offset:4096\n"
      "s_mul_i32 s3, s3, 0x60000\n"
      "v_mfma_f32_16x16x32_bf16 v[64:67], a[180:183], a[116:119], v[64:67]\n"
      "s_add_u32 s40, s36, s3\n"
      "s_addc_u32 s41, s37, 0\n"
      "v_mfma_f32_16x16x32_bf16 v[96:99], a[188:191], a[116:119], v[96:99]\n"
      "v_mfma_f32_16x16x32_bf16 v[96:99], a[184:187], a[112:115], v[96:99]\n"
      "ds_read_b128 a[52:55], v19 offset:4096\n"
      "v_mfma_f32_16x16x32_bf16 v[100:103], a[184:187], a[120:123], v[100:103]\n"
      "ds_read_b128 a[56:59], v18 offset:6144\n"
      "v_mfma_f32_16x16x32_bf16 v[100:103], a[188:191], a[124:127], v[100:103]\n"
      "global_load_lds_dwordx4 v3, s[38:39] offset:2048\n"
      "v_mfma_f32_16x16x32_bf16 v[68:71], a[180:183], a[124:127], v[68:71]\n"
      "ds_read_b128 a[60:63], v19 offset:6144\n"
      "v_mfma_f32_16x16x32_bf16 v[68:71], a[176:179], a[120:123], v[68:71]\n"
      "v_mfma_f32_16x16x32_bf16 v[72:75], a[176:179], a[128:131], v[72:75]\n"
      "v_mfma_f32_16x16x32_bf16 v[72:75], a[180:183], a[132:135], v[72:75]\n"
      "ds_read_b64_tr_b16 a[100:101], v12 offset:2048\n"
      "v_mfma_f32_16x16x32_bf16 v[104:107], a[188:191], a[132:135], v[104:107]\n"
      "ds_read_b64_tr_b16 a[102:103], v12 offset:5120\n"
      "v_mfma_f32_16x16x32_bf16 v[104:107], a[184:187], a[128:131], v[104:107]\n"
      "global_load_lds_dwordx4 v4, s[38:39] offset:3072\n"
      "v_mfma_f32_16x16x32_bf16 v[108:111], a[184:187], a[136:139], v[108:111]\n"
      "ds_read_b64_tr_b16 a[108:109], v13 offset:2048\n"
      "s_add_u32 m0, s98, 0xe000\n"
      "v_mfma_f32_16x16x32_bf16 v[108:111], a[188:191], a[140:143], v[108:111]\n"
      "v_mfma_f32_16x16x32_bf16 v[76:79], a[180:183], a[140:143], v[76:79]\n"
      "v_mfma_f32_16x16x32_bf16 v[76:79], a[176:179], a[136:139], v[76:79]\n"
      "ds_read_b64_tr_b16 a[110:111], v13 offset:5120\n"
      "v_mfma_f32_16x16x32_bf16 v[80:83], a[176:179], a[144:147], v[80:83]\n"
      "ds_read_b64_tr_b16 a[68:69], v12 offset:0\n"
      "v_mfma_f32_16x16x32_bf16 v[80:83], a[180:183], a[148:151], v[80:83]\n"
      "global_load_lds_dwordx4 v5, s[40:41] offset:0\n"
      "v_mfma_f32_16x16x32_bf16 v[112:115], a[188:191], a[148:151], v[112:115]\n"
      "ds_read_b64_tr_b16 a[70:71], v12 offset:3072\n"
      "v_mfma_f32_16x16x32_bf16 v[112:115], a[184:187], a[144:147], v[112:115]\n"
      "v_mfma_f32_16x16x32_bf16 v[116:119], a[184:187], a[152:155], v[116:119]\n"
      "v_mfma_f32_16x16x32_bf16 v[116:119], a[188:191], a[156:159], v[116:119]\n"
      "ds_read_b64_tr_b16 a[76:77], v13 offset:0\n"
      "v_mfma_f32_16x16x32_bf16 v[84:87], a[180:183], a[156:159], v[84:87]\n"
      "ds_read_b64_tr_b16 a[78:79], v13 offset:3072\n"
      "v_mfma_f32_16x16x32_bf16 v[84:87], a[176:179], a[152:155], v[84:87]\n"
      "global_load_lds_dwordx4 v6, s[40:41] offset:1024\n"
      "v_mfma_f32_16x16x32_bf16 v[88:91], a[176:179], a[160:163], v[88:91]\n"
      "v_mfma_f32_16x16x32_bf16 v[88:91], a[180:183], a[164:167], v[88:91]\n"
      "v_mfma_f32_16x16x32_bf16 v[120:123], a[188:191], a[164:167], v[120:123]\n"
      "v_mfma_f32_16x16x32_bf16 v[120:123], a[184:187], a[160:163], v[120:123]\n"
      "v_mfma_f32_16x16x32_bf16 v[124:127], a[184:187], a[168:171], v[124:127]\n"
      "v_mfma_f32_16x16x32_bf16 v[124:127], a[188:191], a[172:175], v[124:127]\n"
      "global_load_lds_dwordx4 v7, s[40:41] offset:2048\n"
      "v_mfma_f32_16x16x32_bf16 v[92:95], a[180:183], a[172:175], v[92:95]\n"
      "s_add_u32 s38, s38, 0x20000\n"
      "s_addc_u32 s39, s39, 0\n"
      "v_mfma_f32_16x16x32_bf16 v[92:95], a[176:179], a[168:171], v[92:95]\n"
      "s_add_u32 m0, s14, 0x15000\n"
      "s_waitcnt vmcnt(14) lgkmcnt(0)\n"
      "v_mfma_f32_16x16x32_bf16 v[128:131], a[0:3], a[80:83], v[128:131]\n"
      "s_barrier\n"
      "v_mfma_f32_16x16x32_bf16 v[128:131], a[4:7], a[84:87], v[128:131]\n"
      "ds_read_b64_tr_b16 a[192:193], v8 offset:1024\n"
      "v_mfma_f32_16x16x32_bf16 v[160:163], a[4:7], a[92:95], v[160:163]\n"
      "ds_read_b64_tr_b16 a[194:195], v8 offset:4096\n"
      "v_mfma_f32_16x16x32_bf16 v[160:163], a[0:3], a[88:91], v[160:163]\n"
      "global_load_lds_dwordx4 v1, s[38:39] offset:0\n"
      "v_mfma_f32_16x16x32_bf16 v[164:167], a[8:11], a[88:91], v[164:167]\n"
      "ds_read_b64_tr_b16 a[200:201], v9 offset:1024\n"
      "v_mfma_f32_16x16x32_bf16 v[164:167], a[12:15], a[92:95], v[164:167]\n"
      "v_mfma_f32_16x16x32_bf16 v[132:135], a[12:15], a[84:87], v[132:135]\n"
      "v_mfma_f32_16x16x32_bf16 v[132:135], a[8:11], a[80:83], v[132:135]\n"
      "ds_read_b64_tr_b16 a[202:203], v9 offset:4096\n"
      "v_mfma_f32_16x16x32_bf16 v[136:139], a[16:19], a[80:83], v[136:139]\n"
      "ds_read_b128 a[112:115], v14 offset:0\n"
      "v_mfma_f32_16x16x32_bf16 v[136:139], a[20:23], a[84:87], v[136:139]\n"
      "global_load_lds_dwordx4 v2, s[38:39] offset:1024\n"
      "v_mfma_f32_16x16x32_bf16 v[168:171], a[20:23], a[92:95], v[168:171]\n"
      "ds_read_b128 a[116:119], v15 offset:0\n"
      "s_add_u32 s40, s40, 0x30000\n"
      "v_mfma_f32_16x16x32_bf16 v[168:171], a[16:19], a[88:91], v[168:171]\n"
      "s_addc_u32 s41, s41, 0\n"
      "v_mfma_f32_16x16x32_bf16 v[172:175], a[24:27], a[88:91], v[172:175]\n"
      "v_mfma_f32_16x16x32_bf16 v[172:175], a[28:31], a[92:95], v[172:175]\n"
      "ds_read_b128 a[120:123], v14 offset:2048\n"
      "v_mfma_f32_16x16x32_bf16 v[140:143], a[28:31], a[84:87], v[140:143]\n"
      "ds_read_b128 a[124:127], v15 offset:2048\n"
      "v_mfma_f32_16x16x32_bf16 v[140:143], a[24:27], a[80:83], v[140:143]\n"
      "global_load_lds_dwordx4 v3, s[38:39] offset:2048\n"
      "v_mfma_f32_16x16x32_bf16 v[144:147], a[32:35], a[80:83], v[144:147]\n"
      "ds_read_b128 a[128:131], v14 offset:4096\n"
      "v_mfma_f32_16x16x32_bf16 v[144:147], a[36:39], a[84:87], v[144:147]\n"
      "v_mfma_f32_16x16x32_bf16 v[176:179], a[36:39], a[92:95], v[176:179]\n"
      "v_mfma_f32_16x16x32_bf16 v[176:179], a[32:35], a[88:91], v[176:179]\n"
      "ds_read_b128 a[132:135], v15 offset:4096\n"
      "v_mfma_f32_16x16x32_bf16 v[180:183], a[40:43], a[88:91], v[180:183]\n"
      "ds_read_b128 a[136:139], v14 offset:6144\n"
      "v_mfma_f32_16x16x32_bf16 v[180:183], a[44:47], a[92:95], v[180:183]\n"
      "global_load_lds_dwordx4 v4, s[38:39] offset:3072\n"
      "v_mfma_f32_16x16x32_bf16 v[148:151], a[44:47], a[84:87], v[148:151]\n"
      "ds_read_b128 a[140:143], v15 offset:6144\n"
      "s_add_u32 m0, s98, 0x15000\n"
      "v_mfma_f32_16x16x32_bf16 v[148:151], a[40:43], a[80:83], v[148:151]\n"
      "v_mfma_f32_16x16x32_bf16 v[152:155], a[48:51], a[80:83], v[152:155]\n"
      "v_mfma_f32_16x16x32_bf16 v[152:155], a[52:55], a[84:87], v[152:155]\n"
      "ds_read_b64_tr_b16 a[208:209], v8 offset:2048\n"
      "v_mfma_f32_16x16x32_bf16 v[184:187], a[52:55], a[92:95], v[184:187]\n"
      "ds_read_b64_tr_b16 a[210:211], v8 offset:5120\n"
      "v_mfma_f32_16x16x32_bf16 v[184:187], a[48:51], a[88:91], v[184:187]\n"
      "global_load_lds_dwordx4 v5, s[40:41] offset:0\n"
      "v_mfma_f32_16x16x32_bf16 v[188:191], a[56:59], a[88:91], v[188:191]\n"
      "ds_read_b64_tr_b16 a[216:217], v9 offset:2048\n"
      "v_mfma_f32_16x16x32_bf16 v[188:191], a[60:63], a[92:95], v[188:191]\n"
      "v_mfma_f32_16x16x32_bf16 v[156:159], a[60:63], a[84:87], v[156:159]\n"
      "v_mfma_f32_16x16x32_bf16 v[156:159], a[56:59], a[80:83], v[156:159]\n"
      "ds_read_b64_tr_b16 a[218:219], v9 offset:5120\n"
      "v_mfma_f32_16x16x32_bf16 v[192:195], a[0:3], a[96:99], v[192:195]\n"
      "ds_read_b64_tr_b16 a[176:177], v8 offset:0\n"
      "v_mfma_f32_16x16x32_bf16 v[192:195], a[4:7], a[100:103], v[192:195]\n"
      "global_load_lds_dwordx4 v6, s[40:41] offset:1024\n"
      "v_mfma_f32_16x16x32_bf16 v[224:227], a[4:7], a[108:111], v[224:227]\n"
      "ds_read_b64_tr_b16 a[178:179], v8 offset:3072\n"
      ".Lgm_w2:\n"
      "s_cbranch_execz .Lgm_r0\n"
      "v_mfma_f32_16x16x32_bf16 v[224:227], a[0:3], a[104:107], v[224:227]\n"
      "v_mfma_f32_16x16x32_bf16 v[228:231], a[8:11], a[104:107], v[228:231]\n"
      "v_mfma_f32_16x16x32_bf16 v[228:231], a[12:15], a[108:111], v[228:231]\n"
      "ds_read_b64_tr_b16 a[184:185], v9 offset:0\n"
      "v_mfma_f32_16x16x32_bf16 v[196:199], a[12:15], a[100:103], v[196:199]\n"
      "ds_read_b64_tr_b16 a[186:187], v9 offset:3072\n"
      "v_mfma_f32_16x16x32_bf16 v[196:199], a[8:11], a[96:99], v[196:199]\n"
      "global_load_lds_dwordx4 v7, s[40:41] offset:2048\n"
      "v_mfma_f32_16x16x32_bf16 v[200:203], a[16:19], a[96:99], v[200:203]\n"
      "s_add_u32 s3, s97, 7\n"
      "s_and_b32 s3, s3, 15\n"
      "v_mfma_f32_16x16x32_bf16 v[200:203], a[20:23], a[100:103], v[200:203]\n"
      "s_lshl_b32 s1, s3, 7\n"
      "s_add_u32 s38, s34, s1\n"
      "v_mfma_f32_16x16x32_bf16 v[232:235], a[20:23], a[108:111], v[232:235]\n"
      "s_addc_u32 s39, s35, 0\n"
      "s_add_u32 m0, s14, 0x1c000\n"
      "v_mfma_f32_16x16x32_bf16 v[232:235], a[16:19], a[104:107], v[232:235]\n"
      "v_mfma_f32_16x16x32_bf16 v[236:239], a[24:27], a[104:107], v[236:239]\n"
      "v_mfma_f32_16x16x32_bf16 v[236:239], a[28:31], a[108:111], v[236:239]\n"
      "global_load_lds_dwordx4 v1, s[38:39] offset:0\n"
      "v_mfma_f32_16x16x32_bf16 v[204:207], a[28:31], a[100:103], v[204:207]\n"
      "v_mfma_f32_16x16x32_bf16 v[204:207], a[24:27], a[96:99], v[204:207]\n"
      "s_waitcnt vmcnt(15) lgkmcnt(0)\n"
      "v_mfma_f32_16x16x32_bf16 v[208:211], a[32:35], a[96:99], v[208:211]\n"
      "s_barrier\n"
      "v_mfma_f32_16x16x32_bf16 v[208:211], a[36:39], a[100:103], v[208:211]\n"
      "ds_read_b64_tr_b16 a[196:197], v8 offset:29696\n"
      "v_mfma_f32_16x16x32_bf16 v[240:243], a[36:39], a[108:111], v[240:243]\n"
      "ds_read_b64_tr_b16 a[198:199], v8 offset:32768\n"
      "v_mfma_f32_16x16x32_bf16 v[240:243], a[32:35], a[104:107], v[240:243]\n"
      "global_load_lds_dwordx4 v2, s[38:39] offset:1024\n"
      "v_mfma_f32_16x16x32_bf16 v[244:247], a[40:43], a[104:107], v[244:247]\n"
      "ds_read_b64_tr_b16 a[204:205], v9 offset:29696\n"
      "s_mul_i32 s3, s3, 0x60000\n"
      "v_mfma_f32_16x16x32_bf16 v[244:247], a[44:47], a[108:111], v[244:247]\n"
      "s_add_u32 s40, s36, s3\n"
      "s_addc_u32 s41, s37, 0\n"
      "v_mfma_f32_16x16x32_bf16 v[212:215], a[44:47], a[100:103], v[212:215]\n"
      "v_mfma_f32_16x16x32_bf16 v[212:215], a[40:43], a[96:99], v[212:215]\n"
      "ds_read_b64_tr_b16 a[206:207], v9 offset:32768\n"
      "v_mfma_f32_16x16x32_bf16 v[216:219], a[48:51], a[96:99], v[216:219]\n"
      "ds_read_b128 a[144:147], v14 offset:28672\n"
      "v_mfma_f32_16x16x32_bf16 v[216:219], a[52:55], a[100:103], v[216:219]\n"
      "global_load_lds_dwordx4 v3, s[38:39] offset:2048\n"
      "v_mfma_f32_16x16x32_bf16 v[248:251], a[52:55], a[108:111], v[248:251]\n"
      "ds_read_b128 a[148:151], v15 offset:28672\n"
      "v_mfma_f32_16x16x32_bf16 v[248:251], a[48:51], a[104:107], v[248:251]\n"
      "v_mfma_f32_16x16x32_bf16 v[252:255], a[56:59], a[104:107], v[252:255]\n"
      "v_mfma_f32_16x16x32_bf16 v[252:255], a[60:63], a[108:111], v[252:255]\n"
      "ds_read_b128 a[152:155], v14 offset:30720\n"
      "v_mfma_f32_16x16x32_bf16 v[220:223], a[60:63], a[100:103], v[220:223]\n"
      "ds_read_b128 a[156:159], v15 offset:30720\n"
      "v_mfma_f32_16x16x32_bf16 v[220:223], a[56:59], a[96:99], v[220:223]\n"
      "global_load_lds_dwordx4 v4, s[38:39] offset:3072\n"
      "v_mfma_f32_16x16x32_bf16 v[64:67], a[64:67], a[0:3], v[64:67]\n"
      "ds_read_b128 a[160:163], v14 offset:32768\n"
      "s_add_u32 m0, s98, 0x1c000\n"
      "v_mfma_f32_16x16x32_bf16 v[64:67], a[68:71], a[4:7], v[64:67]\n"
      "v_mfma_f32_16x16x32_bf16 v[96:99], a[76:79], a[4:7], v[96:99]\n"
      "v_mfma_f32_16x16x32_bf16 v[96:99], a[72:75], a[0:3], v[96:99]\n"
      "ds_read_b128 a[164:167], v15 offset:32768\n"
      "v_mfma_f32_16x16x32_bf16 v[100:103], a[72:75], a[8:11], v[100:103]\n"
      "ds_read_b128 a[168:171], v14 offset:34816\n"
      "v_mfma_f32_16x16x32_bf16 v[100:103], a[76:79], a[12:15], v[100:103]\n"
      "global_load_lds_dwordx4 v5, s[40:41] offset:0\n"
      "v_mfma_f32_16x16x32_bf16 v[68:71], a[68:71], a[12:15], v[68:71]\n"
      "ds_read_b128 a[172:175], v15 offset:34816\n"
      "v_mfma_f32_16x16x32_bf16 v[68:71], a[64:67], a[8:11], v[68:71]\n"
      "v_mfma_f32_16x16x32_bf16 v[72:75], a[64:67], a[16:19], v[72:75]\n"
      "v_mfma_f32_16x16x32_bf16 v[72:75], a[68:71], a[20:23], v[72:75]\n"
      "ds_read_b64_tr_b16 a[212:213], v8 offset:30720\n"
      "v_mfma_f32_16x16x32_bf16 v[104:107], a[76:79], a[20:23], v[104:107]\n"
      "ds_read_b64_tr_b16 a[214:215], v8 offset:33792\n"
      "v_mfma_f32_16x16x32_bf16 v[104:107], a[72:75], a[16:19], v[104:107]\n"
      "global_load_lds_dwordx4 v6, s[40:41] offset:1024\n"
      "v_mfma_f32_16x16x32_bf16 v[108:111], a[72:75], a[24:27], v[108:111]\n"
      "ds_read_b64_tr_b16 a[220:221], v9 offset:30720\n"
      "v_mfma_f32_16x16x32_bf16 v[108:111], a[76:79], a[28:31], v[108:111]\n"
      "v_mfma_f32_16x16x32_bf16 v[76:79], a[68:71], a[28:31], v[76:79]\n"
      "v_mfma_f32_16x16x32_bf16 v[76:79], a[64:67], a[24:27], v[76:79]\n"
      "ds_read_b64_tr_b16 a[222:223], v9 offset:33792\n"
      "v_mfma_f32_16x16x32_bf16 v[80:83], a[64:67], a[32:35], v[80:83]\n"
      "ds_read_b64_tr_b16 a[180:181], v8 offset:28672\n"
      "v_mfma_f32_16x16x32_bf16 v[80:83], a[68:71], a[36:39], v[80:83]\n"
      "global_load_lds_dwordx4 v7, s[40:41] offset:2048\n"
      "v_mfma_f32_16x16x32_bf16 v[112:115], a[76:79], a[36:39], v[112:115]\n"
      "ds_read_b64_tr_b16 a[182:183], v8 offset:31744\n"
      "s_add_u32 s38, s38, 0x20000\n"
      "v_mfma_f32_16x16x32_bf16 v[112:115], a[72:75], a[32:35], v[112:115]\n"
      "s_addc_u32 s39, s39, 0\n"
      "s_add_u32 m0, s14, 0\n"
      "v_mfma_f32_16x16x32_bf16 v[116:119], a[72:75], a[40:43], v[116:119]\n"
      "v_mfma_f32_16x16x32_bf16 v[116:119], a[76:79], a[44:47], v[116:119]\n"
      "ds_read_b64_tr_b16 a[188:189], v9 offset:28672\n"
      "v_mfma_f32_16x16x32_bf16 v[84:87], a[68:71], a[44:47], v[84:87]\n"
      "ds_read_b64_tr_b16 a[190:191], v9 offset:31744\n"
      "v_mfma_f32_16x16x32_bf16 v[84:87], a[64:67], a[40:43], v[84:87]\n"
      "global_load_lds_dwordx4 v1, s[38:39] offset:0\n"
      "v_mfma_f32_16x16x32_bf16 v[88:91], a[64:67], a[48:51], v[88:91]\n"
      "v_mfma_f32_16x16x32_bf16 v[88:91], a[68:71], a[52:55], v[88:91]\n"
      "v_mfma_f32_16x16x32_bf16 v[120:123], a[76:79], a[52:55], v[120:123]\n"
      "v_mfma_f32_16x16x32_bf16 v[120:123], a[72:75], a[48:51], v[120:123]\n"
      "v_mfma_f32_16x16x32_bf16 v[124:127], a[72:75], a[56:59], v[124:127]\n"
      "v_mfma_f32_16x16x32_bf16 v[124:127], a[76:79], a[60:63], v[124:127]\n"
      "global_load_lds_dwordx4 v2, s[38:39] offset:1024\n"
      "v_mfma_f32_16x16x32_bf16 v[92:95], a[68:71], a[60:63], v[92:95]\n"
      "s_add_u32 s40, s40, 0x30000\n"
      "s_addc_u32 s41, s41, 0\n"
      "v_mfma_f32_16x16x32_bf16 v[92:95], a[64:67], a[56:59], v[92:95]\n"
      "s_waitcnt vmcnt(16) lgkmcnt(0)\n"
      "v_mfma_f32_16x16x32_bf16 v[128:131], a[112:115], a[192:195], v[128:131]\n"
      "s_barrier\n"
      "v_mfma_f32_16x16x32_bf16 v[128:131], a[116:119], a[196:199], v[128:131]\n"
      "ds_read_b64_tr_b16 a[80:81], v10 offset:1024\n"
      "v_mfma_f32_16x16x32_bf16 v[160:163], a[116:119], a[204:207], v[160:163]\n"
      "ds_read_b64_tr_b16 a[82:83], v10 offset:4096\n"
      "v_mfma_f32_16x16x32_bf16 v[160:163], a[112:115], a[200:203], v[160:163]\n"
      "global_load_lds_dwordx4 v3, s[38:39] offset:2048\n"
      "v_mfma_f32_16x16x32_bf16 v[164:167], a[120:123], a[200:203], v[164:167]\n"
      "ds_read_b64_tr_b16 a[88:89], v11 offset:1024\n"
      "v_mfma_f32_16x16x32_bf16 v[164:167], a[124:127], a[204:207], v[164:167]\n"
      "v_mfma_f32_16x16x32_bf16 v[132:135], a[124:127], a[196:199], v[132:135]\n"
      "v_mfma_f32_16x16x32_bf16 v[132:135], a[120:123], a[192:195], v[132:135]\n"
      "ds_read_b64_tr_b16 a[90:91], v11 offset:4096\n"
      "v_mfma_f32_16x16x32_bf16 v[136:139], a[128:131], a[192:195], v[136:139]\n"
      "ds_read_b128 a[0:3], v16 offset:0\n"
      "v_mfma_f32_16x16x32_bf16 v[136:139], a[132:135], a[196:199], v[136:139]\n"
      "global_load_lds_dwordx4 v4, s[38:39] offset:3072\n"
      "v_mfma_f32_16x16x32_bf16 v[168:171], a[132:135], a[204:207], v[168:171]\n"
      "ds_read_b128 a[4:7], v17 offset:0\n"
      "s_add_u32 m0, s98, 0\n"
      "v_mfma_f32_16x16x32_bf16 v[168:171], a[128:131], a[200:203], v[168:171]\n"
      "v_mfma_f32_16x16x32_bf16 v[172:175], a[136:139], a[200:203], v[172:175]\n"
      "v_mfma_f32_16x16x32_bf16 v[172:175], a[140:143], a[204:207], v[172:175]\n"
      "ds_read_b128 a[8:11], v16 offset:2048\n"
      "v_mfma_f32_16x16x32_bf16 v[140:143], a[140:143], a[196:199], v[140:143]\n"
      "ds_read_b128 a[12:15], v17 offset:2048\n"
      "v_mfma_f32_16x16x32_bf16 v[140:143], a[136:139], a[192:195], v[140:143]\n"
      "global_load_lds_dwordx4 v5, s[40:41] offset:0\n"
      "v_mfma_f32_16x16x32_bf16 v[144:147], a[144:147], a[192:195], v[144:147]\n"
      "ds_read_b128 a[16:19], v16 offset:4096\n"
      "v_mfma_f32_16x16x32_bf16 v[144:147], a[148:151], a[196:199], v[144:147]\n"
      "v_mfma_f32_16x16x32_bf16 v[176:179], a[148:151], a[204:207], v[176:179]\n"
      "v_mfma_f32_16x16x32_bf16 v[176:179], a[144:147], a[200:203], v[176:179]\n"
      "ds_read_b128 a[20:23], v17 offset:4096\n"
      "v_mfma_f32_16x16x32_bf16 v[180:183], a[152:155], a[200:203], v[180:183]\n"
      "ds_read_b128 a[24:27], v16 offset:6144\n"
      "v_mfma_f32_16x16x32_bf16 v[180:183], a[156:159], a[204:207], v[180:183]\n"
      "global_load_lds_dwordx4 v6, s[40:41] offset:1024\n"
      "v_mfma_f32_16x16x32_bf16 v[148:151], a[156:159], a[196:199], v[148:151]\n"
      "ds_read_b128 a[28:31], v17 offset:6144\n"
      "v_mfma_f32_16x16x32_bf16 v[148:151], a[152:155], a[192:195], v[148:151]\n"
      "v_mfma_f32_16x16x32_bf16 v[152:155], a[160:163], a[192:195], v[152:155]\n"
      "v_mfma_f32_16x16x32_bf16 v[152:155], a[164:167], a[196:199], v[152:155]\n"
      "ds_read_b64_tr_b16 a[96:97], v10 offset:2048\n"
      "v_mfma_f32_16x16x32_bf16 v[184:187], a[164:167], a[204:207], v[184:187]\n"
      "ds_read_b64_tr_b16 a[98:99], v10 offset:5120\n"
      "v_mfma_f32_16x16x32_bf16 v[184:187], a[160:163], a[200:203], v[184:187]\n"
      "global_load_lds_dwordx4 v7, s[40:41] offset:2048\n"
      "v_mfma_f32_16x16x32_bf16 v[188:191], a[168:171], a[200:203], v[188:191]\n"
      "ds_read_b64_tr_b16 a[104:105], v11 offset:2048\n"
      "s_add_u32 s3, s97, 8\n"
      "v_mfma_f32_16x16x32_bf16 v[188:191], a[172:175], a[204:207], v[188:191]\n"
      "s_and_b32 s3, s3, 15\n"
      "s_lshl_b32 s1, s3, 7\n"
      "v_mfma_f32_16x16x32_bf16 v[156:159], a[172:175], a[196:199], v[156:159]\n"
      "s_add_u32 s38, s34, s1\n"
      "s_addc_u32 s39, s35, 0\n"
      "v_mfma_f32_16x16x32_bf16 v[156:159], a[168:171], a[192:195], v[156:159]\n"
      "ds_read_b64_tr_b16 a[106:107], v11 offset:5120\n"
      "s_add_u32 m0, s14, 0x7000\n"
      "v_mfma_f32_16x16x32_bf16 v[192:195], a[112:115], a[208:211], v[192:195]\n"
      "ds_read_b64_tr_b16 a[64:65], v10 offset:0\n"
      "v_mfma_f32_16x16x32_bf16 v[192:195], a[116:119], a[212:215], v[192:195]\n"
      "global_load_lds_dwordx4 v1, s[38:39] offset:0\n"
      "v_mfma_f32_16x16x32_bf16 v[224:227], a[116:119], a[220:223], v[224:227]\n"
      "ds_read_b64_tr_b16 a[66:67], v10 offset:3072\n"
      "v_mfma_f32_16x16x32_bf16 v[224:227], a[112:115], a[216:219], v[224:227]\n"
      "v_mfma_f32_16x16x32_bf16 v[228:231], a[120:123], a[216:219], v[228:231]\n"
      "v_mfma_f32_16x16x32_bf16 v[228:231], a[124:127], a[220:223], v[228:231]\n"
      "ds_read_b64_tr_b16 a[72:73], v11 offset:0\n"
      "v_mfma_f32_16x16x32_bf16 v[196:199], a[124:127], a[212:215], v[196:199]\n"
      "ds_read_b64_tr_b16 a[74:75], v11 offset:3072\n"
      "v_mfma_f32_16x16x32_bf16 v[196:199], a[120:123], a[208:211], v[196:199]\n"
      "global_load_lds_dwordx4 v2, s[38:39] offset:1024\n"
      "v_mfma_f32_16x16x32_bf16 v[200:203], a[128:131], a[208:211], v[200:203]\n"
      "s_mul_i32 s3, s3, 0x60000\n"
      "s_add_u32 s40, s36, s3\n"
      "v_mfma_f32_16x16x32_bf16 v[200:203], a[132:135], a[212:215], v[200:203]\n"
      "s_addc_u32 s41, s37, 0\n"
      "v_mfma_f32_16x16x32_bf16 v[232:235], a[132:135], a[220:223], v[232:235]\n"
      "v_mfma_f32_16x16x32_bf16 v[232:235], a[128:131], a[216:219], v[232:235]\n"
      "v_mfma_f32_16x16x32_bf16 v[236:239], a[136:139], a[216:219], v[236:239]\n"
      "v_mfma_f32_16x16x32_bf16 v[236:239], a[140:143], a[220:223], v[236:239]\n"
      "global_load_lds_dwordx4 v3, s[38:39] offset:2048\n"
      "v_mfma_f32_16x16x32_bf16 v[204:207], a[140:143], a[212:215], v[204:207]\n"
      "v_mfma_f32_16x16x32_bf16 v[204:207], a[136:139], a[208:211], v[204:207]\n"
      "s_waitcnt vmcnt(17) lgkmcnt(0)\n"
      "v_mfma_f32_16x16x32_bf16 v[208:211], a[144:147], a[208:211], v[208:211]\n"
      "s_barrier\n"
      "v_mfma_f32_16x16x32_bf16 v[208:211], a[148:151], a[212:215], v[208:211]\n"
      "ds_read_b64_tr_b16 a[84:85], v10 offset:29696\n"
      "v_mfma_f32_16x16x32_bf16 v[240:243], a[148:151], a[220:223], v[240:243]\n"
      "ds_read_b64_tr_b16 a[86:87], v10 offset:32768\n"
      "v_mfma_f32_16x16x32_bf16 v[240:243], a[144:147], a[216:219], v[240:243]\n"
      "global_load_lds_dwordx4 v4, s[38:39] offset:3072\n"
      "v_mfma_f32_16x16x32_bf16 v[244:247], a[152:155], a[216:219], v[244:247]\n"
      "ds_read_b64_tr_b16 a[92:93], v11 offset:29696\n"
      "s_add_u32 m0, s98, 0x7000\n"
      "v_mfma_f32_16x16x32_bf16 v[244:247], a[156:159], a[220:223], v[244:247]\n"
      "v_mfma_f32_16x16x32_bf16 v[212:215], a[156:159], a[212:215], v[212:215]\n"
      "v_mfma_f32_16x16x32_bf16 v[212:215], a[152:155], a[208:211], v[212:215]\n"
      "ds_read_b64_tr_b16 a[94:95], v11 offset:32768\n"
      "v_mfma_f32_16x16x32_bf16 v[216:219], a[160:163], a[208:211], v[216:219]\n"
      "ds_read_b128 a[32:35], v16 offset:28672\n"
      "v_mfma_f32_16x16x32_bf16 v[216:219], a[164:167], a[212:215], v[216:219]\n"
      "global_load_lds_dwordx4 v5, s[40:41] offset:0\n"
      "v_mfma_f32_16x16x32_bf16 v[248:251], a[164:167], a[220:223], v[248:251]\n"
      "ds_read_b128 a[36:39], v17 offset:28672\n"
      "v_mfma_f32_16x16x32_bf16 v[248:251], a[160:163], a[216:219], v[248:251]\n"
      "v_mfma_f32_16x16x32_bf16 v[252:255], a[168:171], a[216:219], v[252:255]\n"
      "v_mfma_f32_16x16x32_bf16 v[252:255], a[172:175], a[220:223], v[252:255]\n"
      "ds_read_b128 a[40:43], v16 offset:30720\n"
      "v_mfma_f32_16x16x32_bf16 v[220:223], a[172:175], a[212:215], v[220:223]\n"
      "ds_read_b128 a[44:47], v17 offset:30720\n"
      "v_mfma_f32_16x16x32_bf16 v[220:223], a[168:171], a[208:211], v[220:223]\n"
      "global_load_lds_dwordx4 v6, s[40:41] offset:1024\n"
      "v_mfma_f32_16x16x32_bf16 v[64:67], a[176:179], a[112:115], v[64:67]\n"
      "ds_read_b128 a[48:51], v16 offset:32768\n"
      "v_mfma_f32_16x16x32_bf16 v[64:67], a[180:183], a[116:119], v[64:67]\n"
      "v_mfma_f32_16x16x32_bf16 v[96:99], a[188:191], a[116:119], v[96:99]\n"
      "v_mfma_f32_16x16x32_bf16 v[96:99], a[184:187], a[112:115], v[96:99]\n"
      "ds_read_b128 a[52:55], v17 offset:32768\n"
      "v_mfma_f32_16x16x32_bf16 v[100:103], a[184:187], a[120:123], v[100:103]\n"
      "ds_read_b128 a[56:59], v16 offset:34816\n"
      "v_mfma_f32_16x16x32_bf16 v[100:103], a[188:191], a[124:127], v[100:103]\n"
      "global_load_lds_dwordx4 v7, s[40:41] offset:2048\n"
      "v_mfma_f32_16x16x32_bf16 v[68:71], a[180:183], a[124:127], v[68:71]\n"
      "ds_read_b128 a[60:63], v17 offset:34816\n"
      "s_add_u32 s38, s38, 0x20000\n"
      "v_mfma_f32_16x16x32_bf16 v[68:71], a[176:179], a[120:123], v[68:71]\n"
      "s_addc_u32 s39, s39, 0\n"
      "s_add_u32 m0, s14, 0xe000\n"
      "v_mfma_f32_16x16x32_bf16 v[72:75], a[176:179], a[128:131], v[72:75]\n"
      "v_mfma_f32_16x16x32_bf16 v[72:75], a[180:183], a[132:135], v[72:75]\n"
      "ds_read_b64_tr_b16 a[100:101], v10 offset:30720\n"
      "v_mfma_f32_16x16x32_bf16 v[104:107], a[188:191], a[132:135], v[104:107]\n"
      "ds_read_b64_tr_b16 a[102:103], v10 offset:33792\n"
      "v_mfma_f32_16x16x32_bf16 v[104:107], a[184:187], a[128:131], v[104:107]\n"
      "global_load_lds_dwordx4 v1, s[38:39] offset:0\n"
      "v_mfma_f32_16x16x32_bf16 v[108:111], a[184:187], a[136:139], v[108:111]\n"
      "ds_read_b64_tr_b16 a[108:109], v11 offset:30720\n"
      "v_mfma_f32_16x16x32_bf16 v[108:111], a[188:191], a[140:143], v[108:111]\n"
      "v_mfma_f32_16x16x32_bf16 v[76:79], a[180:183], a[140:143], v[76:79]\n"
      "v_mfma_f32_16x16x32_bf16 v[76:79], a[176:179], a[136:139], v[76:79]\n"
      "ds_read_b64_tr_b16 a[110:111], v11 offset:33792\n"
      "v_mfma_f32_16x16x32_bf16 v[80:83], a[176:179], a[144:147], v[80:83]\n"
      "ds_read_b64_tr_b16 a[68:69], v10 offset:28672\n"
      "v_mfma_f32_16x16x32_bf16 v[80:83], a[180:183], a[148:151], v[80:83]\n"
      "global_load_lds_dwordx4 v2, s[38:39] offset:1024\n"
      "v_mfma_f32_16x16x32_bf16 v[112:115], a[188:191], a[148:151], v[112:115]\n"
      "ds_read_b64_tr_b16 a[70:71], v10 offset:31744\n"
      "s_add_u32 s40, s40, 0x30000\n"
      "v_mfma_f32_16x16x32_bf16 v[112:115], a[184:187], a[144:147], v[112:115]\n"
      "s_addc_u32 s41, s41, 0\n"
      "v_mfma_f32_16x16x32_bf16 v[116:119], a[184:187], a[152:155], v[116:119]\n"
      "v_mfma_f32_16x16x32_bf16 v[116:119], a[188:191], a[156:159], v[116:119]\n"
      "ds_read_b64_tr_b16 a[76:77], v11 offset:28672\n"
      "v_mfma_f32_16x16x32_bf16 v[84:87], a[180:183], a[156:159], v[84:87]\n"
      "ds_read_b64_tr_b16 a[78:79], v11 offset:31744\n"
      "v_mfma_f32_16x16x32_bf16 v[84:87], a[176:179], a[152:155], v[84:87]\n"
      "global_load_lds_dwordx4 v3, s[38:39] offset:2048\n"
      "v_mfma_f32_16x16x32_bf16 v[88:91], a[176:179], a[160:163], v[88:91]\n"
      "v_mfma_f32_16x16x32_bf16 v[88:91], a[180:183], a[164:167], v[88:91]\n"
      "v_mfma_f32_16x16x32_bf16 v[120:123], a[188:191], a[164:167], v[120:123]\n"
      "v_mfma_f32_16x16x32_bf16 v[120:123], a[184:187], a[160:163], v[120:123]\n"
      "v_mfma_f32_16x16x32_bf16 v[124:127], a[184:187], a[168:171], v[124:127]\n"
      "v_mfma_f32_16x16x32_bf16 v[124:127], a[188:191], a[172:175], v[124:127]\n"
      "global_load_lds_dwordx4 v4, s[38:39] offset:3072\n"
      "v_mfma_f32_16x16x32_bf16 v[92:95], a[180:183], a[172:175], v[92:95]\n"
      "s_add_u32 m0, s98, 0xe000\n"
      "v_mfma_f32_16x16x32_bf16 v[92:95], a[176:179], a[168:171], v[92:95]\n"
      "s_waitcnt vmcnt(18) lgkmcnt(0)\n"
      "v_mfma_f32_16x16x32_bf16 v[128:131], a[0:3], a[80:83], v[128:131]\n"
      "s_barrier\n"
      "v_mfma_f32_16x16x32_bf16 v[128:131], a[4:7], a[84:87], v[128:131]\n"
      "ds_read_b64_tr_b16 a[192:193], v12 offset:1024\n"
      "v_mfma_f32_16x16x32_bf16 v[160:163], a[4:7], a[92:95], v[160:163]\n"
      "ds_read_b64_tr_b16 a[194:195], v12 offset:4096\n"
      "v_mfma_f32_16x16x32_bf16 v[160:163], a[0:3], a[88:91], v[160:163]\n"
      "global_load_lds_dwordx4 v5, s[40:41] offset:0\n"
      "v_mfma_f32_16x16x32_bf16 v[164:167], a[8:11], a[88:91], v[164:167]\n"
      "ds_read_b64_tr_b16 a[200:201], v13 offset:1024\n"
      "v_mfma_f32_16x16x32_bf16 v[164:167], a[12:15], a[92:95], v[164:167]\n"
      "v_mfma_f32_16x16x32_bf16 v[132:135], a[12:15], a[84:87], v[132:135]\n"
      "v_mfma_f32_16x16x32_bf16 v[132:135], a[8:11], a[80:83], v[132:135]\n"
      "ds_read_b64_tr_b16 a[202:203], v13 offset:4096\n"
      "v_mfma_f32_16x16x32_bf16 v[136:139], a[16:19], a[80:83], v[136:139]\n"
      "ds_read_b128 a[112:115], v18 offset:0\n"
      "v_mfma_f32_16x16x32_bf16 v[136:139], a[20:23], a[84:87], v[136:139]\n"
      "global_load_lds_dwordx4 v6, s[40:41] offset:1024\n"
      "v_mfma_f32_16x16x32_bf16 v[168:171], a[20:23], a[92:95], v[168:171]\n"
      "ds_read_b128 a[116:119], v19 offset:0\n"
      "v_mfma_f32_16x16x32_bf16 v[168:171], a[16:19], a[88:91], v[168:171]\n"
      "v_mfma_f32_16x16x32_bf16 v[172:175], a[24:27], a[88:91], v[172:175]\n"
      "v_mfma_f32_16x16x32_bf16 v[172:175], a[28:31], a[92:95], v[172:175]\n"
      "ds_read_b128 a[120:123], v18 offset:2048\n"
      "v_mfma_f32_16x16x32_bf16 v[140:143], a[28:31], a[84:87], v[140:143]\n"
      "ds_read_b128 a[124:127], v19 offset:2048\n"
      "v_mfma_f32_16x16x32_bf16 v[140:143], a[24:27], a[80:83], v[140:143]\n"
      "global_load_lds_dwordx4 v7, s[40:41] offset:2048\n"
      "v_mfma_f32_16x16x32_bf16 v[144:147], a[32:35], a[80:83], v[144:147]\n"
      "ds_read_b128 a[128:131], v18 offset:4096\n"
      "s_add_u32 s3, s97, 9\n"
      "v_mfma_f32_16x16x32_bf16 v[144:147], a[36:39], a[84:87], v[144:147]\n"
      "s_and_b32 s3, s3, 15\n"
      "s_lshl_b32 s1, s3, 7\n"
      "v_mfma_f32_16x16x32_bf16 v[176:179], a[36:39], a[92:95], v[176:179]\n"
      "s_add_u32 s38, s34, s1\n"
      "s_addc_u32 s39, s35, 0\n"
      "v_mfma_f32_16x16x32_bf16 v[176:179], a[32:35], a[88:91], v[176:179]\n"
      "ds_read_b128 a[132:135], v19 offset:4096\n"
      "s_add_u32 m0, s14, 0x15000\n"
      "v_mfma_f32_16x16x32_bf16 v[180:183], a[40:43], a[88:91], v[180:183]\n"
      "ds_read_b128 a[136:139], v18 offset:6144\n"
      "v_mfma_f32_16x16x32_bf16 v[180:183], a[44:47], a[92:95], v[180:183]\n"
      "global_load_lds_dwordx4 v1, s[38:39] offset:0\n"
      "v_mfma_f32_16x16x32_bf16 v[148:151], a[44:47], a[84:87], v[148:151]\n"
      "ds_read_b128 a[140:143], v19 offset:6144\n"
      "v_mfma_f32_16x16x32_bf16 v[148:151], a[40:43], a[80:83], v[148:151]\n"
      "v_mfma_f32_16x16x32_bf16 v[152:155], a[48:51], a[80:83], v[152:155]\n"
      "v_mfma_f32_16x16x32_bf16 v[152:155], a[52:55], a[84:87], v[152:155]\n"
      "ds_read_b64_tr_b16 a[208:209], v12 offset:2048\n"
      "v_mfma_f32_16x16x32_bf16 v[184:187], a[52:55], a[92:95], v[184:187]\n"
      "ds_read_b64_tr_b16 a[210:211], v12 offset:5120\n"
      "v_mfma_f32_16x16x32_bf16 v[184:187], a[48:51], a[88:91], v[184:187]\n"
      "global_load_lds_dwordx4 v2, s[38:39] offset:1024\n"
      "v_mfma_f32_16x16x32_bf16 v[188:191], a[56:59], a[88:91], v[188:191]\n"
      "ds_read_b64_tr_b16 a[216:217], v13 offset:2048\n"
      "s_mul_i32 s3, s3, 0x60000\n"
      "v_mfma_f32_16x16x32_bf16 v[188:191], a[60:63], a[92:95], v[188:191]\n"
      "s_add_u32 s40, s36, s3\n"
      "s_addc_u32 s41, s37, 0\n"
      "v_mfma_f32_16x16x32_bf16 v[156:159], a[60:63], a[84:87], v[156:159]\n"
      "v_mfma_f32_16x16x32_bf16 v[156:159], a[56:59], a[80:83], v[156:159]\n"
      "ds_read_b64_tr_b16 a[218:219], v13 offset:5120\n"
      "v_mfma_f32_16x16x32_bf16 v[192:195], a[0:3], a[96:99], v[192:195]\n"
      "ds_read_b64_tr_b16 a[176:177], v12 offset:0\n"
      "v_mfma_f32_16x16x32_bf16 v[192:195], a[4:7], a[100:103], v[192:195]\n"
      "global_load_lds_dwordx4 v3, s[38:39] offset:2048\n"
      "v_mfma_f32_16x16x32_bf16 v[224:227], a[4:7], a[108:111], v[224:227]\n"
      "ds_read_b64_tr_b16 a[178:179], v12 offset:3072\n"
      "v_mfma_f32_16x16x32_bf16 v[224:227], a[0:3], a[104:107], v[224:227]\n"
      "v_mfma_f32_16x16x32_bf16 v[228:231], a[8:11], a[104:107], v[228:231]\n"
      "v_mfma_f32_16x16x32_bf16 v[228:231], a[12:15], a[108:111], v[228:231]\n"
      "ds_read_b64_tr_b16 a[184:185], v13 offset:0\n"
      "v_mfma_f32_16x16x32_bf16 v[196:199], a[12:15], a[100:103], v[196:199]\n"
      "ds_read_b64_tr_b16 a[186:187], v13 offset:3072\n"
      "v_mfma_f32_16x16x32_bf16 v[196:199], a[8:11], a[96:99], v[196:199]\n"
      "global_load_lds_dwordx4 v4, s[38:39] offset:3072\n"
      "v_mfma_f32_16x16x32_bf16 v[200:203], a[16:19], a[96:99], v[200:203]\n"
      "s_add_u32 m0, s98, 0x15000\n"
      "v_mfma_f32_16x16x32_bf16 v[200:203], a[20:23], a[100:103], v[200:203]\n"
      "v_mfma_f32_16x16x32_bf16 v[232:235], a[20:23], a[108:111], v[232:235]\n"
      "v_mfma_f32_16x16x32_bf16 v[232:235], a[16:19], a[104:107], v[232:235]\n"
      "v_mfma_f32_16x16x32_bf16 v[236:239], a[24:27], a[104:107], v[236:239]\n"
      "v_mfma_f32_16x16x32_bf16 v[236:239], a[28:31], a[108:111], v[236:239]\n"
      "global_load_lds_dwordx4 v5, s[40:41] offset:0\n"
      "v_mfma_f32_16x16x32_bf16 v[204:207], a[28:31], a[100:103], v[204:207]\n"
      "v_mfma_f32_16x16x32_bf16 v[204:207], a[24:27], a[96:99], v[204:207]\n"
      "s_waitcnt vmcnt(19) lgkmcnt(0)\n"
      "v_mfma_f32_16x16x32_bf16 v[208:211], a[32:35], a[96:99], v[208:211]\n"
      "s_barrier\n"
      "v_mfma_f32_16x16x32_bf16 v[208:211], a[36:39], a[100:103], v[208:211]\n"
      "ds_read_b64_tr_b16 a[196:197], v8 offset:1024\n"
      "v_mfma_f32_16x16x32_bf16 v[240:243], a[36:39], a[108:111], v[240:243]\n"
      "ds_read_b64_tr_b16 a[198:199], v8 offset:4096\n"
      "v_mfma_f32_16x16x32_bf16 v[240:243], a[32:35], a[104:107], v[240:243]\n"
      "global_load_lds_dwordx4 v6, s[40:41] offset:1024\n"
      "v_mfma_f32_16x16x32_bf16 v[244:247], a[40:43], a[104:107], v[244:247]\n"
      "ds_read_b64_tr_b16 a[204:205], v9 offset:1024\n"
      "v_mfma_f32_16x16x32_bf16 v[244:247], a[44:47], a[108:111], v[244:247]\n"
      "v_mfma_f32_16x16x32_bf16 v[212:215], a[44:47], a[100:103], v[212:215]\n"
      "v_mfma_f32_16x16x32_bf16 v[212:215], a[40:43], a[96:99], v[212:215]\n"
      "ds_read_b64_tr_b16 a[206:207], v9 offset:4096\n"
      "v_mfma_f32_16x16x32_bf16 v[216:219], a[48:51], a[96:99], v[216:219]\n"
      "ds_read_b128 a[144:147], v14 offset:0\n"
      "v_mfma_f32_16x16x32_bf16 v[216:219], a[52:55], a[100:103], v[216:219]\n"
      "global_load_lds_dwordx4 v7, s[40:41] offset:2048\n"
      "v_mfma_f32_16x16x32_bf16 v[248:251], a[52:55], a[108:111], v[248:251]\n"
      "ds_read_b128 a[148:151], v15 offset:0\n"
      "s_add_u32 s38, s38, 0x20000\n"
      "v_mfma_f32_16x16x32_bf16 v[248:251], a[48:51], a[104:107], v[248:251]\n"
      "s_addc_u32 s39, s39, 0\n"
      "s_add_u32 m0, s14, 0x1c000\n"
      "v_mfma_f32_16x16x32_bf16 v[252:255], a[56:59], a[104:107], v[252:255]\n"
      "v_mfma_f32_16x16x32_bf16 v[252:255], a[60:63], a[108:111], v[252:255]\n"
      "ds_read_b128 a[152:155], v14 offset:2048\n"
      "v_mfma_f32_16x16x32_bf16 v[220:223], a[60:63], a[100:103], v[220:223]\n"
      "ds_read_b128 a[156:159], v15 offset:2048\n"
      "v_mfma_f32_16x16x32_bf16 v[220:223], a[56:59], a[96:99], v[220:223]\n"
      "global_load_lds_dwordx4 v1, s[38:39] offset:0\n"
      "v_mfma_f32_16x16x32_bf16 v[64:67], a[64:67], a[0:3], v[64:67]\n"
      "ds_read_b128 a[160:163], v14 offset:4096\n"
      "v_mfma_f32_16x16x32_bf16 v[64:67], a[68:71], a[4:7], v[64:67]\n"
      "v_mfma_f32_16x16x32_bf16 v[96:99], a[76:79], a[4:7], v[96:99]\n"
      "v_mfma_f32_16x16x32_bf16 v[96:99], a[72:75], a[0:3], v[96:99]\n"
      "ds_read_b128 a[164:167], v15 offset:4096\n"
      "v_mfma_f32_16x16x32_bf16 v[100:103], a[72:75], a[8:11], v[100:103]\n"
      "ds_read_b128 a[168:171], v14 offset:6144\n"
      "v_mfma_f32_16x16x32_bf16 v[100:103], a[76:79], a[12:15], v[100:103]\n"
      "global_load_lds_dwordx4 v2, s[38:39] offset:1024\n"
      "v_mfma_f32_16x16x32_bf16 v[68:71], a[68:71], a[12:15], v[68:71]\n"
      "ds_read_b128 a[172:175], v15 offset:6144\n"
      "s_add_u32 s40, s40, 0x30000\n"
      "v_mfma_f32_16x16x32_bf16 v[68:71], a[64:67], a[8:11], v[68:71]\n"
      "s_addc_u32 s41, s41, 0\n"
      "v_mfma_f32_16x16x32_bf16 v[72:75], a[64:67], a[16:19], v[72:75]\n"
      "v_mfma_f32_16x16x32_bf16 v[72:75], a[68:71], a[20:23], v[72:75]\n"
      "ds_read_b64_tr_b16 a[212:213], v8 offset:2048\n"
      "v_mfma_f32_16x16x32_bf16 v[104:107], a[76:79], a[20:23], v[104:107]\n"
      "ds_read_b64_tr_b16 a[214:215], v8 offset:5120\n"
      "v_mfma_f32_16x16x32_bf16 v[104:107], a[72:75], a[16:19], v[104:107]\n"
      "global_load_lds_dwordx4 v3, s[38:39] offset:2048\n"
      "v_mfma_f32_16x16x32_bf16 v[108:111], a[72:75], a[24:27], v[108:111]\n"
      "ds_read_b64_tr_b16 a[220:221], v9 offset:2048\n"
      "v_mfma_f32_16x16x32_bf16 v[108:111], a[76:79], a[28:31], v[108:111]\n"
      "v_mfma_f32_16x16x32_bf16 v[76:79], a[68:71], a[28:31], v[76:79]\n"
      "v_mfma_f32_16x16x32_bf16 v[76:79], a[64:67], a[24:27], v[76:79]\n"
      "ds_read_b64_tr_b16 a[222:223], v9 offset:5120\n"
      "v_mfma_f32_16x16x32_bf16 v[80:83], a[64:67], a[32:35], v[80:83]\n"
      "ds_read_b64_tr_b16 a[180:181], v8 offset:0\n"
      "v_mfma_f32_16x16x32_bf16 v[80:83], a[68:71], a[36:39], v[80:83]\n"
      "global_load_lds_dwordx4 v4, s[38:39] offset:3072\n"
      "v_mfma_f32_16x16x32_bf16 v[112:115], a[76:79], a[36:39], v[112:115]\n"
      "ds_read_b64_tr_b16 a[182:183], v8 offset:3072\n"
      "s_add_u32 m0, s98, 0x1c000\n"
      "v_mfma_f32_16x16x32_bf16 v[112:115], a[72:75], a[32:35], v[112:115]\n"
      "v_mfma_f32_16x16x32_bf16 v[116:119], a[72:75], a[40:43], v[116:119]\n"
      "v_mfma_f32_16x16x32_bf16 v[116:119], a[76:79], a[44:47], v[116:119]\n"
      "ds_read_b64_tr_b16 a[188:189], v9 offset:0\n"
      "v_mfma_f32_16x16x32_bf16 v[84:87], a[68:71], a[44:47], v[84:87]\n"
      "ds_read_b64_tr_b16 a[190:191], v9 offset:3072\n"
      "v_mfma_f32_16x16x32_bf16 v[84:87], a[64:67], a[40:43], v[84:87]\n"
      "global_load_lds_dwordx4 v5, s[40:41] offset:0\n"
      "v_mfma_f32_16x16x32_bf16 v[88:91], a[64:67], a[48:51], v[88:91]\n"
      "v_mfma_f32_16x16x32_bf16 v[88:91], a[68:71], a[52:55], v[88:91]\n"
      "v_mfma_f32_16x16x32_bf16 v[120:123], a[76:79], a[52:55], v[120:123]\n"
      "v_mfma_f32_16x16x32_bf16 v[120:123], a[72:75], a[48:51], v[120:123]\n"
      "v_mfma_f32_16x16x32_bf16 v[124:127], a[72:75], a[56:59], v[124:127]\n"
      "v_mfma_f32_16x16x32_bf16 v[124:127], a[76:79], a[60:63], v[124:127]\n"
      "global_load_lds_dwordx4 v6, s[40:41] offset:1024\n"
      "v_mfma_f32_16x16x32_bf16 v[92:95], a[68:71], a[60:63], v[92:95]\n"
      "v_mfma_f32_16x16x32_bf16 v[92:95], a[64:67], a[56:59], v[92:95]\n"
      "s_waitcnt vmcnt(20) lgkmcnt(0)\n"
      "v_mfma_f32_16x16x32_bf16 v[128:131], a[112:115], a[192:195], v[128:131]\n"
      "s_barrier\n"
      "v_mfma_f32_16x16x32_bf16 v[128:131], a[116:119], a[196:199], v[128:131]\n"
      "ds_read_b64_tr_b16 a[80:81], v8 offset:29696\n"
      "v_mfma_f32_16x16x32_bf16 v[160:163], a[116:119], a[204:207], v[160:163]\n"
      "ds_read_b64_tr_b16 a[82:83], v8 offset:32768\n"
      "v_mfma_f32_16x16x32_bf16 v[160:163], a[112:115], a[200:203], v[160:163]\n"
      "global_load_lds_dwordx4 v7, s[40:41] offset:2048\n"
      "v_mfma_f32_16x16x32_bf16 v[164:167], a[120:123], a[200:203], v[164:167]\n"
      "ds_read_b64_tr_b16 a[88:89], v9 offset:29696\n"
      "s_add_u32 s3, s97, 10\n"
      "v_mfma_f32_16x16x32_bf16 v[164:167], a[124:127], a[204:207], v[164:167]\n"
      "s_and_b32 s3, s3, 15\n"
      "s_lshl_b32 s1, s3, 7\n"
      "v_mfma_f32_16x16x32_bf16 v[132:135], a[124:127], a[196:199], v[132:135]\n"
      "s_add_u32 s38, s34, s1\n"
      "s_addc_u32 s39, s35, 0\n"
      "v_mfma_f32_16x16x32_bf16 v[132:135], a[120:123], a[192:195], v[132:135]\n"
      "ds_read_b64_tr_b16 a[90:91], v9 offset:32768\n"
      "s_add_u32 m0, s14, 0\n"
      "v_mfma_f32_16x16x32_bf16 v[136:139], a[128:131], a[192:195], v[136:139]\n"
      "ds_read_b128 a[0:3], v14 offset:28672\n"
      "v_mfma_f32_16x16x32_bf16 v[136:139], a[132:135], a[196:199], v[136:139]\n"
      "global_load_lds_dwordx4 v1, s[38:39] offset:0\n"
      "v_mfma_f32_16x16x32_bf16 v[168:171], a[132:135], a[204:207], v[168:171]\n"
      "ds_read_b128 a[4:7], v15 offset:28672\n"
      "v_mfma_f32_16x16x32_bf16 v[168:171], a[128:131], a[200:203], v[168:171]\n"
      "v_mfma_f32_16x16x32_bf16 v[172:175], a[136:139], a[200:203], v[172:175]\n"
      "v_mfma_f32_16x16x32_bf16 v[172:175], a[140:143], a[204:207], v[172:175]\n"
      "ds_read_b128 a[8:11], v14 offset:30720\n"
      "v_mfma_f32_16x16x32_bf16 v[140:143], a[140:143], a[196:199], v[140:143]\n"
      "ds_read_b128 a[12:15], v15 offset:30720\n"
      "v_mfma_f32_16x16x32_bf16 v[140:143], a[136:139], a[192:195], v[140:143]\n"
      "global_load_lds_dwordx4 v2, s[38:39] offset:1024\n"
      "v_mfma_f32_16x16x32_bf16 v[144:147], a[144:147], a[192:195], v[144:147]\n"
      "ds_read_b128 a[16:19], v14 offset:32768\n"
      "s_mul_i32 s3, s3, 0x60000\n"
      "v_mfma_f32_16x16x32_bf16 v[144:147], a[148:151], a[196:199], v[144:147]\n"
      "s_add_u32 s40, s36, s3\n"
      "s_addc_u32 s41, s37, 0\n"
      "v_mfma_f32_16x16x32_bf16 v[176:179], a[148:151], a[204:207], v[176:179]\n"
      "v_mfma_f32_16x16x32_bf16 v[176:179], a[144:147], a[200:203], v[176:179]\n"
      "ds_read_b128 a[20:23], v15 offset:32768\n"
      "v_mfma_f32_16x16x32_bf16 v[180:183], a[152:155], a[200:203], v[180:183]\n"
      "ds_read_b128 a[24:27], v14 offset:34816\n"
      "v_mfma_f32_16x16x32_bf16 v[180:183], a[156:159], a[204:207], v[180:183]\n"
      "global_load_lds_dwordx4 v3, s[38:39] offset:2048\n"
      "v_mfma_f32_16x16x32_bf16 v[148:151], a[156:159], a[196:199], v[148:151]\n"
      "ds_read_b128 a[28:31], v15 offset:34816\n"
      "v_mfma_f32_16x16x32_bf16 v[148:151], a[152:155], a[192:195], v[148:151]\n"
      "v_mfma_f32_16x16x32_bf16 v[152:155], a[160:163], a[192:195], v[152:155]\n"
      "v_mfma_f32_16x16x32_bf16 v[152:155], a[164:167], a[196:199], v[152:155]\n"
      "ds_read_b64_tr_b16 a[96:97], v8 offset:30720\n"
      "v_mfma_f32_16x16x32_bf16 v[184:187], a[164:167], a[204:207], v[184:187]\n"
      "ds_read_b64_tr_b16 a[98:99], v8 offset:33792\n"
      "v_mfma_f32_16x16x32_bf16 v[184:187], a[160:163], a[200:203], v[184:187]\n"
      "global_load_lds_dwordx4 v4, s[38:39] offset:3072\n"
      "v_mfma_f32_16x16x32_bf16 v[188:191], a[168:171], a[200:203], v[188:191]\n"
      "ds_read_b64_tr_b16 a[104:105], v9 offset:30720\n"
      "s_add_u32 m0, s98, 0\n"
      "v_mfma_f32_16x16x32_bf16 v[188:191], a[172:175], a[204:207], v[188:191]\n"
      "global_load_dword v0, v63, s[46:47] offset:0\n"
      "v_mfma_f32_16x16x32_bf16 v[156:159], a[172:175], a[196:199], v[156:159]\n"
      "v_mfma_f32_16x16x32_bf16 v[156:159], a[168:171], a[192:195], v[156:159]\n"
      "ds_read_b64_tr_b16 a[106:107], v9 offset:33792\n"
      "v_mfma_f32_16x16x32_bf16 v[192:195], a[112:115], a[208:211], v[192:195]\n"
      "ds_read_b64_tr_b16 a[64:65], v8 offset:28672\n"
      "v_mfma_f32_16x16x32_bf16 v[192:195], a[116:119], a[212:215], v[192:195]\n"
      "global_load_lds_dwordx4 v5, s[40:41] offset:0\n"
      "v_mfma_f32_16x16x32_bf16 v[224:227], a[116:119], a[220:223], v[224:227]\n"
      "ds_read_b64_tr_b16 a[66:67], v8 offset:31744\n"
      "v_mfma_f32_16x16x32_bf16 v[224:227], a[112:115], a[216:219], v[224:227]\n"
      "v_mfma_f32_16x16x32_bf16 v[228:231], a[120:123], a[216:219], v[228:231]\n"
      "v_mfma_f32_16x16x32_bf16 v[228:231], a[124:127], a[220:223], v[228:231]\n"
      "ds_read_b64_tr_b16 a[72:73], v9 offset:28672\n"
      "v_mfma_f32_16x16x32_bf16 v[196:199], a[124:127], a[212:215], v[196:199]\n"
      "ds_read_b64_tr_b16 a[74:75], v9 offset:31744\n"
      "v_mfma_f32_16x16x32_bf16 v[196:199], a[120:123], a[208:211], v[196:199]\n"
      "global_load_lds_dwordx4 v6, s[40:41] offset:1024\n"
      "v_mfma_f32_16x16x32_bf16 v[200:203], a[128:131], a[208:211], v[200:203]\n"
      ".Lgm_w3:\n"
      "s_cbranch_execz .Lgm_r0\n"
      "v_mfma_f32_16x16x32_bf16 v[200:203], a[132:135], a[212:215], v[200:203]\n"
      "v_mfma_f32_16x16x32_bf16 v[232:235], a[132:135], a[220:223], v[232:235]\n"
      "v_mfma_f32_16x16x32_bf16 v[232:235], a[128:131], a[216:219], v[232:235]\n"
      "v_mfma_f32_16x16x32_bf16 v[236:239], a[136:139], a[216:219], v[236:239]\n"
      "v_mfma_f32_16x16x32_bf16 v[236:239], a[140:143], a[220:223], v[236:239]\n"
      "global_load_lds_dwordx4 v7, s[40:41] offset:2048\n"
      "v_mfma_f32_16x16x32_bf16 v[204:207], a[140:143], a[212:215], v[204:207]\n"
      "s_add_u32 s38, s38, 0x20000\n"
      "s_addc_u32 s39, s39, 0\n"
      "v_mfma_f32_16x16x32_bf16 v[204:207], a[136:139], a[208:211], v[204:207]\n"
      "s_add_u32 m0, s14, 0x7000\n"
      "s_waitcnt vmcnt(22) lgkmcnt(0)\n"
      "v_mfma_f32_16x16x32_bf16 v[208:211], a[144:147], a[208:211], v[208:211]\n"
      "s_barrier\n"
      "v_mfma_f32_16x16x32_bf16 v[208:211], a[148:151], a[212:215], v[208:211]\n"
      "ds_read_b64_tr_b16 a[84:85], v10 offset:1024\n"
      "v_mfma_f32_16x16x32_bf16 v[240:243], a[148:151], a[220:223], v[240:243]\n"
      "ds_read_b64_tr_b16 a[86:87], v10 offset:4096\n"
      "v_mfma_f32_16x16x32_bf16 v[240:243], a[144:147], a[216:219], v[240:243]\n"
      "global_load_lds_dwordx4 v1, s[38:39] offset:0\n"
      "v_mfma_f32_16x16x32_bf16 v[244:247], a[152:155], a[216:219], v[244:247]\n"
      "ds_read_b64_tr_b16 a[92:93], v11 offset:1024\n"
      "v_mfma_f32_16x16x32_bf16 v[244:247], a[156:159], a[220:223], v[244:247]\n"
      "v_mfma_f32_16x16x32_bf16 v[212:215], a[156:159], a[212:215], v[212:215]\n"
      "v_mfma_f32_16x16x32_bf16 v[212:215], a[152:155], a[208:211], v[212:215]\n"
      "ds_read_b64_tr_b16 a[94:95], v11 offset:4096\n"
      "v_mfma_f32_16x16x32_bf16 v[216:219], a[160:163], a[208:211], v[216:219]\n"
      "ds_read_b128 a[32:35], v16 offset:0\n"
      "v_mfma_f32_16x16x32_bf16 v[216:219], a[164:167], a[212:215], v[216:219]\n"
      "global_load_lds_dwordx4 v2, s[38:39] offset:1024\n"
      "v_mfma_f32_16x16x32_bf16 v[248:251], a[164:167], a[220:223], v[248:251]\n"
      "ds_read_b128 a[36:39], v17 offset:0\n"
      "s_add_u32 s40, s40, 0x30000\n"
      "v_mfma_f32_16x16x32_bf16 v[248:251], a[160:163], a[216:219], v[248:251]\n"
      "s_addc_u32 s41, s41, 0\n"
      "v_mfma_f32_16x16x32_bf16 v[252:255], a[168:171], a[216:219], v[252:255]\n"
      "v_mfma_f32_16x16x32_bf16 v[252:255], a[172:175], a[220:223], v[252:255]\n"
      "ds_read_b128 a[40:43], v16 offset:2048\n"
      "v_mfma_f32_16x16x32_bf16 v[220:223], a[172:175], a[212:215], v[220:223]\n"
      "ds_read_b128 a[44:47], v17 offset:2048\n"
      "v_mfma_f32_16x16x32_bf16 v[220:223], a[168:171], a[208:211], v[220:223]\n"
      "global_load_lds_dwordx4 v3, s[38:39] offset:2048\n"
      "v_mfma_f32_16x16x32_bf16 v[64:67], a[176:179], a[112:115], v[64:67]\n"
      "ds_read_b128 a[48:51], v16 offset:4096\n"
      "v_mfma_f32_16x16x32_bf16 v[64:67], a[180:183], a[116:119], v[64:67]\n"
      "v_mfma_f32_16x16x32_bf16 v[96:99], a[188:191], a[116:119], v[96:99]\n"
      "v_mfma_f32_16x16x32_bf16 v[96:99], a[184:187], a[112:115], v[96:99]\n"
      "ds_read_b128 a[52:55], v17 offset:4096\n"
      "v_mfma_f32_16x16x32_bf16 v[100:103], a[184:187], a[120:123], v[100:103]\n"
      "ds_read_b128 a[56:59], v16 offset:6144\n"
      "v_mfma_f32_16x16x32_bf16 v[100:103], a[188:191], a[124:127], v[100:103]\n"
      "global_load_lds_dwordx4 v4, s[38:39] offset:3072\n"
      "v_mfma_f32_16x16x32_bf16 v[68:71], a[180:183], a[124:127], v[68:71]\n"
      "ds_read_b128 a[60:63], v17 offset:6144\n"
      "s_add_u32 m0, s98, 0x7000\n"
      "v_mfma_f32_16x16x32_bf16 v[68:71], a[176:179], a[120:123], v[68:71]\n"
      "v_mfma_f32_16x16x32_bf16 v[72:75], a[176:179], a[128:131], v[72:75]\n"
      "v_mfma_f32_16x16x32_bf16 v[72:75], a[180:183], a[132:135], v[72:75]\n"
      "ds_read_b64_tr_b16 a[100:101], v10 offset:2048\n"
      "v_mfma_f32_16x16x32_bf16 v[104:107], a[188:191], a[132:135], v[104:107]\n"
      "ds_read_b64_tr_b16 a[102:103], v10 offset:5120\n"
      "v_mfma_f32_16x16x32_bf16 v[104:107], a[184:187], a[128:131], v[104:107]\n"
      "global_load_lds_dwordx4 v5, s[40:41] offset:0\n"
      "v_mfma_f32_16x16x32_bf16 v[108:111], a[184:187], a[136:139], v[108:111]\n"
      "ds_read_b64_tr_b16 a[108:109], v11 offset:2048\n"
      "v_mfma_f32_16x16x32_bf16 v[108:111], a[188:191], a[140:143], v[108:111]\n"
      "v_mfma_f32_16x16x32_bf16 v[76:79], a[180:183], a[140:143], v[76:79]\n"
      "v_mfma_f32_16x16x32_bf16 v[76:79], a[176:179], a[136:139], v[76:79]\n"
      "ds_read_b64_tr_b16 a[110:111], v11 offset:5120\n"
      "v_mfma_f32_16x16x32_bf16 v[80:83], a[176:179], a[144:147], v[80:83]\n"
      "ds_read_b64_tr_b16 a[68:69], v10 offset:0\n"
      "v_mfma_f32_16x16x32_bf16 v[80:83], a[180:183], a[148:151], v[80:83]\n"
      "global_load_lds_dwordx4 v6, s[40:41] offset:1024\n"
      "v_mfma_f32_16x16x32_bf16 v[112:115], a[188:191], a[148:151], v[112:115]\n"
      "ds_read_b64_tr_b16 a[70:71], v10 offset:3072\n"
      "v_mfma_f32_16x16x32_bf16 v[112:115], a[184:187], a[144:147], v[112:115]\n"
      "v_mfma_f32_16x16x32_bf16 v[116:119], a[184:187], a[152:155], v[116:119]\n"
      "v_mfma_f32_16x16x32_bf16 v[116:119], a[188:191], a[156:159], v[116:119]\n"
      "ds_read_b64_tr_b16 a[76:77], v11 offset:0\n"
      "v_mfma_f32_16x16x32_bf16 v[84:87], a[180:183], a[156:159], v[84:87]\n"
      "ds_read_b64_tr_b16 a[78:79], v11 offset:3072\n"
      "v_mfma_f32_16x16x32_bf16 v[84:87], a[176:179], a[152:155], v[84:87]\n"
      "global_load_lds_dwordx4 v7, s[40:41] offset:2048\n"
      "v_mfma_f32_16x16x32_bf16 v[88:91], a[176:179], a[160:163], v[88:91]\n"
      "s_add_u32 s3, s97, 11\n"
      "s_and_b32 s3, s3, 15\n"
      "v_mfma_f32_16x16x32_bf16 v[88:91], a[180:183], a[164:167], v[88:91]\n"
      "s_lshl_b32 s1, s3, 7\n"
      "s_add_u32 s38, s34, s1\n"
      "v_mfma_f32_16x16x32_bf16 v[120:123], a[188:191], a[164:167], v[120:123]\n"
      "s_addc_u32 s39, s35, 0\n"
      "s_add_u32 m0, s14, 0xe000\n"
      "v_mfma_f32_16x16x32_bf16 v[120:123], a[184:187], a[160:163], v[120:123]\n"
      "v_mfma_f32_16x16x32_bf16 v[124:127], a[184:187], a[168:171], v[124:127]\n"
      "v_mfma_f32_16x16x32_bf16 v[124:127], a[188:191], a[172:175], v[124:127]\n"
      "global_load_dwordx4 a[224:227], v24, s[70:71] offset:-4096\n"
      "v_mfma_f32_16x16x32_bf16 v[92:95], a[180:183], a[172:175], v[92:95]\n"
      "v_mfma_f32_16x16x32_bf16 v[92:95], a[176:179], a[168:171], v[92:95]\n"
      "s_waitcnt vmcnt(23) lgkmcnt(0)\n"
      "v_mfma_f32_16x16x32_bf16 v[128:131], a[0:3], a[80:83], v[128:131]\n"
      "s_barrier\n"
      "v_mfma_f32_16x16x32_bf16 v[128:131], a[4:7], a[84:87], v[128:131]\n"
      "ds_read_b64_tr_b16 a[192:193], v10 offset:29696\n"
      "v_mfma_f32_16x16x32_bf16 v[160:163], a[4:7], a[92:95], v[160:163]\n"
      "ds_read_b64_tr_b16 a[194:195], v10 offset:32768\n"
      "v_mfma_f32_16x16x32_bf16 v[160:163], a[0:3], a[88:91], v[160:163]\n"
      "global_load_lds_dwordx4 v1, s[38:39] offset:0\n"
      "v_mfma_f32_16x16x32_bf16 v[164:167], a[8:11], a[88:91], v[164:167]\n"
      "ds_read_b64_tr_b16 a[200:201], v11 offset:29696\n"
      "v_mfma_f32_16x16x32_bf16 v[164:167], a[12:15], a[92:95], v[164:167]\n"
      "v_mfma_f32_16x16x32_bf16 v[132:135], a[12:15], a[84:87], v[132:135]\n"
      "v_mfma_f32_16x16x32_bf16 v[132:135], a[8:11], a[80:83], v[132:135]\n"
      "ds_read_b64_tr_b16 a[202:203], v11 offset:32768\n"
      "v_mfma_f32_16x16x32_bf16 v[136:139], a[16:19], a[80:83], v[136:139]\n"
      "ds_read_b128 a[112:115], v16 offset:28672\n"
      "v_mfma_f32_16x16x32_bf16 v[136:139], a[20:23], a[84:87], v[136:139]\n"
      "global_load_lds_dwordx4 v2, s[38:39] offset:1024\n"
      "v_mfma_f32_16x16x32_bf16 v[168:171], a[20:23], a[92:95], v[168:171]\n"
      "ds_read_b128 a[116:119], v17 offset:28672\n"
      "s_mul_i32 s3, s3, 0x60000\n"
      "v_mfma_f32_16x16x32_bf16 v[168:171], a[16:19], a[88:91], v[168:171]\n"
      "s_add_u32 s40, s36, s3\n"
      "s_addc_u32 s41, s37, 0\n"
      "v_mfma_f32_16x16x32_bf16 v[172:175], a[24:27], a[88:91], v[172:175]\n"
      "v_mfma_f32_16x16x32_bf16 v[172:175], a[28:31], a[92:95], v[172:175]\n"
      "ds_read_b128 a[120:123], v16 offset:30720\n"
      "v_mfma_f32_16x16x32_bf16 v[140:143], a[28:31], a[84:87], v[140:143]\n"
      "ds_read_b128 a[124:127], v17 offset:30720\n"
      "v_mfma_f32_16x16x32_bf16 v[140:143], a[24:27], a[80:83], v[140:143]\n"
      "global_load_lds_dwordx4 v3, s[38:39] offset:2048\n"
      "v_mfma_f32_16x16x32_bf16 v[144:147], a[32:35], a[80:83], v[144:147]\n"
      "ds_read_b128 a[128:131], v16 offset:32768\n"
      "v_mfma_f32_16x16x32_bf16 v[144:147], a[36:39], a[84:87], v[144:147]\n"
      "v_mfma_f32_16x16x32_bf16 v[176:179], a[36:39], a[92:95], v[176:179]\n"
      "v_mfma_f32_16x16x32_bf16 v[176:179], a[32:35], a[88:91], v[176:179]\n"
      "ds_read_b128 a[132:135], v17 offset:32768\n"
      "v_mfma_f32_16x16x32_bf16 v[180:183], a[40:43], a[88:91], v[180:183]\n"
      "ds_read_b128 a[136:139], v16 offset:34816\n"
      "v_mfma_f32_16x16x32_bf16 v[180:183], a[44:47], a[92:95], v[180:183]\n"
      "global_load_lds_dwordx4 v4, s[38:39] offset:3072\n"
      "v_mfma_f32_16x16x32_bf16 v[148:151], a[44:47], a[84:87], v[148:151]\n"
      "ds_read_b128 a[140:143], v17 offset:34816\n"
      "s_add_u32 m0, s98, 0xe000\n"
      "v_mfma_f32_16x16x32_bf16 v[148:151], a[40:43], a[80:83], v[148:151]\n"
      "v_mfma_f32_16x16x32_bf16 v[152:155], a[48:51], a[80:83], v[152:155]\n"
      "v_mfma_f32_16x16x32_bf16 v[152:155], a[52:55], a[84:87], v[152:155]\n"
      "ds_read_b64_tr_b16 a[208:209], v10 offset:30720\n"
      "v_mfma_f32_16x16x32_bf16 v[184:187], a[52:55], a[92:95], v[184:187]\n"
      "ds_read_b64_tr_b16 a[210:211], v10 offset:33792\n"
      "v_mfma_f32_16x16x32_bf16 v[184:187], a[48:51], a[88:91], v[184:187]\n"
      "global_load_lds_dwordx4 v5, s[40:41] offset:0\n"
      "v_mfma_f32_16x16x32_bf16 v[188:191], a[56:59], a[88:91], v[188:191]\n"
      "ds_read_b64_tr_b16 a[216:217], v11 offset:30720\n"
      "v_mfma_f32_16x16x32_bf16 v[188:191], a[60:63], a[92:95], v[188:191]\n"
      "v_mfma_f32_16x16x32_bf16 v[156:159], a[60:63], a[84:87], v[156:159]\n"
      "v_mfma_f32_16x16x32_bf16 v[156:159], a[56:59], a[80:83], v[156:159]\n"
      "ds_read_b64_tr_b16 a[218:219], v11 offset:33792\n"
      "v_mfma_f32_16x16x32_bf16 v[192:195], a[0:3], a[96:99], v[192:195]\n"
      "ds_read_b64_tr_b16 a[176:177], v10 offset:28672\n"
      "v_mfma_f32_16x16x32_bf16 v[192:195], a[4:7], a[100:103], v[192:195]\n"
      "global_load_lds_dwordx4 v6, s[40:41] offset:1024\n"
      "v_mfma_f32_16x16x32_bf16 v[224:227], a[4:7], a[108:111], v[224:227]\n"
      "ds_read_b64_tr_b16 a[178:179], v10 offset:31744\n"
      "v_mfma_f32_16x16x32_bf16 v[224:227], a[0:3], a[104:107], v[224:227]\n"
      "v_mfma_f32_16x16x32_bf16 v[228:231], a[8:11], a[104:107], v[228:231]\n"
      "v_mfma_f32_16x16x32_bf16 v[228:231], a[12:15], a[108:111], v[228:231]\n"
      "ds_read_b64_tr_b16 a[184:185], v11 offset:28672\n"
      "v_mfma_f32_16x16x32_bf16 v[196:199], a[12:15], a[100:103], v[196:199]\n"
      "ds_read_b64_tr_b16 a[186:187], v11 offset:31744\n"
      "v_mfma_f32_16x16x32_bf16 v[196:199], a[8:11], a[96:99], v[196:199]\n"
      "global_load_lds_dwordx4 v7, s[40:41] offset:2048\n"
      "v_mfma_f32_16x16x32_bf16 v[200:203], a[16:19], a[96:99], v[200:203]\n"
      "s_add_u32 s38, s38, 0x20000\n"
      "s_addc_u32 s39, s39, 0\n"
      "v_mfma_f32_16x16x32_bf16 v[200:203], a[20:23], a[100:103], v[200:203]\n"
      "s_add_u32 m0, s14, 0x15000\n"
      "v_mfma_f32_16x16x32_bf16 v[232:235], a[20:23], a[108:111], v[232:235]\n"
      "v_mfma_f32_16x16x32_bf16 v[232:235], a[16:19], a[104:107], v[232:235]\n"
      "v_mfma_f32_16x16x32_bf16 v[236:239], a[24:27], a[104:107], v[236:239]\n"
      "v_mfma_f32_16x16x32_bf16 v[236:239], a[28:31], a[108:111], v[236:239]\n"
      "global_load_dwordx4 a[228:231], v24, s[70:71] offset:-3072\n"
      "v_mfma_f32_16x16x32_bf16 v[204:207], a[28:31], a[100:103], v[204:207]\n"
      "v_mfma_f32_16x16x32_bf16 v[204:207], a[24:27], a[96:99], v[204:207]\n"
      "s_waitcnt vmcnt(24) lgkmcnt(0)\n"
      "v_mfma_f32_16x16x32_bf16 v[208:211], a[32:35], a[96:99], v[208:211]\n"
      "s_barrier\n"
      "v_mfma_f32_16x16x32_bf16 v[208:211], a[36:39], a[100:103], v[208:211]\n"
      "ds_read_b64_tr_b16 a[196:197], v12 offset:1024\n"
      "v_mfma_f32_16x16x32_bf16 v[240:243], a[36:39], a[108:111], v[240:243]\n"
      "ds_read_b64_tr_b16 a[198:199], v12 offset:4096\n"
      "v_mfma_f32_16x16x32_bf16 v[240:243], a[32:35], a[104:107], v[240:243]\n"
      "global_load_lds_dwordx4 v1, s[38:39] offset:0\n"
      "v_mfma_f32_16x16x32_bf16 v[244:247], a[40:43], a[104:107], v[244:247]\n"
      "ds_read_b64_tr_b16 a[204:205], v13 offset:1024\n"
      "v_mfma_f32_16x16x32_bf16 v[244:247], a[44:47], a[108:111], v[244:247]\n"
      "v_mfma_f32_16x16x32_bf16 v[212:215], a[44:47], a[100:103], v[212:215]\n"
      "v_mfma_f32_16x16x32_bf16 v[212:215], a[40:43], a[96:99], v[212:215]\n"
      "ds_read_b64_tr_b16 a[206:207], v13 offset:4096\n"
      "v_mfma_f32_16x16x32_bf16 v[216:219], a[48:51], a[96:99], v[216:219]\n"
      "ds_read_b128 a[144:147], v18 offset:0\n"
      "v_mfma_f32_16x16x32_bf16 v[216:219], a[52:55], a[100:103], v[216:219]\n"
      "global_load_lds_dwordx4 v2, s[38:39] offset:1024\n"
      "v_mfma_f32_16x16x32_bf16 v[248:251], a[52:55], a[108:111], v[248:251]\n"
      "ds_read_b128 a[148:151], v19 offset:0\n"
      "s_add_u32 s40, s40, 0x30000\n"
      "v_mfma_f32_16x16x32_bf16 v[248:251], a[48:51], a[104:107], v[248:251]\n"
      "s_addc_u32 s41, s41, 0\n"
      "v_mfma_f32_16x16x32_bf16 v[252:255], a[56:59], a[104:107], v[252:255]\n"
      "v_mfma_f32_16x16x32_bf16 v[252:255], a[60:63], a[108:111], v[252:255]\n"
      "ds_read_b128 a[152:155], v18 offset:2048\n"
      "v_mfma_f32_16x16x32_bf16 v[220:223], a[60:63], a[100:103], v[220:223]\n"
      "ds_read_b128 a[156:159], v19 offset:2048\n"
      "v_mfma_f32_16x16x32_bf16 v[220:223], a[56:59], a[96:99], v[220:223]\n"
      "global_load_lds_dwordx4 v3, s[38:39] offset:2048\n"
      "v_mfma_f32_16x16x32_bf16 v[64:67], a[64:67], a[0:3], v[64:67]\n"
      "ds_read_b128 a[160:163], v18 offset:4096\n"
      "v_mfma_f32_16x16x32_bf16 v[64:67], a[68:71], a[4:7], v[64:67]\n"
      "v_mfma_f32_16x16x32_bf16 v[96:99], a[76:79], a[4:7], v[96:99]\n"
      "v_mfma_f32_16x16x32_bf16 v[96:99], a[72:75], a[0:3], v[96:99]\n"
      "ds_read_b128 a[164:167], v19 offset:4096\n"
      "v_mfma_f32_16x16x32_bf16 v[100:103], a[72:75], a[8:11], v[100:103]\n"
      "ds_read_b128 a[168:171], v18 offset:6144\n"
      "v_mfma_f32_16x16x32_bf16 v[100:103], a[76:79], a[12:15], v[100:103]\n"
      "global_load_lds_dwordx4 v4, s[38:39] offset:3072\n"
      "v_mfma_f32_16x16x32_bf16 v[68:71], a[68:71], a[12:15], v[68:71]\n"
      "ds_read_b128 a[172:175], v19 offset:6144\n"
      "s_add_u32 m0, s98, 0x15000\n"
      "v_mfma_f32_16x16x32_bf16 v[68:71], a[64:67], a[8:11], v[68:71]\n"
      "v_mfma_f32_16x16x32_bf16 v[72:75], a[64:67], a[16:19], v[72:75]\n"
      "v_mfma_f32_16x16x32_bf16 v[72:75], a[68:71], a[20:23], v[72:75]\n"
      "ds_read_b64_tr_b16 a[212:213], v12 offset:2048\n"
      "v_mfma_f32_16x16x32_bf16 v[104:107], a[76:79], a[20:23], v[104:107]\n"
      "ds_read_b64_tr_b16 a[214:215], v12 offset:5120\n"
      "v_mfma_f32_16x16x32_bf16 v[104:107], a[72:75], a[16:19], v[104:107]\n"
      "global_load_lds_dwordx4 v5, s[40:41] offset:0\n"
      "v_mfma_f32_16x16x32_bf16 v[108:111], a[72:75], a[24:27], v[108:111]\n"
      "ds_read_b64_tr_b16 a[220:221], v13 offset:2048\n"
      "v_mfma_f32_16x16x32_bf16 v[108:111], a[76:79], a[28:31], v[108:111]\n"
      "v_mfma_f32_16x16x32_bf16 v[76:79], a[68:71], a[28:31], v[76:79]\n"
      "v_mfma_f32_16x16x32_bf16 v[76:79], a[64:67], a[24:27], v[76:79]\n"
      "ds_read_b64_tr_b16 a[222:223], v13 offset:5120\n"
      "v_mfma_f32_16x16x32_bf16 v[80:83], a[64:67], a[32:35], v[80:83]\n"
      "ds_read_b64_tr_b16 a[180:181], v12 offset:0\n"
      "v_mfma_f32_16x16x32_bf16 v[80:83], a[68:71], a[36:39], v[80:83]\n"
      "global_load_lds_dwordx4 v6, s[40:41] offset:1024\n"
      "v_mfma_f32_16x16x32_bf16 v[112:115], a[76:79], a[36:39], v[112:115]\n"
      "ds_read_b64_tr_b16 a[182:183], v12 offset:3072\n"
      "v_mfma_f32_16x16x32_bf16 v[112:115], a[72:75], a[32:35], v[112:115]\n"
      "v_mfma_f32_16x16x32_bf16 v[116:119], a[72:75], a[40:43], v[116:119]\n"
      "v_mfma_f32_16x16x32_bf16 v[116:119], a[76:79], a[44:47], v[116:119]\n"
      "ds_read_b64_tr_b16 a[188:189], v13 offset:0\n"
      "v_mfma_f32_16x16x32_bf16 v[84:87], a[68:71], a[44:47], v[84:87]\n"
      "ds_read_b64_tr_b16 a[190:191], v13 offset:3072\n"
      "v_mfma_f32_16x16x32_bf16 v[84:87], a[64:67], a[40:43], v[84:87]\n"
      "global_load_lds_dwordx4 v7, s[40:41] offset:2048\n"
      "v_mfma_f32_16x16x32_bf16 v[88:91], a[64:67], a[48:51], v[88:91]\n"
      "s_add_u32 s3, s97, 12\n"
      "s_and_b32 s3, s3, 15\n"
      "v_mfma_f32_16x16x32_bf16 v[88:91], a[68:71], a[52:55], v[88:91]\n"
      "s_lshl_b32 s1, s3, 7\n"
      "s_add_u32 s38, s34, s1\n"
      "v_mfma_f32_16x16x32_bf16 v[120:123], a[76:79], a[52:55], v[120:123]\n"
      "s_addc_u32 s39, s35, 0\n"
      "s_add_u32 m0, s14, 0x1c000\n"
      "v_mfma_f32_16x16x32_bf16 v[120:123], a[72:75], a[48:51], v[120:123]\n"
      "v_mfma_f32_16x16x32_bf16 v[124:127], a[72:75], a[56:59], v[124:127]\n"
      "v_mfma_f32_16x16x32_bf16 v[124:127], a[76:79], a[60:63], v[124:127]\n"
      "global_load_dwordx4 a[232:235], v24, s[70:71] offset:-2048\n"
      "v_mfma_f32_16x16x32_bf16 v[92:95], a[68:71], a[60:63], v[92:95]\n"
      "v_mfma_f32_16x16x32_bf16 v[92:95], a[64:67], a[56:59], v[92:95]\n"
      "s_waitcnt vmcnt(24) lgkmcnt(0)\n"
      "v_mfma_f32_16x16x32_bf16 v[128:131], a[112:115], a[192:195], v[128:131]\n"
      "s_barrier\n"
      "v_mfma_f32_16x16x32_bf16 v[128:131], a[116:119], a[196:199], v[128:131]\n"
      "ds_read_b64_tr_b16 a[80:81], v8 offset:1024\n"
      "v_mfma_f32_16x16x32_bf16 v[160:163], a[116:119], a[204:207], v[160:163]\n"
      "ds_read_b64_tr_b16 a[82:83], v8 offset:4096\n"
      "v_mfma_f32_16x16x32_bf16 v[160:163], a[112:115], a[200:203], v[160:163]\n"
      "global_load_lds_dwordx4 v1, s[38:39] offset:0\n"
      "v_mfma_f32_16x16x32_bf16 v[164:167], a[120:123], a[200:203], v[164:167]\n"
      "ds_read_b64_tr_b16 a[88:89], v9 offset:1024\n"
      "v_mfma_f32_16x16x32_bf16 v[164:167], a[124:127], a[204:207], v[164:167]\n"
      "v_mfma_f32_16x16x32_bf16 v[132:135], a[124:127], a[196:199], v[132:135]\n"
      "v_mfma_f32_16x16x32_bf16 v[132:135], a[120:123], a[192:195], v[132:135]\n"
      "ds_read_b64_tr_b16 a[90:91], v9 offset:4096\n"
      "v_mfma_f32_16x16x32_bf16 v[136:139], a[128:131], a[192:195], v[136:139]\n"
      "ds_read_b128 a[0:3], v14 offset:0\n"
      "v_mfma_f32_16x16x32_bf16 v[136:139], a[132:135], a[196:199], v[136:139]\n"
      "global_load_lds_dwordx4 v2, s[38:39] offset:1024\n"
      "v_mfma_f32_16x16x32_bf16 v[168:171], a[132:135], a[204:207], v[168:171]\n"
      "ds_read_b128 a[4:7], v15 offset:0\n"
      "s_mul_i32 s3, s3, 0x60000\n"
      "v_mfma_f32_16x16x32_bf16 v[168:171], a[128:131], a[200:203], v[168:171]\n"
      "s_add_u32 s40, s36, s3\n"
      "s_addc_u32 s41, s37, 0\n"
      "v_mfma_f32_16x16x32_bf16 v[172:175], a[136:139], a[200:203], v[172:175]\n"
      "v_mfma_f32_16x16x32_bf16 v[172:175], a[140:143], a[204:207], v[172:175]\n"
      "ds_read_b128 a[8:11], v14 offset:2048\n"
      "v_mfma_f32_16x16x32_bf16 v[140:143], a[140:143], a[196:199], v[140:143]\n"
      "ds_read_b128 a[12:15], v15 offset:2048\n"
      "v_mfma_f32_16x16x32_bf16 v[140:143], a[136:139], a[192:195], v[140:143]\n"
      "global_load_lds_dwordx4 v3, s[38:39] offset:2048\n"
      "v_mfma_f32_16x16x32_bf16 v[144:147], a[144:147], a[192:195], v[144:147]\n"
      "ds_read_b128 a[16:19], v14 offset:4096\n"
      "v_mfma_f32_16x16x32_bf16 v[144:147], a[148:151], a[196:199], v[144:147]\n"
      "v_mfma_f32_16x16x32_bf16 v[176:179], a[148:151], a[204:207], v[176:179]\n"
      "v_mfma_f32_16x16x32_bf16 v[176:179], a[144:147], a[200:203], v[176:179]\n"
      "ds_read_b128 a[20:23], v15 offset:4096\n"
      "v_mfma_f32_16x16x32_bf16 v[180:183], a[152:155], a[200:203], v[180:183]\n"
      "ds_read_b128 a[24:27], v14 offset:6144\n"
      "v_mfma_f32_16x16x32_bf16 v[180:183], a[156:159], a[204:207], v[180:183]\n"
      "global_load_lds_dwordx4 v4, s[38:39] offset:3072\n"
      "v_mfma_f32_16x16x32_bf16 v[148:151], a[156:159], a[196:199], v[148:151]\n"
      "ds_read_b128 a[28:31], v15 offset:6144\n"
      "s_add_u32 m0, s98, 0x1c000\n"
      "v_mfma_f32_16x16x32_bf16 v[148:151], a[152:155], a[192:195], v[148:151]\n"
      "v_mfma_f32_16x16x32_bf16 v[152:155], a[160:163], a[192:195], v[152:155]\n"
      "v_mfma_f32_16x16x32_bf16 v[152:155], a[164:167], a[196:199], v[152:155]\n"
      "ds_read_b64_tr_b16 a[96:97], v8 offset:2048\n"
      "v_mfma_f32_16x16x32_bf16 v[184:187], a[164:167], a[204:207], v[184:187]\n"
      "ds_read_b64_tr_b16 a[98:99], v8 offset:5120\n"
      "v_mfma_f32_16x16x32_bf16 v[184:187], a[160:163], a[200:203], v[184:187]\n"
      "global_load_lds_dwordx4 v5, s[40:41] offset:0\n"
      "v_mfma_f32_16x16x32_bf16 v[188:191], a[168:171], a[200:203], v[188:191]\n"
      "ds_read_b64_tr_b16 a[104:105], v9 offset:2048\n"
      "v_mfma_f32_16x16x32_bf16 v[188:191], a[172:175], a[204:207], v[188:191]\n"
      "v_mfma_f32_16x16x32_bf16 v[156:159], a[172:175], a[196:199], v[156:159]\n"
      "v_mfma_f32_16x16x32_bf16 v[156:159], a[168:171], a[192:195], v[156:159]\n"
      "ds_read_b64_tr_b16 a[106:107], v9 offset:5120\n"
      "v_mfma_f32_16x16x32_bf16 v[192:195], a[112:115], a[208:211], v[192:195]\n"
      "ds_read_b64_tr_b16 a[64:65], v8 offset:0\n"
      "v_mfma_f32_16x16x32_bf16 v[192:195], a[116:119], a[212:215], v[192:195]\n"
      "global_load_lds_dwordx4 v6, s[40:41] offset:1024\n"
      "v_mfma_f32_16x16x32_bf16 v[224:227], a[116:119], a[220:223], v[224:227]\n"
      "ds_read_b64_tr_b16 a[66:67], v8 offset:3072\n"
      "v_mfma_f32_16x16x32_bf16 v[224:227], a[112:115], a[216:219], v[224:227]\n"
      "v_mfma_f32_16x16x32_bf16 v[228:231], a[120:123], a[216:219], v[228:231]\n"
      "global_load_dword v0, v63, s[46:47] offset:2048\n"
      "v_mfma_f32_16x16x32_bf16 v[228:231], a[124:127], a[220:223], v[228:231]\n"
      "ds_read_b64_tr_b16 a[72:73], v9 offset:0\n"
      "v_mfma_f32_16x16x32_bf16 v[196:199], a[124:127], a[212:215], v[196:199]\n"
      "ds_read_b64_tr_b16 a[74:75], v9 offset:3072\n"
      "v_mfma_f32_16x16x32_bf16 v[196:199], a[120:123], a[208:211], v[196:199]\n"
      "global_load_lds_dwordx4 v7, s[40:41] offset:2048\n"
      "v_mfma_f32_16x16x32_bf16 v[200:203], a[128:131], a[208:211], v[200:203]\n"
      "s_add_u32 s38, s38, 0x20000\n"
      "s_addc_u32 s39, s39, 0\n"
      "v_mfma_f32_16x16x32_bf16 v[200:203], a[132:135], a[212:215], v[200:203]\n"
      "s_add_u32 m0, s14, 0\n"
      "v_mfma_f32_16x16x32_bf16 v[232:235], a[132:135], a[220:223], v[232:235]\n"
      "v_mfma_f32_16x16x32_bf16 v[232:235], a[128:131], a[216:219], v[232:235]\n"
      "v_mfma_f32_16x16x32_bf16 v[236:239], a[136:139], a[216:219], v[236:239]\n"
      "v_mfma_f32_16x16x32_bf16 v[236:239], a[140:143], a[220:223], v[236:239]\n"
      "global_load_dwordx4 a[236:239], v24, s[70:71] offset:-1024\n"
      "v_mfma_f32_16x16x32_bf16 v[204:207], a[140:143], a[212:215], v[204:207]\n"
      "v_mfma_f32_16x16x32_bf16 v[204:207], a[136:139], a[208:211], v[204:207]\n"
      "s_waitcnt vmcnt(26) lgkmcnt(0)\n"
      "v_mfma_f32_16x16x32_bf16 v[208:211], a[144:147], a[208:211], v[208:211]\n"
      "s_barrier\n"
      "v_mfma_f32_16x16x32_bf16 v[208:211], a[148:151], a[212:215], v[208:211]\n"
      "ds_read_b64_tr_b16 a[84:85], v8 offset:29696\n"
      "v_mfma_f32_16x16x32_bf16 v[240:243], a[148:151], a[220:223], v[240:243]\n"
      "ds_read_b64_tr_b16 a[86:87], v8 offset:32768\n"
      "v_mfma_f32_16x16x32_bf16 v[240:243], a[144:147], a[216:219], v[240:243]\n"
      "global_load_lds_dwordx4 v1, s[38:39] offset:0\n"
      "v_mfma_f32_16x16x32_bf16 v[244:247], a[152:155], a[216:219], v[244:247]\n"
      "ds_read_b64_tr_b16 a[92:93], v9 offset:29696\n"
      "v_mfma_f32_16x16x32_bf16 v[244:247], a[156:159], a[220:223], v[244:247]\n"
      "v_mfma_f32_16x16x32_bf16 v[212:215], a[156:159], a[212:215], v[212:215]\n"
      "v_mfma_f32_16x16x32_bf16 v[212:215], a[152:155], a[208:211], v[212:215]\n"
      "ds_read_b64_tr_b16 a[94:95], v9 offset:32768\n"
      "v_mfma_f32_16x16x32_bf16 v[216:219], a[160:163], a[208:211], v[216:219]\n"
      "ds_read_b128 a[32:35], v14 offset:28672\n"
      "v_mfma_f32_16x16x32_bf16 v[216:219], a[164:167], a[212:215], v[216:219]\n"
      "global_load_lds_dwordx4 v2, s[38:39] offset:1024\n"
      "v_mfma_f32_16x16x32_bf16 v[248:251], a[164:167], a[220:223], v[248:251]\n"
      "ds_read_b128 a[36:39], v15 offset:28672\n"
      "s_add_u32 s40, s40, 0x30000\n"
      "v_mfma_f32_16x16x32_bf16 v[248:251], a[160:163], a[216:219], v[248:251]\n"
      "s_addc_u32 s41, s41, 0\n"
      "v_mfma_f32_16x16x32_bf16 v[252:255], a[168:171], a[216:219], v[252:255]\n"
      "v_mfma_f32_16x16x32_bf16 v[252:255], a[172:175], a[220:223], v[252:255]\n"
      "ds_read_b128 a[40:43], v14 offset:30720\n"
      "v_mfma_f32_16x16x32_bf16 v[220:223], a[172:175], a[212:215], v[220:223]\n"
      "ds_read_b128 a[44:47], v15 offset:30720\n"
      "v_mfma_f32_16x16x32_bf16 v[220:223], a[168:171], a[208:211], v[220:223]\n"
      "global_load_lds_dwordx4 v3, s[38:39] offset:2048\n"
      "v_mfma_f32_16x16x32_bf16 v[64:67], a[176:179], a[112:115], v[64:67]\n"
      "ds_read_b128 a[48:51], v14 offset:32768\n"
      "v_mfma_f32_16x16x32_bf16 v[64:67], a[180:183], a[116:119], v[64:67]\n"
      "v_mfma_f32_16x16x32_bf16 v[96:99], a[188:191], a[116:119], v[96:99]\n"
      "v_mfma_f32_16x16x32_bf16 v[96:99], a[184:187], a[112:115], v[96:99]\n"
      "ds_read_b128 a[52:55], v15 offset:32768\n"
      "v_mfma_f32_16x16x32_bf16 v[100:103], a[184:187], a[120:123], v[100:103]\n"
      "ds_read_b128 a[56:59], v14 offset:34816\n"
      "v_mfma_f32_16x16x32_bf16 v[100:103], a[188:191], a[124:127], v[100:103]\n"
      "global_load_lds_dwordx4 v4, s[38:39] offset:3072\n"
      "v_mfma_f32_16x16x32_bf16 v[68:71], a[180:183], a[124:127], v[68:71]\n"
      "ds_read_b128 a[60:63], v15 offset:34816\n"
      "s_add_u32 m0, s98, 0\n"
      "v_mfma_f32_16x16x32_bf16 v[68:71], a[176:179], a[120:123], v[68:71]\n"
      "v_mfma_f32_16x16x32_bf16 v[72:75], a[176:179], a[128:131], v[72:75]\n"
      "v_mfma_f32_16x16x32_bf16 v[72:75], a[180:183], a[132:135], v[72:75]\n"
      "ds_read_b64_tr_b16 a[100:101], v8 offset:30720\n"
      "v_mfma_f32_16x16x32_bf16 v[104:107], a[188:191], a[132:135], v[104:107]\n"
      "ds_read_b64_tr_b16 a[102:103], v8 offset:33792\n"
      "v_mfma_f32_16x16x32_bf16 v[104:107], a[184:187], a[128:131], v[104:107]\n"
      "global_load_lds_dwordx4 v5, s[40:41] offset:0\n"
      "v_mfma_f32_16x16x32_bf16 v[108:111], a[184:187], a[136:139], v[108:111]\n"
      "ds_read_b64_tr_b16 a[108:109], v9 offset:30720\n"
      "v_mfma_f32_16x16x32_bf16 v[108:111], a[188:191], a[140:143], v[108:111]\n"
      "v_mfma_f32_16x16x32_bf16 v[76:79], a[180:183], a[140:143], v[76:79]\n"
      "v_mfma_f32_16x16x32_bf16 v[76:79], a[176:179], a[136:139], v[76:79]\n"
      "ds_read_b64_tr_b16 a[110:111], v9 offset:33792\n"
      "v_mfma_f32_16x16x32_bf16 v[80:83], a[176:179], a[144:147], v[80:83]\n"
      "ds_read_b64_tr_b16 a[68:69], v8 offset:28672\n"
      "v_mfma_f32_16x16x32_bf16 v[80:83], a[180:183], a[148:151], v[80:83]\n"
      "global_load_lds_dwordx4 v6, s[40:41] offset:1024\n"
      "v_mfma_f32_16x16x32_bf16 v[112:115], a[188:191], a[148:151], v[112:115]\n"
      "ds_read_b64_tr_b16 a[70:71], v8 offset:31744\n"
      "v_mfma_f32_16x16x32_bf16 v[112:115], a[184:187], a[144:147], v[112:115]\n"
      "v_mfma_f32_16x16x32_bf16 v[116:119], a[184:187], a[152:155], v[116:119]\n"
      "v_mfma_f32_16x16x32_bf16 v[116:119], a[188:191], a[156:159], v[116:119]\n"
      "ds_read_b64_tr_b16 a[76:77], v9 offset:28672\n"
      "v_mfma_f32_16x16x32_bf16 v[84:87], a[180:183], a[156:159], v[84:87]\n"
      "ds_read_b64_tr_b16 a[78:79], v9 offset:31744\n"
      "v_mfma_f32_16x16x32_bf16 v[84:87], a[176:179], a[152:155], v[84:87]\n"
      "global_load_lds_dwordx4 v7, s[40:41] offset:2048\n"
      "v_mfma_f32_16x16x32_bf16 v[88:91], a[176:179], a[160:163], v[88:91]\n"
      "s_add_u32 s3, s97, 13\n"
      "s_and_b32 s3, s3, 15\n"
      "v_mfma_f32_16x16x32_bf16 v[88:91], a[180:183], a[164:167], v[88:91]\n"
      "s_lshl_b32 s1, s3, 7\n"
      "s_add_u32 s38, s34, s1\n"
      "v_mfma_f32_16x16x32_bf16 v[120:123], a[188:191], a[164:167], v[120:123]\n"
      "s_addc_u32 s39, s35, 0\n"
      "s_add_u32 m0, s14, 0x7000\n"
      "v_mfma_f32_16x16x32_bf16 v[120:123], a[184:187], a[160:163], v[120:123]\n"
      "global_load_dword v0, v27, s[60:61] offset:0 sc1\n"
      "v_mfma_f32_16x16x32_bf16 v[124:127], a[184:187], a[168:171], v[124:127]\n"
      "global_load_dword v0, v28, s[58:59] offset:0 sc1\n"
      "v_mfma_f32_16x16x32_bf16 v[124:127], a[188:191], a[172:175], v[124:127]\n"
      "global_load_dwordx4 a[240:243], v24, s[70:71] offset:0\n"
      "v_mfma_f32_16x16x32_bf16 v[92:95], a[180:183], a[172:175], v[92:95]\n"
      "global_load_dword v0, v42, s[68:69] offset:0 sc1\n"
      "v_mfma_f32_16x16x32_bf16 v[92:95], a[176:179], a[168:171], v[92:95]\n"
      "s_waitcnt vmcnt(29) lgkmcnt(0)\n"
      "v_mfma_f32_16x16x32_bf16 v[128:131], a[0:3], a[80:83], v[128:131]\n"
      "s_barrier\n"
      "v_mfma_f32_16x16x32_bf16 v[128:131], a[4:7], a[84:87], v[128:131]\n"
      "ds_read_b64_tr_b16 a[192:193], v10 offset:1024\n"
      "v_mfma_f32_16x16x32_bf16 v[160:163], a[4:7], a[92:95], v[160:163]\n"
      "ds_read_b64_tr_b16 a[194:195], v10 offset:4096\n"
      "v_mfma_f32_16x16x32_bf16 v[160:163], a[0:3], a[88:91], v[160:163]\n"
      "global_load_lds_dwordx4 v1, s[38:39] offset:0\n"
      "v_mfma_f32_16x16x32_bf16 v[164:167], a[8:11], a[88:91], v[164:167]\n"
      "ds_read_b64_tr_b16 a[200:201], v11 offset:1024\n"
      "v_mfma_f32_16x16x32_bf16 v[164:167], a[12:15], a[92:95], v[164:167]\n"
      "global_load_dword v0, v27, s[52:53] offset:0 sc1\n"
      "v_mfma_f32_16x16x32_bf16 v[132:135], a[12:15], a[84:87], v[132:135]\n"
      "global_load_dword v0, v38, s[56:57] offset:0 sc1\n"
      "v_mfma_f32_16x16x32_bf16 v[132:135], a[8:11], a[80:83], v[132:135]\n"
      "ds_read_b64_tr_b16 a[202:203], v11 offset:4096\n"
      "v_mfma_f32_16x16x32_bf16 v[136:139], a[16:19], a[80:83], v[136:139]\n"
      "ds_read_b128 a[112:115], v16 offset:0\n"
      "v_mfma_f32_16x16x32_bf16 v[136:139], a[20:23], a[84:87], v[136:139]\n"
      "global_load_lds_dwordx4 v2, s[38:39] offset:1024\n"
      "v_mfma_f32_16x16x32_bf16 v[168:171], a[20:23], a[92:95], v[168:171]\n"
      "ds_read_b128 a[116:119], v17 offset:0\n"
      "s_mul_i32 s3, s3, 0x60000\n"
      "v_mfma_f32_16x16x32_bf16 v[168:171], a[16:19], a[88:91], v[168:171]\n"
      "s_add_u32 s40, s36, s3\n"
      "s_addc_u32 s41, s37, 0\n"
      "v_mfma_f32_16x16x32_bf16 v[172:175], a[24:27], a[88:91], v[172:175]\n"
      "s_cmp_eq_u32 s5, 0\n"
      "s_cselect_b64 s[48:49], s[20:21], s[24:25]\n"
      "v_mfma_f32_16x16x32_bf16 v[172:175], a[28:31], a[92:95], v[172:175]\n"
      "ds_read_b128 a[120:123], v16 offset:2048\n"
      "s_lshr_b32 s90, s33, 3\n"
      "v_mfma_f32_16x16x32_bf16 v[140:143], a[28:31], a[84:87], v[140:143]\n"
      "ds_read_b128 a[124:127], v17 offset:2048\n"
      "s_lshl1_add_u32 s90, s90, s6\n"
      "v_mfma_f32_16x16x32_bf16 v[140:143], a[24:27], a[80:83], v[140:143]\n"
      "global_load_lds_dwordx4 v3, s[38:39] offset:2048\n"
      "v_mfma_f32_16x16x32_bf16 v[144:147], a[32:35], a[80:83], v[144:147]\n"
      "ds_read_b128 a[128:131], v16 offset:4096\n"
      "s_lshl_b32 s90, s90, 15\n"
      "v_mfma_f32_16x16x32_bf16 v[144:147], a[36:39], a[84:87], v[144:147]\n"
      "s_and_b32 s91, s33, 1\n"
      "s_lshl_b32 s91, s91, 10\n"
      "v_mfma_f32_16x16x32_bf16 v[176:179], a[36:39], a[92:95], v[176:179]\n"
      "s_add_u32 s90, s90, s91\n"
      "s_lshr_b32 s91, s48, 7\n"
      "v_mfma_f32_16x16x32_bf16 v[176:179], a[32:35], a[88:91], v[176:179]\n"
      "ds_read_b128 a[132:135], v17 offset:4096\n"
      "s_sub_u32 s91, 3, s91\n"
      "v_mfma_f32_16x16x32_bf16 v[180:183], a[40:43], a[88:91], v[180:183]\n"
      "ds_read_b128 a[136:139], v16 offset:6144\n"
      "s_and_b32 s91, s91, 7\n"
      "v_mfma_f32_16x16x32_bf16 v[180:183], a[44:47], a[92:95], v[180:183]\n"
      "global_load_lds_dwordx4 v4, s[38:39] offset:3072\n"
      "v_mfma_f32_16x16x32_bf16 v[148:151], a[44:47], a[84:87], v[148:151]\n"
      "ds_read_b128 a[140:143], v17 offset:6144\n"
      "s_add_u32 m0, s98, 0x7000\n"
      "v_mfma_f32_16x16x32_bf16 v[148:151], a[40:43], a[80:83], v[148:151]\n"
      "s_lshl_b32 s91, s91, 7\n"
      "s_add_u32 s90, s90, s91\n"
      "v_mfma_f32_16x16x32_bf16 v[152:155], a[48:51], a[80:83], v[152:155]\n"
      "s_add_u32 s48, s48, s90\n"
      "s_addc_u32 s49, s49, 0\n"
      "v_and_b32 v43, 15, v31\n"
      "v_mfma_f32_16x16x32_bf16 v[152:155], a[52:55], a[84:87], v[152:155]\n"
      "ds_read_b64_tr_b16 a[208:209], v10 offset:2048\n"
      "v_lshlrev_b32 v43, 11, v43\n"
      "v_mfma_f32_16x16x32_bf16 v[184:187], a[52:55], a[92:95], v[184:187]\n"
      "ds_read_b64_tr_b16 a[210:211], v10 offset:5120\n"
      "v_mfma_f32_16x16x32_bf16 v[184:187], a[48:51], a[88:91], v[184:187]\n"
      "global_load_lds_dwordx4 v5, s[40:41] offset:0\n"
      "v_mfma_f32_16x16x32_bf16 v[188:191], a[56:59], a[88:91], v[188:191]\n"
      "ds_read_b64_tr_b16 a[216:217], v11 offset:2048\n"
      "v_mfma_f32_16x16x32_bf16 v[188:191], a[60:63], a[92:95], v[188:191]\n"
      "v_mfma_f32_16x16x32_bf16 v[156:159], a[60:63], a[84:87], v[156:159]\n"
      "v_mfma_f32_16x16x32_bf16 v[156:159], a[56:59], a[80:83], v[156:159]\n"
      "ds_read_b64_tr_b16 a[218:219], v11 offset:5120\n"
      "v_mfma_f32_16x16x32_bf16 v[192:195], a[0:3], a[96:99], v[192:195]\n"
      "ds_read_b64_tr_b16 a[176:177], v10 offset:0\n"
      "v_mfma_f32_16x16x32_bf16 v[192:195], a[4:7], a[100:103], v[192:195]\n"
      "global_load_lds_dwordx4 v6, s[40:41] offset:1024\n"
      "v_mfma_f32_16x16x32_bf16 v[224:227], a[4:7], a[108:111], v[224:227]\n"
      "ds_read_b64_tr_b16 a[178:179], v10 offset:3072\n"
      "v_mfma_f32_16x16x32_bf16 v[224:227], a[0:3], a[104:107], v[224:227]\n"
      "v_mfma_f32_16x16x32_bf16 v[228:231], a[8:11], a[104:107], v[228:231]\n"
      "v_mfma_f32_16x16x32_bf16 v[228:231], a[12:15], a[108:111], v[228:231]\n"
      "ds_read_b64_tr_b16 a[184:185], v11 offset:0\n"
      "v_mfma_f32_16x16x32_bf16 v[196:199], a[12:15], a[100:103], v[196:199]\n"
      "ds_read_b64_tr_b16 a[186:187], v11 offset:3072\n"
      "v_mfma_f32_16x16x32_bf16 v[196:199], a[8:11], a[96:99], v[196:199]\n"
      "global_load_lds_dwordx4 v7, s[40:41] offset:2048\n"
      "v_mfma_f32_16x16x32_bf16 v[200:203], a[16:19], a[96:99], v[200:203]\n"
      "s_add_u32 s38, s38, 0x20000\n"
      "s_addc_u32 s39, s39, 0\n"
      ".Lgm_w4:\n"
      "s_cbranch_execz .Lgm_r0\n"
      "v_mfma_f32_16x16x32_bf16 v[200:203], a[20:23], a[100:103], v[200:203]\n"
      "s_add_u32 m0, s14, 0xe000\n"
      "v_mfma_f32_16x16x32_bf16 v[232:235], a[20:23], a[108:111], v[232:235]\n"
      "v_mfma_f32_16x16x32_bf16 v[232:235], a[16:19], a[104:107], v[232:235]\n"
      "v_mfma_f32_16x16x32_bf16 v[236:239], a[24:27], a[104:107], v[236:239]\n"
      "v_mfma_f32_16x16x32_bf16 v[236:239], a[28:31], a[108:111], v[236:239]\n"
      "global_load_dwordx4 a[244:247], v24, s[70:71] offset:1024\n"
      "v_mfma_f32_16x16x32_bf16 v[204:207], a[28:31], a[100:103], v[204:207]\n"
      "v_mfma_f32_16x16x32_bf16 v[204:207], a[24:27], a[96:99], v[204:207]\n"
      "s_waitcnt vmcnt(31) lgkmcnt(0)\n"
      "v_mfma_f32_16x16x32_bf16 v[208:211], a[32:35], a[96:99], v[208:211]\n"
      "s_barrier\n"
      "v_mfma_f32_16x16x32_bf16 v[208:211], a[36:39], a[100:103], v[208:211]\n"
      "ds_read_b64_tr_b16 a[196:197], v10 offset:29696\n"
      "v_mfma_f32_16x16x32_bf16 v[240:243], a[36:39], a[108:111], v[240:243]\n"
      "ds_read_b64_tr_b16 a[198:199], v10 offset:32768\n"
      "v_mfma_f32_16x16x32_bf16 v[240:243], a[32:35], a[104:107], v[240:243]\n"
      "global_load_lds_dwordx4 v1, s[38:39] offset:0\n"
      "v_mfma_f32_16x16x32_bf16 v[244:247], a[40:43], a[104:107], v[244:247]\n"
      "ds_read_b64_tr_b16 a[204:205], v11 offset:29696\n"
      "v_mfma_f32_16x16x32_bf16 v[244:247], a[44:47], a[108:111], v[244:247]\n"
      "v_mfma_f32_16x16x32_bf16 v[212:215], a[44:47], a[100:103], v[212:215]\n"
      "v_mfma_f32_16x16x32_bf16 v[212:215], a[40:43], a[96:99], v[212:215]\n"
      "ds_read_b64_tr_b16 a[206:207], v11 offset:32768\n"
      "v_mfma_f32_16x16x32_bf16 v[216:219], a[48:51], a[96:99], v[216:219]\n"
      "ds_read_b128 a[144:147], v16 offset:28672\n"
      "v_mfma_f32_16x16x32_bf16 v[216:219], a[52:55], a[100:103], v[216:219]\n"
      "global_load_lds_dwordx4 v2, s[38:39] offset:1024\n"
      "v_mfma_f32_16x16x32_bf16 v[248:251], a[52:55], a[108:111], v[248:251]\n"
      "ds_read_b128 a[148:151], v17 offset:28672\n"
      "s_add_u32 s40, s40, 0x30000\n"
      "v_mfma_f32_16x16x32_bf16 v[248:251], a[48:51], a[104:107], v[248:251]\n"
      "s_addc_u32 s41, s41, 0\n"
      "v_mfma_f32_16x16x32_bf16 v[252:255], a[56:59], a[104:107], v[252:255]\n"
      "v_mfma_f32_16x16x32_bf16 v[252:255], a[60:63], a[108:111], v[252:255]\n"
      "ds_read_b128 a[152:155], v16 offset:30720\n"
      "v_mfma_f32_16x16x32_bf16 v[220:223], a[60:63], a[100:103], v[220:223]\n"
      "ds_read_b128 a[156:159], v17 offset:30720\n"
      "v_mfma_f32_16x16x32_bf16 v[220:223], a[56:59], a[96:99], v[220:223]\n"
      "global_load_lds_dwordx4 v3, s[38:39] offset:2048\n"
      "v_mfma_f32_16x16x32_bf16 v[64:67], a[64:67], a[0:3], v[64:67]\n"
      "ds_read_b128 a[160:163], v16 offset:32768\n"
      "v_mfma_f32_16x16x32_bf16 v[64:67], a[68:71], a[4:7], v[64:67]\n"
      "v_mfma_f32_16x16x32_bf16 v[96:99], a[76:79], a[4:7], v[96:99]\n"
      "v_mfma_f32_16x16x32_bf16 v[96:99], a[72:75], a[0:3], v[96:99]\n"
      "ds_read_b128 a[164:167], v17 offset:32768\n"
      "v_mfma_f32_16x16x32_bf16 v[100:103], a[72:75], a[8:11], v[100:103]\n"
      "ds_read_b128 a[168:171], v16 offset:34816\n"
      "v_mfma_f32_16x16x32_bf16 v[100:103], a[76:79], a[12:15], v[100:103]\n"
      "global_load_lds_dwordx4 v4, s[38:39] offset:3072\n"
      "v_mfma_f32_16x16x32_bf16 v[68:71], a[68:71], a[12:15], v[68:71]\n"
      "ds_read_b128 a[172:175], v17 offset:34816\n"
      "s_add_u32 m0, s98, 0xe000\n"
      "v_mfma_f32_16x16x32_bf16 v[68:71], a[64:67], a[8:11], v[68:71]\n"
      "v_mfma_f32_16x16x32_bf16 v[72:75], a[64:67], a[16:19], v[72:75]\n"
      "v_mfma_f32_16x16x32_bf16 v[72:75], a[68:71], a[20:23], v[72:75]\n"
      "ds_read_b64_tr_b16 a[212:213], v10 offset:30720\n"
      "v_mfma_f32_16x16x32_bf16 v[104:107], a[76:79], a[20:23], v[104:107]\n"
      "ds_read_b64_tr_b16 a[214:215], v10 offset:33792\n"
      "v_mfma_f32_16x16x32_bf16 v[104:107], a[72:75], a[16:19], v[104:107]\n"
      "global_load_lds_dwordx4 v5, s[40:41] offset:0\n"
      "v_mfma_f32_16x16x32_bf16 v[108:111], a[72:75], a[24:27], v[108:111]\n"
      "ds_read_b64_tr_b16 a[220:221], v11 offset:30720\n"
      "v_mfma_f32_16x16x32_bf16 v[108:111], a[76:79], a[28:31], v[108:111]\n"
      "v_mfma_f32_16x16x32_bf16 v[76:79], a[68:71], a[28:31], v[76:79]\n"
      "v_mfma_f32_16x16x32_bf16 v[76:79], a[64:67], a[24:27], v[76:79]\n"
      "ds_read_b64_tr_b16 a[222:223], v11 offset:33792\n"
      "v_mfma_f32_16x16x32_bf16 v[80:83], a[64:67], a[32:35], v[80:83]\n"
      "ds_read_b64_tr_b16 a[180:181], v10 offset:28672\n"
      "v_mfma_f32_16x16x32_bf16 v[80:83], a[68:71], a[36:39], v[80:83]\n"
      "global_load_lds_dwordx4 v6, s[40:41] offset:1024\n"
      "v_mfma_f32_16x16x32_bf16 v[112:115], a[76:79], a[36:39], v[112:115]\n"
      "ds_read_b64_tr_b16 a[182:183], v10 offset:31744\n"
      "v_mfma_f32_16x16x32_bf16 v[112:115], a[72:75], a[32:35], v[112:115]\n"
      "v_mfma_f32_16x16x32_bf16 v[116:119], a[72:75], a[40:43], v[116:119]\n"
      "v_mfma_f32_16x16x32_bf16 v[116:119], a[76:79], a[44:47], v[116:119]\n"
      "ds_read_b64_tr_b16 a[188:189], v11 offset:28672\n"
      "v_mfma_f32_16x16x32_bf16 v[84:87], a[68:71], a[44:47], v[84:87]\n"
      "ds_read_b64_tr_b16 a[190:191], v11 offset:31744\n"
      "v_mfma_f32_16x16x32_bf16 v[84:87], a[64:67], a[40:43], v[84:87]\n"
      "global_load_lds_dwordx4 v7, s[40:41] offset:2048\n"
      "v_mfma_f32_16x16x32_bf16 v[88:91], a[64:67], a[48:51], v[88:91]\n"
      "s_add_u32 s3, s97, 14\n"
      "s_and_b32 s3, s3, 15\n"
      "v_mfma_f32_16x16x32_bf16 v[88:91], a[68:71], a[52:55], v[88:91]\n"
      "s_lshl_b32 s1, s3, 7\n"
      "s_add_u32 s38, s34, s1\n"
      "v_mfma_f32_16x16x32_bf16 v[120:123], a[76:79], a[52:55], v[120:123]\n"
      "s_addc_u32 s39, s35, 0\n"
      "s_add_u32 m0, s14, 0x15000\n"
      "v_mfma_f32_16x16x32_bf16 v[120:123], a[72:75], a[48:51], v[120:123]\n"
      "v_mfma_f32_16x16x32_bf16 v[124:127], a[72:75], a[56:59], v[124:127]\n"
      "v_mfma_f32_16x16x32_bf16 v[124:127], a[76:79], a[60:63], v[124:127]\n"
      "global_load_dwordx4 a[248:251], v24, s[70:71] offset:2048\n"
      "v_mfma_f32_16x16x32_bf16 v[92:95], a[68:71], a[60:63], v[92:95]\n"
      "v_mfma_f32_16x16x32_bf16 v[92:95], a[64:67], a[56:59], v[92:95]\n"
      "s_waitcnt vmcnt(30) lgkmcnt(0)\n"
      "v_mfma_f32_16x16x32_bf16 v[128:131], a[112:115], a[192:195], v[128:131]\n"
      "s_barrier\n"
      "v_mfma_f32_16x16x32_bf16 v[128:131], a[116:119], a[196:199], v[128:131]\n"
      "ds_read_b64_tr_b16 a[80:81], v12 offset:1024\n"
      "v_mfma_f32_16x16x32_bf16 v[160:163], a[116:119], a[204:207], v[160:163]\n"
      "ds_read_b64_tr_b16 a[82:83], v12 offset:4096\n"
      "v_mfma_f32_16x16x32_bf16 v[160:163], a[112:115], a[200:203], v[160:163]\n"
      "global_load_lds_dwordx4 v1, s[38:39] offset:0\n"
      "v_mfma_f32_16x16x32_bf16 v[164:167], a[120:123], a[200:203], v[164:167]\n"
      "ds_read_b64_tr_b16 a[88:89], v13 offset:1024\n"
      "v_mfma_f32_16x16x32_bf16 v[164:167], a[124:127], a[204:207], v[164:167]\n"
      "v_mfma_f32_16x16x32_bf16 v[132:135], a[124:127], a[196:199], v[132:135]\n"
      "v_mfma_f32_16x16x32_bf16 v[132:135], a[120:123], a[192:195], v[132:135]\n"
      "ds_read_b64_tr_b16 a[90:91], v13 offset:4096\n"
      "v_mfma_f32_16x16x32_bf16 v[136:139], a[128:131], a[192:195], v[136:139]\n"
      "ds_read_b128 a[0:3], v18 offset:0\n"
      "v_mfma_f32_16x16x32_bf16 v[136:139], a[132:135], a[196:199], v[136:139]\n"
      "global_load_lds_dwordx4 v2, s[38:39] offset:1024\n"
      "v_mfma_f32_16x16x32_bf16 v[168:171], a[132:135], a[204:207], v[168:171]\n"
      "ds_read_b128 a[4:7], v19 offset:0\n"
      "s_mul_i32 s3, s3, 0x60000\n"
      "v_mfma_f32_16x16x32_bf16 v[168:171], a[128:131], a[200:203], v[168:171]\n"
      "s_add_u32 s40, s36, s3\n"
      "s_addc_u32 s41, s37, 0\n"
      "v_mfma_f32_16x16x32_bf16 v[172:175], a[136:139], a[200:203], v[172:175]\n"
      "v_mfma_f32_16x16x32_bf16 v[172:175], a[140:143], a[204:207], v[172:175]\n"
      "ds_read_b128 a[8:11], v18 offset:2048\n"
      "v_mfma_f32_16x16x32_bf16 v[140:143], a[140:143], a[196:199], v[140:143]\n"
      "ds_read_b128 a[12:15], v19 offset:2048\n"
      "v_mfma_f32_16x16x32_bf16 v[140:143], a[136:139], a[192:195], v[140:143]\n"
      "global_load_lds_dwordx4 v3, s[38:39] offset:2048\n"
      "v_mfma_f32_16x16x32_bf16 v[144:147], a[144:147], a[192:195], v[144:147]\n"
      "ds_read_b128 a[16:19], v18 offset:4096\n"
      "v_mfma_f32_16x16x32_bf16 v[144:147], a[148:151], a[196:199], v[144:147]\n"
      "v_mfma_f32_16x16x32_bf16 v[176:179], a[148:151], a[204:207], v[176:179]\n"
      "v_mfma_f32_16x16x32_bf16 v[176:179], a[144:147], a[200:203], v[176:179]\n"
      "ds_read_b128 a[20:23], v19 offset:4096\n"
      "v_mfma_f32_16x16x32_bf16 v[180:183], a[152:155], a[200:203], v[180:183]\n"
      "ds_read_b128 a[24:27], v18 offset:6144\n"
      "v_mfma_f32_16x16x32_bf16 v[180:183], a[156:159], a[204:207], v[180:183]\n"
      "global_load_lds_dwordx4 v4, s[38:39] offset:3072\n"
      "v_mfma_f32_16x16x32_bf16 v[148:151], a[156:159], a[196:199], v[148:151]\n"
      "ds_read_b128 a[28:31], v19 offset:6144\n"
      "s_add_u32 m0, s98, 0x15000\n"
      "v_mfma_f32_16x16x32_bf16 v[148:151], a[152:155], a[192:195], v[148:151]\n"
      "v_mfma_f32_16x16x32_bf16 v[152:155], a[160:163], a[192:195], v[152:155]\n"
      "v_mfma_f32_16x16x32_bf16 v[152:155], a[164:167], a[196:199], v[152:155]\n"
      "ds_read_b64_tr_b16 a[96:97], v12 offset:2048\n"
      "v_mfma_f32_16x16x32_bf16 v[184:187], a[164:167], a[204:207], v[184:187]\n"
      "ds_read_b64_tr_b16 a[98:99], v12 offset:5120\n"
      "v_mfma_f32_16x16x32_bf16 v[184:187], a[160:163], a[200:203], v[184:187]\n"
      "global_load_lds_dwordx4 v5, s[40:41] offset:0\n"
      "v_mfma_f32_16x16x32_bf16 v[188:191], a[168:171], a[200:203], v[188:191]\n"
      "ds_read_b64_tr_b16 a[104:105], v13 offset:2048\n"
      "v_mfma_f32_16x16x32_bf16 v[188:191], a[172:175], a[204:207], v[188:191]\n"
      "v_mfma_f32_16x16x32_bf16 v[156:159], a[172:175], a[196:199], v[156:159]\n"
      "v_mfma_f32_16x16x32_bf16 v[156:159], a[168:171], a[192:195], v[156:159]\n"
      "ds_read_b64_tr_b16 a[106:107], v13 offset:5120\n"
      "v_mfma_f32_16x16x32_bf16 v[192:195], a[112:115], a[208:211], v[192:195]\n"
      "ds_read_b64_tr_b16 a[64:65], v12 offset:0\n"
      "v_mfma_f32_16x16x32_bf16 v[192:195], a[116:119], a[212:215], v[192:195]\n"
      "global_load_lds_dwordx4 v6, s[40:41] offset:1024\n"
      "v_mfma_f32_16x16x32_bf16 v[224:227], a[116:119], a[220:223], v[224:227]\n"
      "ds_read_b64_tr_b16 a[66:67], v12 offset:3072\n"
      "v_mfma_f32_16x16x32_bf16 v[224:227], a[112:115], a[216:219], v[224:227]\n"
      "v_mfma_f32_16x16x32_bf16 v[228:231], a[120:123], a[216:219], v[228:231]\n"
      "v_mfma_f32_16x16x32_bf16 v[228:231], a[124:127], a[220:223], v[228:231]\n"
      "ds_read_b64_tr_b16 a[72:73], v13 offset:0\n"
      "v_mfma_f32_16x16x32_bf16 v[196:199], a[124:127], a[212:215], v[196:199]\n"
      "ds_read_b64_tr_b16 a[74:75], v13 offset:3072\n"
      "v_mfma_f32_16x16x32_bf16 v[196:199], a[120:123], a[208:211], v[196:199]\n"
      "global_load_lds_dwordx4 v7, s[40:41] offset:2048\n"
      "v_mfma_f32_16x16x32_bf16 v[200:203], a[128:131], a[208:211], v[200:203]\n"
      "s_add_u32 s38, s38, 0x20000\n"
      "s_addc_u32 s39, s39, 0\n"
      "v_mfma_f32_16x16x32_bf16 v[200:203], a[132:135], a[212:215], v[200:203]\n"
      "s_add_u32 m0, s14, 0x1c000\n"
      "v_mfma_f32_16x16x32_bf16 v[232:235], a[132:135], a[220:223], v[232:235]\n"
      "v_mfma_f32_16x16x32_bf16 v[232:235], a[128:131], a[216:219], v[232:235]\n"
      "v_mfma_f32_16x16x32_bf16 v[236:239], a[136:139], a[216:219], v[236:239]\n"
      "global_load_dword v0, v43, s[48:49] offset:0\n"
      "v_mfma_f32_16x16x32_bf16 v[236:239], a[140:143], a[220:223], v[236:239]\n"
      "global_load_dwordx4 a[252:255], v24, s[70:71] offset:3072\n"
      "v_mfma_f32_16x16x32_bf16 v[204:207], a[140:143], a[212:215], v[204:207]\n"
      "v_mfma_f32_16x16x32_bf16 v[204:207], a[136:139], a[208:211], v[204:207]\n"
      "s_waitcnt vmcnt(31) lgkmcnt(0)\n"
      "v_mfma_f32_16x16x32_bf16 v[208:211], a[144:147], a[208:211], v[208:211]\n"
      "s_barrier\n"
      "v_mfma_f32_16x16x32_bf16 v[208:211], a[148:151], a[212:215], v[208:211]\n"
      "ds_read_b64_tr_b16 a[84:85], v8 offset:1024\n"
      "v_mfma_f32_16x16x32_bf16 v[240:243], a[148:151], a[220:223], v[240:243]\n"
      "ds_read_b64_tr_b16 a[86:87], v8 offset:4096\n"
      "v_mfma_f32_16x16x32_bf16 v[240:243], a[144:147], a[216:219], v[240:243]\n"
      "global_load_lds_dwordx4 v1, s[38:39] offset:0\n"
      "v_mfma_f32_16x16x32_bf16 v[244:247], a[152:155], a[216:219], v[244:247]\n"
      "ds_read_b64_tr_b16 a[92:93], v9 offset:1024\n"
      "v_mfma_f32_16x16x32_bf16 v[244:247], a[156:159], a[220:223], v[244:247]\n"
      "v_mfma_f32_16x16x32_bf16 v[212:215], a[156:159], a[212:215], v[212:215]\n"
      "v_mfma_f32_16x16x32_bf16 v[212:215], a[152:155], a[208:211], v[212:215]\n"
      "ds_read_b64_tr_b16 a[94:95], v9 offset:4096\n"
      "v_mfma_f32_16x16x32_bf16 v[216:219], a[160:163], a[208:211], v[216:219]\n"
      "ds_read_b128 a[32:35], v14 offset:0\n"
      "v_mfma_f32_16x16x32_bf16 v[216:219], a[164:167], a[212:215], v[216:219]\n"
      "global_load_lds_dwordx4 v2, s[38:39] offset:1024\n"
      "v_mfma_f32_16x16x32_bf16 v[248:251], a[164:167], a[220:223], v[248:251]\n"
      "ds_read_b128 a[36:39], v15 offset:0\n"
      "s_add_u32 s40, s40, 0x30000\n"
      "v_mfma_f32_16x16x32_bf16 v[248:251], a[160:163], a[216:219], v[248:251]\n"
      "s_addc_u32 s41, s41, 0\n"
      "v_mfma_f32_16x16x32_bf16 v[252:255], a[168:171], a[216:219], v[252:255]\n"
      "v_mfma_f32_16x16x32_bf16 v[252:255], a[172:175], a[220:223], v[252:255]\n"
      "ds_read_b128 a[40:43], v14 offset:2048\n"
      "v_mfma_f32_16x16x32_bf16 v[220:223], a[172:175], a[212:215], v[220:223]\n"
      "ds_read_b128 a[44:47], v15 offset:2048\n"
      "v_mfma_f32_16x16x32_bf16 v[220:223], a[168:171], a[208:211], v[220:223]\n"
      "global_load_lds_dwordx4 v3, s[38:39] offset:2048\n"
      "v_mfma_f32_16x16x32_bf16 v[64:67], a[176:179], a[112:115], v[64:67]\n"
      "ds_read_b128 a[48:51], v14 offset:4096\n"
      "v_mfma_f32_16x16x32_bf16 v[64:67], a[180:183], a[116:119], v[64:67]\n"
      "v_mfma_f32_16x16x32_bf16 v[96:99], a[188:191], a[116:119], v[96:99]\n"
      "v_mfma_f32_16x16x32_bf16 v[96:99], a[184:187], a[112:115], v[96:99]\n"
      "ds_read_b128 a[52:55], v15 offset:4096\n"
      "v_mfma_f32_16x16x32_bf16 v[100:103], a[184:187], a[120:123], v[100:103]\n"
      "ds_read_b128 a[56:59], v14 offset:6144\n"
      "v_mfma_f32_16x16x32_bf16 v[100:103], a[188:191], a[124:127], v[100:103]\n"
      "global_load_lds_dwordx4 v4, s[38:39] offset:3072\n"
      "v_mfma_f32_16x16x32_bf16 v[68:71], a[180:183], a[124:127], v[68:71]\n"
      "ds_read_b128 a[60:63], v15 offset:6144\n"
      "s_add_u32 m0, s98, 0x1c000\n"
      "v_mfma_f32_16x16x32_bf16 v[68:71], a[176:179], a[120:123], v[68:71]\n"
      "v_mfma_f32_16x16x32_bf16 v[72:75], a[176:179], a[128:131], v[72:75]\n"
      "v_mfma_f32_16x16x32_bf16 v[72:75], a[180:183], a[132:135], v[72:75]\n"
      "ds_read_b64_tr_b16 a[100:101], v8 offset:2048\n"
      "v_mfma_f32_16x16x32_bf16 v[104:107], a[188:191], a[132:135], v[104:107]\n"
      "ds_read_b64_tr_b16 a[102:103], v8 offset:5120\n"
      "v_mfma_f32_16x16x32_bf16 v[104:107], a[184:187], a[128:131], v[104:107]\n"
      "global_load_lds_dwordx4 v5, s[40:41] offset:0\n"
      "v_mfma_f32_16x16x32_bf16 v[108:111], a[184:187], a[136:139], v[108:111]\n"
      "ds_read_b64_tr_b16 a[108:109], v9 offset:2048\n"
      "v_mfma_f32_16x16x32_bf16 v[108:111], a[188:191], a[140:143], v[108:111]\n"
      "v_mfma_f32_16x16x32_bf16 v[76:79], a[180:183], a[140:143], v[76:79]\n"
      "v_mfma_f32_16x16x32_bf16 v[76:79], a[176:179], a[136:139], v[76:79]\n"
      "ds_read_b64_tr_b16 a[110:111], v9 offset:5120\n"
      "v_mfma_f32_16x16x32_bf16 v[80:83], a[176:179], a[144:147], v[80:83]\n"
      "ds_read_b64_tr_b16 a[68:69], v8 offset:0\n"
      "v_mfma_f32_16x16x32_bf16 v[80:83], a[180:183], a[148:151], v[80:83]\n"
      "global_load_lds_dwordx4 v6, s[40:41] offset:1024\n"
      "v_mfma_f32_16x16x32_bf16 v[112:115], a[188:191], a[148:151], v[112:115]\n"
      "ds_read_b64_tr_b16 a[70:71], v8 offset:3072\n"
      "v_mfma_f32_16x16x32_bf16 v[112:115], a[184:187], a[144:147], v[112:115]\n"
      "v_mfma_f32_16x16x32_bf16 v[116:119], a[184:187], a[152:155], v[116:119]\n"
      "v_mfma_f32_16x16x32_bf16 v[116:119], a[188:191], a[156:159], v[116:119]\n"
      "ds_read_b64_tr_b16 a[76:77], v9 offset:0\n"
      "v_mfma_f32_16x16x32_bf16 v[84:87], a[180:183], a[156:159], v[84:87]\n"
      "ds_read_b64_tr_b16 a[78:79], v9 offset:3072\n"
      "v_mfma_f32_16x16x32_bf16 v[84:87], a[176:179], a[152:155], v[84:87]\n"
      "global_load_lds_dwordx4 v7, s[40:41] offset:2048\n"
      "v_mfma_f32_16x16x32_bf16 v[88:91], a[176:179], a[160:163], v[88:91]\n"
      "s_add_u32 s3, s97, 15\n"
      "s_and_b32 s3, s3, 15\n"
      "v_mfma_f32_16x16x32_bf16 v[88:91], a[180:183], a[164:167], v[88:91]\n"
      "s_lshl_b32 s1, s3, 7\n"
      "s_add_u32 s38, s34, s1\n"
      "v_mfma_f32_16x16x32_bf16 v[120:123], a[188:191], a[164:167], v[120:123]\n"
      "s_addc_u32 s39, s35, 0\n"
      "s_add_u32 m0, s14, 0\n"
      "v_mfma_f32_16x16x32_bf16 v[120:123], a[184:187], a[160:163], v[120:123]\n"
      "v_mfma_f32_16x16x32_bf16 v[124:127], a[184:187], a[168:171], v[124:127]\n"
      "v_mfma_f32_16x16x32_bf16 v[124:127], a[188:191], a[172:175], v[124:127]\n"
      "v_mfma_f32_16x16x32_bf16 v[92:95], a[180:183], a[172:175], v[92:95]\n"
      "s_getpc_b64 s[74:75]\n"
      ".Lgm_pc1150:\n"
      "s_lshr_b32 s12, s33, 3\n"
      "v_mfma_f32_16x16x32_bf16 v[92:95], a[176:179], a[168:171], v[92:95]\n"
      "s_lshl2_add_u32 s12, s12, s4\n"
      "s_lshl_b32 s12, s12, 7\n"
      "s_waitcnt vmcnt(25) lgkmcnt(0)\n"
      "v_mfma_f32_16x16x32_bf16 v[128:131], a[0:3], a[80:83], v[128:131]\n"
      "s_barrier\n"
      "s_add_u32 s12, s12, 0x800\n"
      "v_mfma_f32_16x16x32_bf16 v[128:131], a[4:7], a[84:87], v[128:131]\n"
      "ds_read_b64_tr_b16 a[192:193], v8 offset:29696\n"
      "v_mov_b32 v62, s12\n"
      "v_mfma_f32_16x16x32_bf16 v[160:163], a[4:7], a[92:95], v[160:163]\n"
      "ds_read_b64_tr_b16 a[194:195], v8 offset:32768\n"
      "v_min_u32 v62, (.Lgm_last - .Lgm_pc1150 - 8) & 0xfffffffc, v62\n"
      "v_mfma_f32_16x16x32_bf16 v[160:163], a[0:3], a[88:91], v[160:163]\n"
      "global_load_lds_dwordx4 v1, s[38:39] offset:0\n"
      "v_mfma_f32_16x16x32_bf16 v[164:167], a[8:11], a[88:91], v[164:167]\n"
      "ds_read_b64_tr_b16 a[200:201], v9 offset:29696\n"
      "v_mfma_f32_16x16x32_bf16 v[164:167], a[12:15], a[92:95], v[164:167]\n"
      "global_load_dword v0, v62, s[74:75] offset:0\n"
      "v_mfma_f32_16x16x32_bf16 v[132:135], a[12:15], a[84:87], v[132:135]\n"
      "v_mfma_f32_16x16x32_bf16 v[132:135], a[8:11], a[80:83], v[132:135]\n"
      "ds_read_b64_tr_b16 a[202:203], v9 offset:32768\n"
      "v_mfma_f32_16x16x32_bf16 v[136:139], a[16:19], a[80:83], v[136:139]\n"
      "ds_read_b128 a[112:115], v14 offset:28672\n"
      "s_getpc_b64 s[74:75]\n"
      ".Lgm_pc1160:\n"
      "v_mfma_f32_16x16x32_bf16 v[136:139], a[20:23], a[84:87], v[136:139]\n"
      "global_load_lds_dwordx4 v2, s[38:39] offset:1024\n"
      "v_mfma_f32_16x16x32_bf16 v[168:171], a[20:23], a[92:95], v[168:171]\n"
      "ds_read_b128 a[116:119], v15 offset:28672\n"
      "s_mul_i32 s3, s3, 0x60000\n"
      "v_mfma_f32_16x16x32_bf16 v[168:171], a[16:19], a[88:91], v[168:171]\n"
      "s_add_u32 s40, s36, s3\n"
      "s_addc_u32 s41, s37, 0\n"
      "v_mfma_f32_16x16x32_bf16 v[172:175], a[24:27], a[88:91], v[172:175]\n"
      "s_lshr_b32 s12, s33, 3\n"
      "s_lshl2_add_u32 s12, s12, s4\n"
      "v_mfma_f32_16x16x32_bf16 v[172:175], a[28:31], a[92:95], v[172:175]\n"
      "ds_read_b128 a[120:123], v14 offset:30720\n"
      "s_lshl_b32 s12, s12, 7\n"
      "v_mfma_f32_16x16x32_bf16 v[140:143], a[28:31], a[84:87], v[140:143]\n"
      "ds_read_b128 a[124:127], v15 offset:30720\n"
      "s_add_u32 s12, s12, 0x4800\n"
      "v_mfma_f32_16x16x32_bf16 v[140:143], a[24:27], a[80:83], v[140:143]\n"
      "global_load_lds_dwordx4 v3, s[38:39] offset:2048\n"
      "v_mfma_f32_16x16x32_bf16 v[144:147], a[32:35], a[80:83], v[144:147]\n"
      "ds_read_b128 a[128:131], v14 offset:32768\n"
      "v_mov_b32 v62, s12\n"
      "v_mfma_f32_16x16x32_bf16 v[144:147], a[36:39], a[84:87], v[144:147]\n"
      "v_min_u32 v62, (.Lgm_last - .Lgm_pc1160 - 8) & 0xfffffffc, v62\n"
      "v_mfma_f32_16x16x32_bf16 v[176:179], a[36:39], a[92:95], v[176:179]\n"
      "global_load_dword v0, v62, s[74:75] offset:0\n"
      "v_mfma_f32_16x16x32_bf16 v[176:179], a[32:35], a[88:91], v[176:179]\n"
      "ds_read_b128 a[132:135], v15 offset:32768\n"
      "v_mfma_f32_16x16x32_bf16 v[180:183], a[40:43], a[88:91], v[180:183]\n"
      "ds_read_b128 a[136:139], v14 offset:34816\n"
      "v_mfma_f32_16x16x32_bf16 v[180:183], a[44:47], a[92:95], v[180:183]\n"
      "global_load_lds_dwordx4 v4, s[38:39] offset:3072\n"
      "v_mfma_f32_16x16x32_bf16 v[148:151], a[44:47], a[84:87], v[148:151]\n"
      "ds_read_b128 a[140:143], v15 offset:34816\n"
      "s_add_u32 m0, s98, 0\n"
      "v_mfma_f32_16x16x32_bf16 v[148:151], a[40:43], a[80:83], v[148:151]\n"
      "v_mfma_f32_16x16x32_bf16 v[152:155], a[48:51], a[80:83], v[152:155]\n"
      "v_mfma_f32_16x16x32_bf16 v[152:155], a[52:55], a[84:87], v[152:155]\n"
      "ds_read_b64_tr_b16 a[208:209], v8 offset:30720\n"
      "v_mfma_f32_16x16x32_bf16 v[184:187], a[52:55], a[92:95], v[184:187]\n"
      "ds_read_b64_tr_b16 a[210:211], v8 offset:33792\n"
      "v_mfma_f32_16x16x32_bf16 v[184:187], a[48:51], a[88:91], v[184:187]\n"
      "global_load_lds_dwordx4 v5, s[40:41] offset:0\n"
      "v_mfma_f32_16x16x32_bf16 v[188:191], a[56:59], a[88:91], v[188:191]\n"
      "ds_read_b64_tr_b16 a[216:217], v9 offset:30720\n"
      "v_mfma_f32_16x16x32_bf16 v[188:191], a[60:63], a[92:95], v[188:191]\n"
      "v_mfma_f32_16x16x32_bf16 v[156:159], a[60:63], a[84:87], v[156:159]\n"
      "v_mfma_f32_16x16x32_bf16 v[156:159], a[56:59], a[80:83], v[156:159]\n"
      "ds_read_b64_tr_b16 a[218:219], v9 offset:33792\n"
      "v_mfma_f32_16x16x32_bf16 v[192:195], a[0:3], a[96:99], v[192:195]\n"
      "ds_read_b64_tr_b16 a[176:177], v8 offset:28672\n"
      "v_mfma_f32_16x16x32_bf16 v[192:195], a[4:7], a[100:103], v[192:195]\n"
      "global_load_lds_dwordx4 v6, s[40:41] offset:1024\n"
      "v_mfma_f32_16x16x32_bf16 v[224:227], a[4:7], a[108:111], v[224:227]\n"
      "ds_read_b64_tr_b16 a[178:179], v8 offset:31744\n"
      "v_mfma_f32_16x16x32_bf16 v[224:227], a[0:3], a[104:107], v[224:227]\n"
      "v_mfma_f32_16x16x32_bf16 v[228:231], a[8:11], a[104:107], v[228:231]\n"
      "v_mfma_f32_16x16x32_bf16 v[228:231], a[12:15], a[108:111], v[228:231]\n"
      "ds_read_b64_tr_b16 a[184:185], v9 offset:28672\n"
      "v_mfma_f32_16x16x32_bf16 v[196:199], a[12:15], a[100:103], v[196:199]\n"
      "ds_read_b64_tr_b16 a[186:187], v9 offset:31744\n"
      "v_mfma_f32_16x16x32_bf16 v[196:199], a[8:11], a[96:99], v[196:199]\n"
      "global_load_lds_dwordx4 v7, s[40:41] offset:2048\n"
      "v_mfma_f32_16x16x32_bf16 v[200:203], a[16:19], a[96:99], v[200:203]\n"
      "s_add_u32 s38, s38, 0x20000\n"
      "s_addc_u32 s39, s39, 0\n"
      "v_mfma_f32_16x16x32_bf16 v[200:203], a[20:23], a[100:103], v[200:203]\n"
      "s_add_u32 m0, s14, 0x7000\n"
      "v_mfma_f32_16x16x32_bf16 v[232:235], a[20:23], a[108:111], v[232:235]\n"
      "v_mfma_f32_16x16x32_bf16 v[232:235], a[16:19], a[104:107], v[232:235]\n"
      "v_mfma_f32_16x16x32_bf16 v[236:239], a[24:27], a[104:107], v[236:239]\n"
      "v_mfma_f32_16x16x32_bf16 v[236:239], a[28:31], a[108:111], v[236:239]\n"
      "v_mfma_f32_16x16x32_bf16 v[204:207], a[28:31], a[100:103], v[204:207]\n"
      "v_mfma_f32_16x16x32_bf16 v[204:207], a[24:27], a[96:99], v[204:207]\n"
      "s_waitcnt vmcnt(26) lgkmcnt(0)\n"
      "v_mfma_f32_16x16x32_bf16 v[208:211], a[32:35], a[96:99], v[208:211]\n"
      "s_barrier\n"
      "v_mfma_f32_16x16x32_bf16 v[208:211], a[36:39], a[100:103], v[208:211]\n"
      "ds_read_b64_tr_b16 a[196:197], v10 offset:1024\n"
      "v_mfma_f32_16x16x32_bf16 v[240:243], a[36:39], a[108:111], v[240:243]\n"
      "ds_read_b64_tr_b16 a[198:199], v10 offset:4096\n"
      "v_mfma_f32_16x16x32_bf16 v[240:243], a[32:35], a[104:107], v[240:243]\n"
      "global_load_lds_dwordx4 v1, s[38:39] offset:0\n"
      "v_mfma_f32_16x16x32_bf16 v[244:247], a[40:43], a[104:107], v[244:247]\n"
      "ds_read_b64_tr_b16 a[204:205], v11 offset:1024\n"
      "v_mfma_f32_16x16x32_bf16 v[244:247], a[44:47], a[108:111], v[244:247]\n"
      "v_mfma_f32_16x16x32_bf16 v[212:215], a[44:47], a[100:103], v[212:215]\n"
      "v_mfma_f32_16x16x32_bf16 v[212:215], a[40:43], a[96:99], v[212:215]\n"
      "ds_read_b64_tr_b16 a[206:207], v11 offset:4096\n"
      "v_mfma_f32_16x16x32_bf16 v[216:219], a[48:51], a[96:99], v[216:219]\n"
      "ds_read_b128 a[144:147], v16 offset:0\n"
      "v_mfma_f32_16x16x32_bf16 v[216:219], a[52:55], a[100:103], v[216:219]\n"
      "global_load_lds_dwordx4 v2, s[38:39] offset:1024\n"
      "v_mfma_f32_16x16x32_bf16 v[248:251], a[52:55], a[108:111], v[248:251]\n"
      "ds_read_b128 a[148:151], v17 offset:0\n"
      "s_add_u32 s40, s40, 0x30000\n"
      "v_mfma_f32_16x16x32_bf16 v[248:251], a[48:51], a[104:107], v[248:251]\n"
      "s_addc_u32 s41, s41, 0\n"
      "v_mfma_f32_16x16x32_bf16 v[252:255], a[56:59], a[104:107], v[252:255]\n"
      "v_mfma_f32_16x16x32_bf16 v[252:255], a[60:63], a[108:111], v[252:255]\n"
      "ds_read_b128 a[152:155], v16 offset:2048\n"
      "v_mfma_f32_16x16x32_bf16 v[220:223], a[60:63], a[100:103], v[220:223]\n"
      "ds_read_b128 a[156:159], v17 offset:2048\n"
      "v_mfma_f32_16x16x32_bf16 v[220:223], a[56:59], a[96:99], v[220:223]\n"
      "global_load_lds_dwordx4 v3, s[38:39] offset:2048\n"
      "v_mfma_f32_16x16x32_bf16 v[64:67], a[64:67], a[0:3], v[64:67]\n"
      "ds_read_b128 a[160:163], v16 offset:4096\n"
      "v_mfma_f32_16x16x32_bf16 v[64:67], a[68:71], a[4:7], v[64:67]\n"
      "v_mfma_f32_16x16x32_bf16 v[96:99], a[76:79], a[4:7], v[96:99]\n"
      "v_mfma_f32_16x16x32_bf16 v[96:99], a[72:75], a[0:3], v[96:99]\n"
      "ds_read_b128 a[164:167], v17 offset:4096\n"
      "v_mfma_f32_16x16x32_bf16 v[100:103], a[72:75], a[8:11], v[100:103]\n"
      "ds_read_b128 a[168:171], v16 offset:6144\n"
      "v_mfma_f32_16x16x32_bf16 v[100:103], a[76:79], a[12:15], v[100:103]\n"
      "global_load_lds_dwordx4 v4, s[38:39] offset:3072\n"
      "v_mfma_f32_16x16x32_bf16 v[68:71], a[68:71], a[12:15], v[68:71]\n"
      "ds_read_b128 a[172:175], v17 offset:6144\n"
      "s_add_u32 m0, s98, 0x7000\n"
      "v_mfma_f32_16x16x32_bf16 v[68:71], a[64:67], a[8:11], v[68:71]\n"
      "v_mfma_f32_16x16x32_bf16 v[72:75], a[64:67], a[16:19], v[72:75]\n"
      "v_mfma_f32_16x16x32_bf16 v[72:75], a[68:71], a[20:23], v[72:75]\n"
      "ds_read_b64_tr_b16 a[212:213], v10 offset:2048\n"
      "v_mfma_f32_16x16x32_bf16 v[104:107], a[76:79], a[20:23], v[104:107]\n"
      "ds_read_b64_tr_b16 a[214:215], v10 offset:5120\n"
      "v_mfma_f32_16x16x32_bf16 v[104:107], a[72:75], a[16:19], v[104:107]\n"
      "global_load_lds_dwordx4 v5, s[40:41] offset:0\n"
      "v_mfma_f32_16x16x32_bf16 v[108:111], a[72:75], a[24:27], v[108:111]\n"
      "ds_read_b64_tr_b16 a[220:221], v11 offset:2048\n"
      "v_mfma_f32_16x16x32_bf16 v[108:111], a[76:79], a[28:31], v[108:111]\n"
      "v_mfma_f32_16x16x32_bf16 v[76:79], a[68:71], a[28:31], v[76:79]\n"
      "v_mfma_f32_16x16x32_bf16 v[76:79], a[64:67], a[24:27], v[76:79]\n"
      "ds_read_b64_tr_b16 a[222:223], v11 offset:5120\n"
      "v_mfma_f32_16x16x32_bf16 v[80:83], a[64:67], a[32:35], v[80:83]\n"
      "ds_read_b64_tr_b16 a[180:181], v10 offset:0\n"
      "v_mfma_f32_16x16x32_bf16 v[80:83], a[68:71], a[36:39], v[80:83]\n"
      "global_load_lds_dwordx4 v6, s[40:41] offset:1024\n"
      "v_mfma_f32_16x16x32_bf16 v[112:115], a[76:79], a[36:39], v[112:115]\n"
      "ds_read_b64_tr_b16 a[182:183], v10 offset:3072\n"
      "v_mfma_f32_16x16x32_bf16 v[112:115], a[72:75], a[32:35], v[112:115]\n"
      "v_mfma_f32_16x16x32_bf16 v[116:119], a[72:75], a[40:43], v[116:119]\n"
      "v_mfma_f32_16x16x32_bf16 v[116:119], a[76:79], a[44:47], v[116:119]\n"
      "ds_read_b64_tr_b16 a[188:189], v11 offset:0\n"
      "v_mfma_f32_16x16x32_bf16 v[84:87], a[68:71], a[44:47], v[84:87]\n"
      "ds_read_b64_tr_b16 a[190:191], v11 offset:3072\n"
      "v_mfma_f32_16x16x32_bf16 v[84:87], a[64:67], a[40:43], v[84:87]\n"
      "global_load_lds_dwordx4 v7, s[40:41] offset:2048\n"
      "v_mfma_f32_16x16x32_bf16 v[88:91], a[64:67], a[48:51], v[88:91]\n"
      "s_add_u32 s3, s0, 0\n"
      "s_and_b32 s3, s3, 7\n"
      "v_mfma_f32_16x16x32_bf16 v[88:91], a[68:71], a[52:55], v[88:91]\n"
      "s_lshl_b32 s3, s3, 18\n"
      "s_add_u32 s38, s42, s3\n"
      "v_mfma_f32_16x16x32_bf16 v[120:123], a[76:79], a[52:55], v[120:123]\n"
      "s_addc_u32 s39, s43, 0\n"
      "s_add_u32 m0, s99, 0x10000\n"
      "v_mfma_f32_16x16x32_bf16 v[120:123], a[72:75], a[48:51], v[120:123]\n"
      "v_mfma_f32_16x16x32_bf16 v[124:127], a[72:75], a[56:59], v[124:127]\n"
      "v_mfma_f32_16x16x32_bf16 v[124:127], a[76:79], a[60:63], v[124:127]\n"
      "v_mfma_f32_16x16x32_bf16 v[92:95], a[68:71], a[60:63], v[92:95]\n"
      "v_mfma_f32_16x16x32_bf16 v[92:95], a[64:67], a[56:59], v[92:95]\n"
      "s_waitcnt vmcnt(25) lgkmcnt(0)\n"
      "v_mfma_f32_16x16x32_bf16 v[128:131], a[112:115], a[192:195], v[128:131]\n"
      "s_barrier\n"
      "v_mfma_f32_16x16x32_bf16 v[128:131], a[116:119], a[196:199], v[128:131]\n"
      "ds_read_b64_tr_b16 a[80:81], v10 offset:29696\n"
      "v_mfma_f32_16x16x32_bf16 v[160:163], a[116:119], a[204:207], v[160:163]\n"
      "ds_read_b64_tr_b16 a[82:83], v10 offset:32768\n"
      "v_mfma_f32_16x16x32_bf16 v[160:163], a[112:115], a[200:203], v[160:163]\n"
      "ds_read_b64_tr_b16 a[88:89], v11 offset:29696\n"
      "v_mfma_f32_16x16x32_bf16 v[164:167], a[120:123], a[200:203], v[164:167]\n"
      "ds_read_b64_tr_b16 a[90:91], v11 offset:32768\n"
      "v_mfma_f32_16x16x32_bf16 v[164:167], a[124:127], a[204:207], v[164:167]\n"
      "ds_read_b128 a[0:3], v16 offset:28672\n"
      "v_mfma_f32_16x16x32_bf16 v[132:135], a[124:127], a[196:199], v[132:135]\n"
      "ds_read_b128 a[4:7], v17 offset:28672\n"
      "v_mfma_f32_16x16x32_bf16 v[132:135], a[120:123], a[192:195], v[132:135]\n"
      "ds_read_b128 a[8:11], v16 offset:30720\n"
      "v_mfma_f32_16x16x32_bf16 v[136:139], a[128:131], a[192:195], v[136:139]\n"
      "ds_read_b128 a[12:15], v17 offset:30720\n"
      "v_mfma_f32_16x16x32_bf16 v[136:139], a[132:135], a[196:199], v[136:139]\n"
      "ds_read_b128 a[16:19], v16 offset:32768\n"
      "v_mfma_f32_16x16x32_bf16 v[168:171], a[132:135], a[204:207], v[168:171]\n"
      "ds_read_b128 a[20:23], v17 offset:32768\n"
      "v_mfma_f32_16x16x32_bf16 v[168:171], a[128:131], a[200:203], v[168:171]\n"
      "ds_read_b128 a[24:27], v16 offset:34816\n"
      "v_mfma_f32_16x16x32_bf16 v[172:175], a[136:139], a[200:203], v[172:175]\n"
      "ds_read_b128 a[28:31], v17 offset:34816\n"
      "v_mfma_f32_16x16x32_bf16 v[172:175], a[140:143], a[204:207], v[172:175]\n"
      "ds_read_b64_tr_b16 a[96:97], v10 offset:30720\n"
      "v_mfma_f32_16x16x32_bf16 v[140:143], a[140:143], a[196:199], v[140:143]\n"
      "ds_read_b64_tr_b16 a[98:99], v10 offset:33792\n"
      "v_mfma_f32_16x16x32_bf16 v[140:143], a[136:139], a[192:195], v[140:143]\n"
      "ds_read_b64_tr_b16 a[104:105], v11 offset:30720\n"
      "v_mfma_f32_16x16x32_bf16 v[144:147], a[144:147], a[192:195], v[144:147]\n"
      "ds_read_b64_tr_b16 a[106:107], v11 offset:33792\n"
      "v_mfma_f32_16x16x32_bf16 v[144:147], a[148:151], a[196:199], v[144:147]\n"
      "ds_read_b64_tr_b16 a[64:65], v10 offset:28672\n"
      "v_mfma_f32_16x16x32_bf16 v[176:179], a[148:151], a[204:207], v[176:179]\n"
      "ds_read_b64_tr_b16 a[66:67], v10 offset:31744\n"
      "v_mfma_f32_16x16x32_bf16 v[176:179], a[144:147], a[200:203], v[176:179]\n"
      "ds_read_b64_tr_b16 a[72:73], v11 offset:28672\n"
      "v_mfma_f32_16x16x32_bf16 v[180:183], a[152:155], a[200:203], v[180:183]\n"
      "ds_read_b64_tr_b16 a[74:75], v11 offset:31744\n"
      "v_mfma_f32_16x16x32_bf16 v[180:183], a[156:159], a[204:207], v[180:183]\n"
      "v_mfma_f32_16x16x32_bf16 v[148:151], a[156:159], a[196:199], v[148:151]\n"
      "v_mfma_f32_16x16x32_bf16 v[148:151], a[152:155], a[192:195], v[148:151]\n"
      "v_mfma_f32_16x16x32_bf16 v[152:155], a[160:163], a[192:195], v[152:155]\n"
      "v_mfma_f32_16x16x32_bf16 v[152:155], a[164:167], a[196:199], v[152:155]\n"
      "s_waitcnt vmcnt(16) lgkmcnt(0)\n"
      "v_mfma_f32_16x16x32_bf16 v[184:187], a[164:167], a[204:207], v[184:187]\n"
      "s_barrier\n"
      "v_mfma_f32_16x16x32_bf16 v[184:187], a[160:163], a[200:203], v[184:187]\n"
      "ds_read_b64_tr_b16 a[84:85], v12 offset:1024\n"
      "v_mfma_f32_16x16x32_bf16 v[188:191], a[168:171], a[200:203], v[188:191]\n"
      "ds_read_b64_tr_b16 a[86:87], v12 offset:4096\n"
      "v_mfma_f32_16x16x32_bf16 v[188:191], a[172:175], a[204:207], v[188:191]\n"
      "ds_read_b64_tr_b16 a[92:93], v13 offset:1024\n"
      "v_mfma_f32_16x16x32_bf16 v[156:159], a[172:175], a[196:199], v[156:159]\n"
      "ds_read_b64_tr_b16 a[94:95], v13 offset:4096\n"
      "v_mfma_f32_16x16x32_bf16 v[156:159], a[168:171], a[192:195], v[156:159]\n"
      "ds_read_b128 a[32:35], v18 offset:0\n"
      "v_mfma_f32_16x16x32_bf16 v[192:195], a[112:115], a[208:211], v[192:195]\n"
      "ds_read_b128 a[36:39], v19 offset:0\n"
      "v_mfma_f32_16x16x32_bf16 v[192:195], a[116:119], a[212:215], v[192:195]\n"
      "ds_read_b128 a[40:43], v18 offset:2048\n"
      "v_mfma_f32_16x16x32_bf16 v[224:227], a[116:119], a[220:223], v[224:227]\n"
      "ds_read_b128 a[44:47], v19 offset:2048\n"
      "v_mfma_f32_16x16x32_bf16 v[224:227], a[112:115], a[216:219], v[224:227]\n"
      "ds_read_b128 a[48:51], v18 offset:4096\n"
      "v_mfma_f32_16x16x32_bf16 v[64:67], a[176:179], a[112:115], v[64:67]\n"
      "ds_read_b128 a[52:55], v19 offset:4096\n"
      "v_mfma_f32_16x16x32_bf16 v[64:67], a[180:183], a[116:119], v[64:67]\n"
      "ds_read_b128 a[56:59], v18 offset:6144\n"
      "v_mfma_f32_16x16x32_bf16 v[96:99], a[188:191], a[116:119], v[96:99]\n"
      "ds_read_b128 a[60:63], v19 offset:6144\n"
      "v_mfma_f32_16x16x32_bf16 v[96:99], a[184:187], a[112:115], v[96:99]\n"
      "ds_read_b64_tr_b16 a[100:101], v12 offset:2048\n"
      "v_mfma_f32_16x16x32_bf16 v[196:199], a[120:123], a[208:211], v[196:199]\n"
      "ds_read_b64_tr_b16 a[102:103], v12 offset:5120\n"
      "v_mfma_f32_16x16x32_bf16 v[196:199], a[124:127], a[212:215], v[196:199]\n"
      "ds_read_b64_tr_b16 a[108:109], v13 offset:2048\n"
      "v_mfma_f32_16x16x32_bf16 v[228:231], a[124:127], a[220:223], v[228:231]\n"
      "ds_read_b64_tr_b16 a[110:111], v13 offset:5120\n"
      ".Lgm_w5:\n"
      "s_cbranch_execz .Lgm_r1\n"
      "v_mfma_f32_16x16x32_bf16 v[228:231], a[120:123], a[216:219], v[228:231]\n"
      "ds_read_b64_tr_b16 a[68:69], v12 offset:0\n"
      "v_mfma_f32_16x16x32_bf16 v[68:71], a[176:179], a[120:123], v[68:71]\n"
      "ds_read_b64_tr_b16 a[70:71], v12 offset:3072\n"
      "v_mfma_f32_16x16x32_bf16 v[68:71], a[180:183], a[124:127], v[68:71]\n"
      "ds_read_b64_tr_b16 a[76:77], v13 offset:0\n"
      "v_mfma_f32_16x16x32_bf16 v[100:103], a[188:191], a[124:127], v[100:103]\n"
      "ds_read_b64_tr_b16 a[78:79], v13 offset:3072\n"
      "v_mfma_f32_16x16x32_bf16 v[100:103], a[184:187], a[120:123], v[100:103]\n"
      "global_load_lds_dwordx4 v32, s[38:39] offset:0\n"
      "v_mfma_f32_16x16x32_bf16 v[200:203], a[128:131], a[208:211], v[200:203]\n"
      "v_mfma_f32_16x16x32_bf16 v[200:203], a[132:135], a[212:215], v[200:203]\n"
      "v_mfma_f32_16x16x32_bf16 v[232:235], a[132:135], a[220:223], v[232:235]\n"
      "v_mfma_f32_16x16x32_bf16 v[232:235], a[128:131], a[216:219], v[232:235]\n"
      "global_load_lds_dwordx4 v33, s[38:39] offset:1024\n"
      "s_waitcnt vmcnt(9) lgkmcnt(0)\n"
      "v_mfma_f32_16x16x32_bf16 v[72:75], a[176:179], a[128:131], v[72:75]\n"
      "s_barrier\n"
      "v_mfma_f32_16x16x32_bf16 v[72:75], a[180:183], a[132:135], v[72:75]\n"
      "ds_read_b64_tr_b16 a[192:193], v8 offset:1024\n"
      "v_mfma_f32_16x16x32_bf16 v[104:107], a[188:191], a[132:135], v[104:107]\n"
      "ds_read_b64_tr_b16 a[194:195], v8 offset:4096\n"
      "v_mfma_f32_16x16x32_bf16 v[104:107], a[184:187], a[128:131], v[104:107]\n"
      "ds_read_b64_tr_b16 a[200:201], v9 offset:1024\n"
      "v_mfma_f32_16x16x32_bf16 v[204:207], a[136:139], a[208:211], v[204:207]\n"
      "ds_read_b64_tr_b16 a[202:203], v9 offset:4096\n"
      "v_mfma_f32_16x16x32_bf16 v[204:207], a[140:143], a[212:215], v[204:207]\n"
      "ds_read_b128 a[112:115], v14 offset:0\n"
      "v_mfma_f32_16x16x32_bf16 v[236:239], a[140:143], a[220:223], v[236:239]\n"
      "ds_read_b128 a[116:119], v15 offset:0\n"
      "v_mfma_f32_16x16x32_bf16 v[236:239], a[136:139], a[216:219], v[236:239]\n"
      "ds_read_b128 a[120:123], v14 offset:2048\n"
      "v_mfma_f32_16x16x32_bf16 v[76:79], a[176:179], a[136:139], v[76:79]\n"
      "ds_read_b128 a[124:127], v15 offset:2048\n"
      "v_mfma_f32_16x16x32_bf16 v[76:79], a[180:183], a[140:143], v[76:79]\n"
      "ds_read_b128 a[128:131], v14 offset:4096\n"
      "v_mfma_f32_16x16x32_bf16 v[108:111], a[188:191], a[140:143], v[108:111]\n"
      "ds_read_b128 a[132:135], v15 offset:4096\n"
      "v_mfma_f32_16x16x32_bf16 v[108:111], a[184:187], a[136:139], v[108:111]\n"
      "ds_read_b128 a[136:139], v14 offset:6144\n"
      "v_mfma_f32_16x16x32_bf16 v[208:211], a[144:147], a[208:211], v[208:211]\n"
      "ds_read_b128 a[140:143], v15 offset:6144\n"
      "v_mfma_f32_16x16x32_bf16 v[208:211], a[148:151], a[212:215], v[208:211]\n"
      "global_load_lds_dwordx4 v34, s[38:39] offset:2048\n"
      "v_mfma_f32_16x16x32_bf16 v[240:243], a[148:151], a[220:223], v[240:243]\n"
      "v_mfma_f32_16x16x32_bf16 v[240:243], a[144:147], a[216:219], v[240:243]\n"
      "v_mfma_f32_16x16x32_bf16 v[80:83], a[176:179], a[144:147], v[80:83]\n"
      "v_mfma_f32_16x16x32_bf16 v[80:83], a[180:183], a[148:151], v[80:83]\n"
      "global_load_lds_dwordx4 v35, s[38:39] offset:3072\n"
      "v_mfma_f32_16x16x32_bf16 v[112:115], a[188:191], a[148:151], v[112:115]\n"
      "s_add_u32 s38, s38, 0x10000\n"
      "s_addc_u32 s39, s39, 0\n"
      "v_mfma_f32_16x16x32_bf16 v[112:115], a[184:187], a[144:147], v[112:115]\n"
      "s_add_u32 m0, s99, 0x11000\n"
      "s_waitcnt vmcnt(4) lgkmcnt(0)\n"
      "v_mfma_f32_16x16x32_bf16 v[212:215], a[152:155], a[208:211], v[212:215]\n"
      "s_barrier\n"
      "v_mfma_f32_16x16x32_bf16 v[212:215], a[156:159], a[212:215], v[212:215]\n"
      "ds_read_b64_tr_b16 a[196:197], v8 offset:29696\n"
      "v_mfma_f32_16x16x32_bf16 v[244:247], a[156:159], a[220:223], v[244:247]\n"
      "ds_read_b64_tr_b16 a[198:199], v8 offset:32768\n"
      "v_mfma_f32_16x16x32_bf16 v[244:247], a[152:155], a[216:219], v[244:247]\n"
      "ds_read_b64_tr_b16 a[204:205], v9 offset:29696\n"
      "v_mfma_f32_16x16x32_bf16 v[84:87], a[176:179], a[152:155], v[84:87]\n"
      "ds_read_b64_tr_b16 a[206:207], v9 offset:32768\n"
      "v_mfma_f32_16x16x32_bf16 v[84:87], a[180:183], a[156:159], v[84:87]\n"
      "ds_read_b128 a[144:147], v14 offset:28672\n"
      "v_mfma_f32_16x16x32_bf16 v[116:119], a[188:191], a[156:159], v[116:119]\n"
      "ds_read_b128 a[148:151], v15 offset:28672\n"
      "v_mfma_f32_16x16x32_bf16 v[116:119], a[184:187], a[152:155], v[116:119]\n"
      "ds_read_b128 a[152:155], v14 offset:30720\n"
      "v_mfma_f32_16x16x32_bf16 v[216:219], a[160:163], a[208:211], v[216:219]\n"
      "ds_read_b128 a[156:159], v15 offset:30720\n"
      "v_mfma_f32_16x16x32_bf16 v[216:219], a[164:167], a[212:215], v[216:219]\n"
      "global_load_lds_dwordx4 v32, s[38:39] offset:0\n"
      "v_mfma_f32_16x16x32_bf16 v[248:251], a[164:167], a[220:223], v[248:251]\n"
      "v_mfma_f32_16x16x32_bf16 v[248:251], a[160:163], a[216:219], v[248:251]\n"
      "v_mfma_f32_16x16x32_bf16 v[88:91], a[176:179], a[160:163], v[88:91]\n"
      "v_mfma_f32_16x16x32_bf16 v[88:91], a[180:183], a[164:167], v[88:91]\n"
      "global_load_lds_dwordx4 v33, s[38:39] offset:1024\n"
      "v_mfma_f32_16x16x32_bf16 v[120:123], a[188:191], a[164:167], v[120:123]\n"
      "v_mfma_f32_16x16x32_bf16 v[120:123], a[184:187], a[160:163], v[120:123]\n"
      "ds_read_b128 a[160:163], v14 offset:32768\n"
      "v_mfma_f32_16x16x32_bf16 v[220:223], a[168:171], a[208:211], v[220:223]\n"
      "ds_read_b128 a[164:167], v15 offset:32768\n"
      "v_mfma_f32_16x16x32_bf16 v[220:223], a[172:175], a[212:215], v[220:223]\n"
      "global_load_lds_dwordx4 v34, s[38:39] offset:2048\n"
      "v_mfma_f32_16x16x32_bf16 v[252:255], a[172:175], a[220:223], v[252:255]\n"
      "v_mfma_f32_16x16x32_bf16 v[252:255], a[168:171], a[216:219], v[252:255]\n"
      "v_mfma_f32_16x16x32_bf16 v[92:95], a[176:179], a[168:171], v[92:95]\n"
      "v_mfma_f32_16x16x32_bf16 v[92:95], a[180:183], a[172:175], v[92:95]\n"
      "global_load_lds_dwordx4 v35, s[38:39] offset:3072\n"
      "v_mfma_f32_16x16x32_bf16 v[124:127], a[188:191], a[172:175], v[124:127]\n"
      "s_add_u32 s3, s0, 1\n"
      "s_and_b32 s3, s3, 7\n"
      "v_mfma_f32_16x16x32_bf16 v[124:127], a[184:187], a[168:171], v[124:127]\n"
      "s_lshl_b32 s3, s3, 18\n"
      "s_add_u32 s38, s42, s3\n"
      "s_addc_u32 s39, s43, 0\n"
      "s_waitcnt lgkmcnt(0)\n"
      "v_mfma_f32_16x16x32_bf16 v[128:131], a[0:3], a[80:83], v[128:131]\n"
      "ds_read_b128 a[172:175], v15 offset:34816\n"
      "s_add_u32 m0, s99, 0x18000\n"
      "v_mfma_f32_16x16x32_bf16 v[128:131], a[4:7], a[84:87], v[128:131]\n"
      "ds_read_b128 a[168:171], v14 offset:34816\n"
      "v_mfma_f32_16x16x32_bf16 v[128:131], a[112:115], a[192:195], v[128:131]\n"
      "ds_read_b64_tr_b16 a[208:209], v8 offset:2048\n"
      "v_mfma_f32_16x16x32_bf16 v[128:131], a[116:119], a[196:199], v[128:131]\n"
      "ds_read_b64_tr_b16 a[210:211], v8 offset:5120\n"
      "v_mfma_f32_16x16x32_bf16 v[132:135], a[8:11], a[80:83], v[132:135]\n"
      "ds_read_b64_tr_b16 a[216:217], v9 offset:2048\n"
      "v_mfma_f32_16x16x32_bf16 v[132:135], a[12:15], a[84:87], v[132:135]\n"
      "ds_read_b64_tr_b16 a[218:219], v9 offset:5120\n"
      "v_mfma_f32_16x16x32_bf16 v[132:135], a[120:123], a[192:195], v[132:135]\n"
      "ds_read_b64_tr_b16 a[212:213], v8 offset:30720\n"
      "v_mfma_f32_16x16x32_bf16 v[132:135], a[124:127], a[196:199], v[132:135]\n"
      "ds_read_b64_tr_b16 a[214:215], v8 offset:33792\n"
      "v_fmaak_f32 v44, v128, v25, 0xb5c1a2a0\n"
      "v_mfma_f32_16x16x32_bf16 v[136:139], a[16:19], a[80:83], v[136:139]\n"
      "ds_read_b64_tr_b16 a[220:221], v9 offset:30720\n"
      "v_fmaak_f32 v45, v129, v25, 0xb5c1a2a0\n"
      "v_mfma_f32_16x16x32_bf16 v[136:139], a[20:23], a[84:87], v[136:139]\n"
      "ds_read_b64_tr_b16 a[222:223], v9 offset:33792\n"
      "v_fmaak_f32 v46, v130, v25, 0xb5c1a2a0\n"
      "v_mfma_f32_16x16x32_bf16 v[136:139], a[128:131], a[192:195], v[136:139]\n"
      "ds_read_b64_tr_b16 a[176:177], v8 offset:0\n"
      "v_fmaak_f32 v47, v131, v25, 0xb5c1a2a0\n"
      "v_mfma_f32_16x16x32_bf16 v[136:139], a[132:135], a[196:199], v[136:139]\n"
      "ds_read_b64_tr_b16 a[178:179], v8 offset:3072\n"
      "v_max_f32 v128, s94, v128\n"
      "v_mfma_f32_16x16x32_bf16 v[140:143], a[24:27], a[80:83], v[140:143]\n"
      "ds_read_b64_tr_b16 a[184:185], v9 offset:0\n"
      "v_max_f32 v129, s94, v129\n"
      "v_mfma_f32_16x16x32_bf16 v[140:143], a[28:31], a[84:87], v[140:143]\n"
      "ds_read_b64_tr_b16 a[186:187], v9 offset:3072\n"
      "v_max_f32 v130, s94, v130\n"
      "v_mfma_f32_16x16x32_bf16 v[140:143], a[136:139], a[192:195], v[140:143]\n"
      "ds_read_b64_tr_b16 a[180:181], v8 offset:28672\n"
      "v_max_f32 v131, s94, v131\n"
      "v_mfma_f32_16x16x32_bf16 v[140:143], a[140:143], a[196:199], v[140:143]\n"
      "ds_read_b64_tr_b16 a[182:183], v8 offset:31744\n"
      "v_fmaak_f32 v48, v132, v25, 0xb5c1a2a0\n"
      "v_mfma_f32_16x16x32_bf16 v[144:147], a[32:35], a[80:83], v[144:147]\n"
      "ds_read_b64_tr_b16 a[188:189], v9 offset:28672\n"
      "v_fmaak_f32 v49, v133, v25, 0xb5c1a2a0\n"
      "v_mfma_f32_16x16x32_bf16 v[144:147], a[36:39], a[84:87], v[144:147]\n"
      "ds_read_b64_tr_b16 a[190:191], v9 offset:31744\n"
      "v_fmaak_f32 v50, v134, v25, 0xb5c1a2a0\n"
      "v_mfma_f32_16x16x32_bf16 v[144:147], a[144:147], a[192:195], v[144:147]\n"
      "global_load_lds_dwordx4 v32, s[38:39] offset:0\n"
      "v_mfma_f32_16x16x32_bf16 v[144:147], a[148:151], a[196:199], v[144:147]\n"
      "v_exp_f32_e64 v44, v44 clamp\n"
      "v_mfma_f32_16x16x32_bf16 v[148:151], a[40:43], a[80:83], v[148:151]\n"
      "v_exp_f32_e64 v45, v45 clamp\n"
      "v_mfma_f32_16x16x32_bf16 v[148:151], a[44:47], a[84:87], v[148:151]\n"
      "v_exp_f32_e64 v46, v46 clamp\n"
      "v_mfma_f32_16x16x32_bf16 v[148:151], a[152:155], a[192:195], v[148:151]\n"
      "v_exp_f32_e64 v47, v47 clamp\n"
      "v_mfma_f32_16x16x32_bf16 v[148:151], a[156:159], a[196:199], v[148:151]\n"
      "global_load_lds_dwordx4 v33, s[38:39] offset:1024\n"
      "v_mfma_f32_16x16x32_bf16 v[152:155], a[48:51], a[80:83], v[152:155]\n"
      "v_exp_f32_e64 v48, v48 clamp\n"
      "v_mfma_f32_16x16x32_bf16 v[152:155], a[52:55], a[84:87], v[152:155]\n"
      "v_add_f32 v128, v128, v44\n"
      "s_waitcnt lgkmcnt(0)\n"
      "v_mfma_f32_16x16x32_bf16 v[152:155], a[160:163], a[192:195], v[152:155]\n"
      "s_barrier\n"
      "v_exp_f32_e64 v49, v49 clamp\n"
      "v_mfma_f32_16x16x32_bf16 v[152:155], a[164:167], a[196:199], v[152:155]\n"
      "v_exp_f32_e64 v50, v50 clamp\n"
      "v_mfma_f32_16x16x32_bf16 v[156:159], a[56:59], a[80:83], v[156:159]\n"
      "global_load_lds_dwordx4 v34, s[38:39] offset:2048\n"
      "v_mfma_f32_16x16x32_bf16 v[156:159], a[60:63], a[84:87], v[156:159]\n"
      "v_add_f32 v129, v129, v45\n"
      "v_add_f32 v130, v130, v46\n"
      "v_mfma_f32_16x16x32_bf16 v[156:159], a[168:171], a[192:195], v[156:159]\n"
      "v_add_f32 v131, v131, v47\n"
      "v_fmaak_f32 v51, v135, v25, 0xb5c1a2a0\n"
      "v_mfma_f32_16x16x32_bf16 v[156:159], a[172:175], a[196:199], v[156:159]\n"
      "v_exp_f32_e64 v51, v51 clamp\n"
      "v_mfma_f32_16x16x32_bf16 v[192:195], a[0:3], a[96:99], v[192:195]\n"
      "v_max_f32 v132, s94, v132\n"
      "v_max_f32 v133, s94, v133\n"
      "v_mfma_f32_16x16x32_bf16 v[192:195], a[4:7], a[100:103], v[192:195]\n"
      "global_load_lds_dwordx4 v35, s[38:39] offset:3072\n"
      "v_mfma_f32_16x16x32_bf16 v[192:195], a[112:115], a[208:211], v[192:195]\n"
      "s_add_u32 s38, s38, 0x10000\n"
      "v_max_f32 v134, s94, v134\n"
      "v_max_f32 v135, s94, v135\n"
      "v_mfma_f32_16x16x32_bf16 v[192:195], a[116:119], a[212:215], v[192:195]\n"
      "s_addc_u32 s39, s39, 0\n"
      "v_add_f32 v132, v132, v48\n"
      "v_add_f32 v133, v133, v49\n"
      "v_mfma_f32_16x16x32_bf16 v[224:227], a[0:3], a[104:107], v[224:227]\n"
      "s_add_u32 m0, s99, 0x19000\n"
      "v_add_f32 v134, v134, v50\n"
      "v_add_f32 v135, v135, v51\n"
      "v_mfma_f32_16x16x32_bf16 v[224:227], a[4:7], a[108:111], v[224:227]\n"
      "v_cvt_pk_bf16_f32 v128, v128, v129\n"
      "v_cvt_pk_bf16_f32 v129, v130, v131\n"
      "v_mfma_f32_16x16x32_bf16 v[224:227], a[112:115], a[216:219], v[224:227]\n"
      "global_load_lds_dwordx4 v32, s[38:39] offset:0\n"
      "v_mfma_f32_16x16x32_bf16 v[224:227], a[116:119], a[220:223], v[224:227]\n"
      "v_cvt_pk_bf16_f32 v130, v132, v133\n"
      "v_cvt_pk_bf16_f32 v131, v134, v135\n"
      "v_mfma_f32_16x16x32_bf16 v[196:199], a[8:11], a[96:99], v[196:199]\n"
      "v_fmaak_f32 v52, v136, v25, 0xb5c1a2a0\n"
      "v_fmaak_f32 v53, v137, v25, 0xb5c1a2a0\n"
      "v_mfma_f32_16x16x32_bf16 v[196:199], a[12:15], a[100:103], v[196:199]\n"
      "v_exp_f32_e64 v52, v52 clamp\n"
      "v_mfma_f32_16x16x32_bf16 v[196:199], a[120:123], a[208:211], v[196:199]\n"
      "v_exp_f32_e64 v53, v53 clamp\n"
      "v_mfma_f32_16x16x32_bf16 v[196:199], a[124:127], a[212:215], v[196:199]\n"
      "global_load_lds_dwordx4 v33, s[38:39] offset:1024\n"
      "v_mfma_f32_16x16x32_bf16 v[228:231], a[8:11], a[104:107], v[228:231]\n"
      "v_fmaak_f32 v54, v138, v25, 0xb5c1a2a0\n"
      "v_fmaak_f32 v55, v139, v25, 0xb5c1a2a0\n"
      "v_mfma_f32_16x16x32_bf16 v[228:231], a[12:15], a[108:111], v[228:231]\n"
      "v_exp_f32_e64 v54, v54 clamp\n"
      "v_mfma_f32_16x16x32_bf16 v[228:231], a[120:123], a[216:219], v[228:231]\n"
      "v_exp_f32_e64 v55, v55 clamp\n"
      "v_mfma_f32_16x16x32_bf16 v[228:231], a[124:127], a[220:223], v[228:231]\n"
      "v_max_f32 v136, s94, v136\n"
      "v_max_f32 v137, s94, v137\n"
      "v_mfma_f32_16x16x32_bf16 v[200:203], a[16:19], a[96:99], v[200:203]\n"
      "global_load_lds_dwordx4 v34, s[38:39] offset:2048\n"
      "v_mfma_f32_16x16x32_bf16 v[200:203], a[20:23], a[100:103], v[200:203]\n"
      "v_max_f32 v138, s94, v138\n"
      "v_max_f32 v139, s94, v139\n"
      "v_mfma_f32_16x16x32_bf16 v[200:203], a[128:131], a[208:211], v[200:203]\n"
      "v_add_f32 v136, v136, v52\n"
      "v_add_f32 v137, v137, v53\n"
      "v_mfma_f32_16x16x32_bf16 v[200:203], a[132:135], a[212:215], v[200:203]\n"
      "v_add_f32 v138, v138, v54\n"
      "v_add_f32 v139, v139, v55\n"
      "v_mfma_f32_16x16x32_bf16 v[232:235], a[16:19], a[104:107], v[232:235]\n"
      "v_fmaak_f32 v44, v140, v25, 0xb5c1a2a0\n"
      "v_fmaak_f32 v45, v141, v25, 0xb5c1a2a0\n"
      "v_mfma_f32_16x16x32_bf16 v[232:235], a[20:23], a[108:111], v[232:235]\n"
      "global_load_lds_dwordx4 v35, s[38:39] offset:3072\n"
      "v_mfma_f32_16x16x32_bf16 v[232:235], a[128:131], a[216:219], v[232:235]\n"
      "s_add_u32 s3, s0, 2\n"
      "v_exp_f32_e64 v44, v44 clamp\n"
      "v_mfma_f32_16x16x32_bf16 v[232:235], a[132:135], a[220:223], v[232:235]\n"
      "s_and_b32 s3, s3, 7\n"
      "v_exp_f32_e64 v45, v45 clamp\n"
      "v_mfma_f32_16x16x32_bf16 v[204:207], a[24:27], a[96:99], v[204:207]\n"
      "s_lshl_b32 s3, s3, 18\n"
      "v_fmaak_f32 v46, v142, v25, 0xb5c1a2a0\n"
      "v_fmaak_f32 v47, v143, v25, 0xb5c1a2a0\n"
      "v_mfma_f32_16x16x32_bf16 v[204:207], a[28:31], a[100:103], v[204:207]\n"
      "s_add_u32 s38, s42, s3\n"
      "v_exp_f32_e64 v46, v46 clamp\n"
      "v_mfma_f32_16x16x32_bf16 v[204:207], a[136:139], a[208:211], v[204:207]\n"
      "s_addc_u32 s39, s43, 0\n"
      "v_exp_f32_e64 v47, v47 clamp\n"
      "v_mfma_f32_16x16x32_bf16 v[204:207], a[140:143], a[212:215], v[204:207]\n"
      "s_add_u32 m0, s99, 0x20000\n"
      "v_max_f32 v140, s94, v140\n"
      "v_max_f32 v141, s94, v141\n"
      "v_mfma_f32_16x16x32_bf16 v[236:239], a[24:27], a[104:107], v[236:239]\n"
      "global_load_lds_dwordx4 v32, s[38:39] offset:0\n"
      "v_mfma_f32_16x16x32_bf16 v[236:239], a[28:31], a[108:111], v[236:239]\n"
      "v_max_f32 v142, s94, v142\n"
      "v_max_f32 v143, s94, v143\n"
      "v_mfma_f32_16x16x32_bf16 v[236:239], a[136:139], a[216:219], v[236:239]\n"
      "v_add_f32 v140, v140, v44\n"
      "v_add_f32 v141, v141, v45\n"
      "v_mfma_f32_16x16x32_bf16 v[236:239], a[140:143], a[220:223], v[236:239]\n"
      "v_add_f32 v142, v142, v46\n"
      "v_add_f32 v143, v143, v47\n"
      "v_mfma_f32_16x16x32_bf16 v[208:211], a[32:35], a[96:99], v[208:211]\n"
      "v_cvt_pk_bf16_f32 v136, v136, v137\n"
      "v_cvt_pk_bf16_f32 v137, v138, v139\n"
      "v_mfma_f32_16x16x32_bf16 v[208:211], a[36:39], a[100:103], v[208:211]\n"
      "global_load_lds_dwordx4 v33, s[38:39] offset:1024\n"
      "v_mfma_f32_16x16x32_bf16 v[208:211], a[144:147], a[208:211], v[208:211]\n"
      "v_cvt_pk_bf16_f32 v138, v140, v141\n"
      "v_cvt_pk_bf16_f32 v139, v142, v143\n"
      "v_mfma_f32_16x16x32_bf16 v[208:211], a[148:151], a[212:215], v[208:211]\n"
      "v_fmaak_f32 v48, v144, v25, 0xb5c1a2a0\n"
      "v_fmaak_f32 v49, v145, v25, 0xb5c1a2a0\n"
      "v_mfma_f32_16x16x32_bf16 v[240:243], a[32:35], a[104:107], v[240:243]\n"
      "v_exp_f32_e64 v48, v48 clamp\n"
      "v_mfma_f32_16x16x32_bf16 v[240:243], a[36:39], a[108:111], v[240:243]\n"
      "v_exp_f32_e64 v49, v49 clamp\n"
      "v_mfma_f32_16x16x32_bf16 v[240:243], a[144:147], a[216:219], v[240:243]\n"
      "global_load_lds_dwordx4 v34, s[38:39] offset:2048\n"
      "v_mfma_f32_16x16x32_bf16 v[240:243], a[148:151], a[220:223], v[240:243]\n"
      "v_fmaak_f32 v50, v146, v25, 0xb5c1a2a0\n"
      "v_fmaak_f32 v51, v147, v25, 0xb5c1a2a0\n"
      "v_mfma_f32_16x16x32_bf16 v[212:215], a[40:43], a[96:99], v[212:215]\n"
      "v_exp_f32_e64 v50, v50 clamp\n"
      "v_mfma_f32_16x16x32_bf16 v[212:215], a[44:47], a[100:103], v[212:215]\n"
      "v_exp_f32_e64 v51, v51 clamp\n"
      "v_mfma_f32_16x16x32_bf16 v[212:215], a[152:155], a[208:211], v[212:215]\n"
      "v_max_f32 v144, s94, v144\n"
      "v_max_f32 v145, s94, v145\n"
      "v_mfma_f32_16x16x32_bf16 v[212:215], a[156:159], a[212:215], v[212:215]\n"
      "global_load_lds_dwordx4 v35, s[38:39] offset:3072\n"
      "v_mfma_f32_16x16x32_bf16 v[244:247], a[40:43], a[104:107], v[244:247]\n"
      "s_add_u32 s38, s38, 0x10000\n"
      "v_max_f32 v146, s94, v146\n"
      "v_max_f32 v147, s94, v147\n"
      "v_mfma_f32_16x16x32_bf16 v[244:247], a[44:47], a[108:111], v[244:247]\n"
      "s_addc_u32 s39, s39, 0\n"
      "v_add_f32 v144, v144, v48\n"
      "v_add_f32 v145, v145, v49\n"
      "v_mfma_f32_16x16x32_bf16 v[244:247], a[152:155], a[216:219], v[244:247]\n"
      "s_add_u32 m0, s99, 0x21000\n"
      "v_add_f32 v146, v146, v50\n"
      "v_add_f32 v147, v147, v51\n"
      "v_mfma_f32_16x16x32_bf16 v[244:247], a[156:159], a[220:223], v[244:247]\n"
      "v_fmaak_f32 v52, v148, v25, 0xb5c1a2a0\n"
      "v_fmaak_f32 v53, v149, v25, 0xb5c1a2a0\n"
      "v_mfma_f32_16x16x32_bf16 v[216:219], a[48:51], a[96:99], v[216:219]\n"
      "global_load_lds_dwordx4 v32, s[38:39] offset:0\n"
      "v_mfma_f32_16x16x32_bf16 v[216:219], a[52:55], a[100:103], v[216:219]\n"
      "v_exp_f32_e64 v52, v52 clamp\n"
      "v_mfma_f32_16x16x32_bf16 v[216:219], a[160:163], a[208:211], v[216:219]\n"
      "v_exp_f32_e64 v53, v53 clamp\n"
      "v_mfma_f32_16x16x32_bf16 v[216:219], a[164:167], a[212:215], v[216:219]\n"
      "v_fmaak_f32 v54, v150, v25, 0xb5c1a2a0\n"
      "v_fmaak_f32 v55, v151, v25, 0xb5c1a2a0\n"
      "v_mfma_f32_16x16x32_bf16 v[248:251], a[48:51], a[104:107], v[248:251]\n"
      "v_exp_f32_e64 v54, v54 clamp\n"
      "v_mfma_f32_16x16x32_bf16 v[248:251], a[52:55], a[108:111], v[248:251]\n"
      "global_load_lds_dwordx4 v33, s[38:39] offset:1024\n"
      "v_mfma_f32_16x16x32_bf16 v[248:251], a[160:163], a[216:219], v[248:251]\n"
      "v_exp_f32_e64 v55, v55 clamp\n"
      "v_mfma_f32_16x16x32_bf16 v[248:251], a[164:167], a[220:223], v[248:251]\n"
      "v_max_f32 v148, s94, v148\n"
      "v_max_f32 v149, s94, v149\n"
      "v_mfma_f32_16x16x32_bf16 v[220:223], a[56:59], a[96:99], v[220:223]\n"
      "v_max_f32 v150, s94, v150\n"
      "v_max_f32 v151, s94, v151\n"
      "v_mfma_f32_16x16x32_bf16 v[220:223], a[60:63], a[100:103], v[220:223]\n"
      "v_add_f32 v148, v148, v52\n"
      "v_add_f32 v149, v149, v53\n"
      "v_mfma_f32_16x16x32_bf16 v[220:223], a[168:171], a[208:211], v[220:223]\n"
      "global_load_lds_dwordx4 v34, s[38:39] offset:2048\n"
      "v_mfma_f32_16x16x32_bf16 v[220:223], a[172:175], a[212:215], v[220:223]\n"
      "v_add_f32 v150, v150, v54\n"
      "v_add_f32 v151, v151, v55\n"
      "v_mfma_f32_16x16x32_bf16 v[252:255], a[56:59], a[104:107], v[252:255]\n"
      "v_cvt_pk_bf16_f32 v144, v144, v145\n"
      "v_cvt_pk_bf16_f32 v145, v146, v147\n"
      "v_mfma_f32_16x16x32_bf16 v[252:255], a[60:63], a[108:111], v[252:255]\n"
      "v_cvt_pk_bf16_f32 v146, v148, v149\n"
      "v_cvt_pk_bf16_f32 v147, v150, v151\n"
      "v_mfma_f32_16x16x32_bf16 v[252:255], a[168:171], a[216:219], v[252:255]\n"
      "v_fmaak_f32 v44, v152, v25, 0xb5c1a2a0\n"
      "v_fmaak_f32 v45, v153, v25, 0xb5c1a2a0\n"
      "v_mfma_f32_16x16x32_bf16 v[252:255], a[172:175], a[220:223], v[252:255]\n"
      "global_load_lds_dwordx4 v35, s[38:39] offset:3072\n"
      "v_mfma_f32_16x16x32_bf16 v[160:163], a[0:3], a[88:91], v[160:163]\n"
      "s_add_u32 s3, s0, 3\n"
      "v_exp_f32_e64 v44, v44 clamp\n"
      "v_mfma_f32_16x16x32_bf16 v[160:163], a[4:7], a[92:95], v[160:163]\n"
      "s_and_b32 s3, s3, 7\n"
      "v_exp_f32_e64 v45, v45 clamp\n"
      "v_mfma_f32_16x16x32_bf16 v[160:163], a[112:115], a[200:203], v[160:163]\n"
      "s_lshl_b32 s3, s3, 18\n"
      "v_fmaak_f32 v46, v154, v25, 0xb5c1a2a0\n"
      "v_fmaak_f32 v47, v155, v25, 0xb5c1a2a0\n"
      "v_mfma_f32_16x16x32_bf16 v[160:163], a[116:119], a[204:207], v[160:163]\n"
      "s_add_u32 s38, s42, s3\n"
      "v_exp_f32_e64 v46, v46 clamp\n"
      "s_addc_u32 s39, s43, 0\n"
      "v_mfma_f32_16x16x32_bf16 v[164:167], a[8:11], a[88:91], v[164:167]\n"
      "s_add_u32 m0, s99, 0\n"
      "s_nop 0\n"
      "global_load_lds_dwordx4 v32, s[38:39] offset:0\n"
      "v_max_f32 v152, s94, v152\n"
      "v_max_f32 v153, s94, v153\n"
      "v_max_f32 v154, s94, v154\n"
      "v_exp_f32_e64 v47, v47 clamp\n"
      "v_max_f32 v155, s94, v155\n"
      "v_add_f32 v152, v152, v44\n"
      "v_mfma_f32_16x16x32_bf16 v[164:167], a[12:15], a[92:95], v[164:167]\n"
      "v_add_f32 v153, v153, v45\n"
      "v_add_f32 v154, v154, v46\n"
      "v_add_f32 v155, v155, v47\n"
      "v_fmaak_f32 v48, v156, v25, 0xb5c1a2a0\n"
      "v_fmaak_f32 v49, v157, v25, 0xb5c1a2a0\n"
      "v_fmaak_f32 v50, v158, v25, 0xb5c1a2a0\n"
      "v_mfma_f32_16x16x32_bf16 v[164:167], a[120:123], a[200:203], v[164:167]\n"
      "v_fmaak_f32 v51, v159, v25, 0xb5c1a2a0\n"
      "v_exp_f32_e64 v48, v48 clamp\n"
      "v_max_f32 v156, s94, v156\n"
      "v_exp_f32_e64 v49, v49 clamp\n"
      "v_mfma_f32_16x16x32_bf16 v[164:167], a[124:127], a[204:207], v[164:167]\n"
      "v_max_f32 v157, s94, v157\n"
      "v_exp_f32_e64 v50, v50 clamp\n"
      "v_max_f32 v158, s94, v158\n"
      "v_exp_f32_e64 v51, v51 clamp\n"
      "v_mfma_f32_16x16x32_bf16 v[168:171], a[16:19], a[88:91], v[168:171]\n"
      "v_max_f32 v159, s94, v159\n"
      "v_add_f32 v156, v156, v48\n"
      "v_add_f32 v157, v157, v49\n"
      "v_add_f32 v158, v158, v50\n"
      "v_add_f32 v159, v159, v51\n"
      "v_cvt_pk_bf16_f32 v152, v152, v153\n"
      "v_mfma_f32_16x16x32_bf16 v[168:171], a[20:23], a[92:95], v[168:171]\n"
      "v_cvt_pk_bf16_f32 v153, v154, v155\n"
      "v_cvt_pk_bf16_f32 v154, v156, v157\n"
      "v_cvt_pk_bf16_f32 v155, v158, v159\n"
      "v_cvt_pk_bf16_f32 v192, v192, v193\n"
      "v_cvt_pk_bf16_f32 v193, v194, v195\n"
      "v_cvt_pk_bf16_f32 v194, v196, v197\n"
      "v_mfma_f32_16x16x32_bf16 v[168:171], a[128:131], a[200:203], v[168:171]\n"
      "v_cvt_pk_bf16_f32 v195, v198, v199\n"
      "v_cvt_pk_bf16_f32 v224, v224, v225\n"
      "v_cvt_pk_bf16_f32 v225, v226, v227\n"
      "v_cvt_pk_bf16_f32 v226, v228, v229\n"
      "v_cvt_pk_bf16_f32 v227, v230, v231\n"
      "v_cvt_pk_bf16_f32 v200, v200, v201\n"
      "v_cvt_pk_bf16_f32 v201, v202, v203\n"
      "v_mfma_f32_16x16x32_bf16 v[168:171], a[132:135], a[204:207], v[168:171]\n"
      "v_cvt_pk_bf16_f32 v202, v204, v205\n"
      "v_cvt_pk_bf16_f32 v203, v206, v207\n"
      "v_cvt_pk_bf16_f32 v232, v232, v233\n"
      "v_cvt_pk_bf16_f32 v233, v234, v235\n"
      "v_cvt_pk_bf16_f32 v234, v236, v237\n"
      "v_cvt_pk_bf16_f32 v235, v238, v239\n"
      "v_mfma_f32_16x16x32_bf16 v[172:175], a[24:27], a[88:91], v[172:175]\n"
      "v_cvt_pk_bf16_f32 v208, v208, v209\n"
      "v_cvt_pk_bf16_f32 v209, v210, v211\n"
      "v_cvt_pk_bf16_f32 v210, v212, v213\n"
      "v_cvt_pk_bf16_f32 v211, v214, v215\n"
      "v_cvt_pk_bf16_f32 v240, v240, v241\n"
      "v_cvt_pk_bf16_f32 v241, v242, v243\n"
      "v_mfma_f32_16x16x32_bf16 v[172:175], a[28:31], a[92:95], v[172:175]\n"
      "v_cvt_pk_bf16_f32 v242, v244, v245\n"
      "v_cvt_pk_bf16_f32 v243, v246, v247\n"
      "v_xor_b32 v196, 0x3f803f80, v29\n"
      "v_xor_b32 v197, 0x3f803f80, v29\n"
      "v_xor_b32 v198, 0x3f803f80, v29\n"
      "v_xor_b32 v199, 0x3f803f80, v29\n"
      "v_mfma_f32_16x16x32_bf16 v[172:175], a[136:139], a[200:203], v[172:175]\n"
      "v_mov_b32 v204, v29\n"
      "v_mov_b32 v205, v29\n"
      "v_mov_b32 v206, v29\n"
      "v_mov_b32 v207, v29\n"
      "v_cvt_pk_bf16_f32 v216, v216, v217\n"
      "v_cvt_pk_bf16_f32 v217, v218, v219\n"
      "v_mfma_f32_16x16x32_bf16 v[172:175], a[140:143], a[204:207], v[172:175]\n"
      "v_cvt_pk_bf16_f32 v218, v220, v221\n"
      "v_cvt_pk_bf16_f32 v219, v222, v223\n"
      "v_cvt_pk_bf16_f32 v248, v248, v249\n"
      "v_cvt_pk_bf16_f32 v249, v250, v251\n"
      "v_cvt_pk_bf16_f32 v250, v252, v253\n"
      "v_cvt_pk_bf16_f32 v251, v254, v255\n"
      "v_mfma_f32_16x16x32_bf16 v[132:135], v[128:131], v[192:195], 0\n"
      "global_load_lds_dwordx4 v33, s[38:39] offset:1024\n"
      "v_fmaak_f32 v52, v160, v25, 0xb5c1a2a0\n"
      "v_fmaak_f32 v53, v161, v25, 0xb5c1a2a0\n"
      "v_fmaak_f32 v54, v162, v25, 0xb5c1a2a0\n"
      "v_fmaak_f32 v55, v163, v25, 0xb5c1a2a0\n"
      "v_exp_f32_e64 v52, v52 clamp\n"
      "v_max_f32 v160, s94, v160\n"
      "v_exp_f32_e64 v53, v53 clamp\n"
      "v_max_f32 v161, s94, v161\n"
      "v_exp_f32_e64 v54, v54 clamp\n"
      "v_mfma_f32_16x16x32_bf16 v[132:135], v[136:139], v[200:203], v[132:135]\n"
      "v_max_f32 v162, s94, v162\n"
      "v_exp_f32_e64 v55, v55 clamp\n"
      "v_max_f32 v163, s94, v163\n"
      "v_add_f32 v160, v160, v52\n"
      "v_add_f32 v161, v161, v53\n"
      "v_add_f32 v162, v162, v54\n"
      "v_add_f32 v163, v163, v55\n"
      "v_fmaak_f32 v44, v164, v25, 0xb5c1a2a0\n"
      "v_fmaak_f32 v45, v165, v25, 0xb5c1a2a0\n"
      "v_mfma_f32_16x16x32_bf16 v[132:135], v[144:147], v[208:211], v[132:135]\n"
      "v_fmaak_f32 v46, v166, v25, 0xb5c1a2a0\n"
      "v_fmaak_f32 v47, v167, v25, 0xb5c1a2a0\n"
      "v_exp_f32_e64 v44, v44 clamp\n"
      "v_max_f32 v164, s94, v164\n"
      "v_exp_f32_e64 v45, v45 clamp\n"
      "v_max_f32 v165, s94, v165\n"
      "v_exp_f32_e64 v46, v46 clamp\n"
      "v_max_f32 v166, s94, v166\n"
      "v_mfma_f32_16x16x32_bf16 v[132:135], v[152:155], v[216:219], v[132:135]\n"
      "v_exp_f32_e64 v47, v47 clamp\n"
      "v_max_f32 v167, s94, v167\n"
      "v_add_f32 v164, v164, v44\n"
      "v_add_f32 v165, v165, v45\n"
      "v_add_f32 v166, v166, v46\n"
      "v_add_f32 v167, v167, v47\n"
      "v_cvt_pk_bf16_f32 v160, v160, v161\n"
      "v_cvt_pk_bf16_f32 v161, v162, v163\n"
      "v_cvt_pk_bf16_f32 v162, v164, v165\n"
      "v_cvt_pk_bf16_f32 v163, v166, v167\n"
      "v_mfma_f32_16x16x32_bf16 v[176:179], a[32:35], a[88:91], v[176:179]\n"
      "global_load_lds_dwordx4 v34, s[38:39] offset:2048\n"
      "v_fmaak_f32 v48, v168, v25, 0xb5c1a2a0\n"
      "v_fmaak_f32 v49, v169, v25, 0xb5c1a2a0\n"
      "v_fmaak_f32 v50, v170, v25, 0xb5c1a2a0\n"
      "v_fmaak_f32 v51, v171, v25, 0xb5c1a2a0\n"
      "v_exp_f32_e64 v48, v48 clamp\n"
      "v_mfma_f32_16x16x32_bf16 v[176:179], a[36:39], a[92:95], v[176:179]\n"
      "v_max_f32 v168, s94, v168\n"
      "v_exp_f32_e64 v49, v49 clamp\n"
      "v_mfma_f32_16x16x32_bf16 v[176:179], a[144:147], a[200:203], v[176:179]\n"
      "v_max_f32 v169, s94, v169\n"
      "v_exp_f32_e64 v50, v50 clamp\n"
      "v_max_f32 v170, s94, v170\n"
      "v_exp_f32_e64 v51, v51 clamp\n"
      "v_mfma_f32_16x16x32_bf16 v[176:179], a[148:151], a[204:207], v[176:179]\n"
      "v_max_f32 v171, s94, v171\n"
      "v_bfi_b32 v132, s86, v26, v132\n"
      "v_bfi_b32 v134, s86, v26, v134\n"
      "v_mfma_f32_16x16x32_bf16 v[180:183], a[40:43], a[88:91], v[180:183]\n"
      "global_store_dwordx4 v24, v[132:135], s[50:51] offset:0 sc1\n"
      "v_mfma_f32_16x16x32_bf16 v[180:183], a[44:47], a[92:95], v[180:183]\n"
      "v_add_f32 v168, v168, v48\n"
      "v_add_f32 v169, v169, v49\n"
      "v_mfma_f32_16x16x32_bf16 v[180:183], a[152:155], a[200:203], v[180:183]\n"
      "v_add_f32 v170, v170, v50\n"
      "v_add_f32 v171, v171, v51\n"
      "v_fmaak_f32 v52, v172, v25, 0xb5c1a2a0\n"
      "v_fmaak_f32 v53, v173, v25, 0xb5c1a2a0\n"
      "v_mfma_f32_16x16x32_bf16 v[180:183], a[156:159], a[204:207], v[180:183]\n"
      "v_fmaak_f32 v54, v174, v25, 0xb5c1a2a0\n"
      "v_fmaak_f32 v55, v175, v25, 0xb5c1a2a0\n"
      "v_exp_f32_e64 v52, v52 clamp\n"
      "v_max_f32 v172, s94, v172\n"
      "v_mfma_f32_16x16x32_bf16 v[140:143], v[128:131], v[224:227], 0\n"
      "v_exp_f32_e64 v53, v53 clamp\n"
      "v_max_f32 v173, s94, v173\n"
      "v_exp_f32_e64 v54, v54 clamp\n"
      "v_mfma_f32_16x16x32_bf16 v[140:143], v[136:139], v[232:235], v[140:143]\n"
      "v_max_f32 v174, s94, v174\n"
      "v_exp_f32_e64 v55, v55 clamp\n"
      "v_max_f32 v175, s94, v175\n"
      "v_mfma_f32_16x16x32_bf16 v[140:143], v[144:147], v[240:243], v[140:143]\n"
      "v_add_f32 v172, v172, v52\n"
      "v_add_f32 v173, v173, v53\n"
      "v_add_f32 v174, v174, v54\n"
      "v_add_f32 v175, v175, v55\n"
      "v_mfma_f32_16x16x32_bf16 v[140:143], v[152:155], v[248:251], v[140:143]\n"
      "v_cvt_pk_bf16_f32 v168, v168, v169\n"
      "v_cvt_pk_bf16_f32 v169, v170, v171\n"
      "v_cvt_pk_bf16_f32 v170, v172, v173\n"
      "v_cvt_pk_bf16_f32 v171, v174, v175\n"
      "v_mfma_f32_16x16x32_bf16 v[184:187], a[48:51], a[88:91], v[184:187]\n"
      "global_load_lds_dwordx4 v35, s[38:39] offset:3072\n"
      "v_fmaak_f32 v44, v176, v25, 0xb5c1a2a0\n"
      "v_fmaak_f32 v45, v177, v25, 0xb5c1a2a0\n"
      "v_fmaak_f32 v46, v178, v25, 0xb5c1a2a0\n"
      "v_fmaak_f32 v47, v179, v25, 0xb5c1a2a0\n"
      "v_exp_f32_e64 v44, v44 clamp\n"
      "v_mfma_f32_16x16x32_bf16 v[184:187], a[52:55], a[92:95], v[184:187]\n"
      "v_max_f32 v176, s94, v176\n"
      "v_exp_f32_e64 v45, v45 clamp\n"
      "v_mfma_f32_16x16x32_bf16 v[184:187], a[160:163], a[200:203], v[184:187]\n"
      "v_max_f32 v177, s94, v177\n"
      "v_exp_f32_e64 v46, v46 clamp\n"
      "v_max_f32 v178, s94, v178\n"
      "v_exp_f32_e64 v47, v47 clamp\n"
      "v_mfma_f32_16x16x32_bf16 v[184:187], a[164:167], a[204:207], v[184:187]\n"
      "v_max_f32 v179, s94, v179\n"
      "v_bfi_b32 v140, s86, v26, v140\n"
      "v_bfi_b32 v142, s86, v26, v142\n"
      "v_mfma_f32_16x16x32_bf16 v[188:191], a[56:59], a[88:91], v[188:191]\n"
      "global_store_dwordx4 v24, v[140:143], s[50:51] offset:2048 sc1\n"
      "v_mfma_f32_16x16x32_bf16 v[188:191], a[60:63], a[92:95], v[188:191]\n"
      "v_add_f32 v176, v176, v44\n"
      "v_add_f32 v177, v177, v45\n"
      "v_mfma_f32_16x16x32_bf16 v[188:191], a[168:171], a[200:203], v[188:191]\n"
      "v_add_f32 v178, v178, v46\n"
      "v_add_f32 v179, v179, v47\n"
      "v_fmaak_f32 v48, v180, v25, 0xb5c1a2a0\n"
      "v_fmaak_f32 v49, v181, v25, 0xb5c1a2a0\n"
      "v_mfma_f32_16x16x32_bf16 v[188:191], a[172:175], a[204:207], v[188:191]\n"
      "v_fmaak_f32 v50, v182, v25, 0xb5c1a2a0\n"
      "v_fmaak_f32 v51, v183, v25, 0xb5c1a2a0\n"
      "v_exp_f32_e64 v48, v48 clamp\n"
      ".Lgm_w6:\n"
      "s_cbranch_execz .Lgm_r1\n"
      "v_max_f32 v180, s94, v180\n"
      "v_mfma_f32_16x16x32_bf16 v[212:215], v[128:131], v[196:199], 0\n"
      "v_exp_f32_e64 v49, v49 clamp\n"
      "v_max_f32 v181, s94, v181\n"
      "v_exp_f32_e64 v50, v50 clamp\n"
      "v_mfma_f32_16x16x32_bf16 v[212:215], v[136:139], v[196:199], v[212:215]\n"
      "v_max_f32 v182, s94, v182\n"
      "v_exp_f32_e64 v51, v51 clamp\n"
      "v_max_f32 v183, s94, v183\n"
      "v_mfma_f32_16x16x32_bf16 v[212:215], v[144:147], v[196:199], v[212:215]\n"
      "v_add_f32 v180, v180, v48\n"
      "v_add_f32 v181, v181, v49\n"
      "v_add_f32 v182, v182, v50\n"
      "v_add_f32 v183, v183, v51\n"
      "v_mfma_f32_16x16x32_bf16 v[212:215], v[152:155], v[196:199], v[212:215]\n"
      "v_cvt_pk_bf16_f32 v176, v176, v177\n"
      "v_cvt_pk_bf16_f32 v177, v178, v179\n"
      "v_cvt_pk_bf16_f32 v178, v180, v181\n"
      "v_cvt_pk_bf16_f32 v179, v182, v183\n"
      "v_mfma_f32_16x16x32_bf16 v[148:151], v[160:163], v[192:195], 0\n"
      "s_add_u32 s38, s38, 0x10000\n"
      "s_addc_u32 s39, s39, 0\n"
      "s_add_u32 m0, s99, 0x1000\n"
      "s_nop 0\n"
      "global_load_lds_dwordx4 v32, s[38:39] offset:0\n"
      "v_fmaak_f32 v52, v184, v25, 0xb5c1a2a0\n"
      "v_fmaak_f32 v53, v185, v25, 0xb5c1a2a0\n"
      "v_fmaak_f32 v54, v186, v25, 0xb5c1a2a0\n"
      "v_fmaak_f32 v55, v187, v25, 0xb5c1a2a0\n"
      "v_exp_f32_e64 v52, v52 clamp\n"
      "v_mfma_f32_16x16x32_bf16 v[156:159], v[160:163], v[224:227], 0\n"
      "v_max_f32 v184, s94, v184\n"
      "v_exp_f32_e64 v53, v53 clamp\n"
      "v_max_f32 v185, s94, v185\n"
      "v_mfma_f32_16x16x32_bf16 v[212:215], v[160:163], v[204:207], v[212:215]\n"
      "v_exp_f32_e64 v54, v54 clamp\n"
      "v_max_f32 v186, s94, v186\n"
      "v_exp_f32_e64 v55, v55 clamp\n"
      "v_mfma_f32_16x16x32_bf16 v[148:151], v[168:171], v[200:203], v[148:151]\n"
      "v_max_f32 v187, s94, v187\n"
      "v_add_f32 v184, v184, v52\n"
      "v_add_f32 v185, v185, v53\n"
      "v_add_f32 v186, v186, v54\n"
      "v_add_f32 v187, v187, v55\n"
      "v_mfma_f32_16x16x32_bf16 v[156:159], v[168:171], v[232:235], v[156:159]\n"
      "v_fmaak_f32 v44, v188, v25, 0xb5c1a2a0\n"
      "v_fmaak_f32 v45, v189, v25, 0xb5c1a2a0\n"
      "v_fmaak_f32 v46, v190, v25, 0xb5c1a2a0\n"
      "v_fmaak_f32 v47, v191, v25, 0xb5c1a2a0\n"
      "v_exp_f32_e64 v44, v44 clamp\n"
      "v_mfma_f32_16x16x32_bf16 v[212:215], v[168:171], v[204:207], v[212:215]\n"
      "v_max_f32 v188, s94, v188\n"
      "v_exp_f32_e64 v45, v45 clamp\n"
      "v_max_f32 v189, s94, v189\n"
      "v_mfma_f32_16x16x32_bf16 v[148:151], v[176:179], v[208:211], v[148:151]\n"
      "v_exp_f32_e64 v46, v46 clamp\n"
      "v_max_f32 v190, s94, v190\n"
      "v_exp_f32_e64 v47, v47 clamp\n"
      "v_mfma_f32_16x16x32_bf16 v[156:159], v[176:179], v[240:243], v[156:159]\n"
      "v_max_f32 v191, s94, v191\n"
      "v_add_f32 v188, v188, v44\n"
      "v_add_f32 v189, v189, v45\n"
      "v_add_f32 v190, v190, v46\n"
      "v_add_f32 v191, v191, v47\n"
      "v_mfma_f32_16x16x32_bf16 v[212:215], v[176:179], v[204:207], v[212:215]\n"
      "v_cvt_pk_bf16_f32 v184, v184, v185\n"
      "v_cvt_pk_bf16_f32 v185, v186, v187\n"
      "v_cvt_pk_bf16_f32 v186, v188, v189\n"
      "v_cvt_pk_bf16_f32 v187, v190, v191\n"
      "s_nop 1\n"
      "v_mfma_f32_16x16x32_bf16 v[148:151], v[184:187], v[216:219], v[148:151]\n"
      "v_mfma_f32_16x16x32_bf16 v[156:159], v[184:187], v[248:251], v[156:159]\n"
      "v_mfma_f32_16x16x32_bf16 v[212:215], v[184:187], v[204:207], v[212:215]\n"
      "s_nop 5\n"
      "v_bfi_b32 v148, s86, v26, v148\n"
      "v_bfi_b32 v150, s86, v26, v150\n"
      "global_store_dwordx4 v24, v[148:151], s[50:51] offset:1024 sc1\n"
      "v_bfi_b32 v156, s86, v26, v156\n"
      "v_bfi_b32 v158, s86, v26, v158\n"
      "global_store_dwordx4 v24, v[156:159], s[50:51] offset:3072 sc1\n"
      "v_bfi_b32 v212, s86, v26, v212\n"
      "v_bfi_b32 v213, s86, v26, v213\n"
      "v_bfi_b32 v214, s86, v26, v214\n"
      "v_bfi_b32 v215, s86, v26, v215\n"
      "s_mov_b32 exec_lo, 0x1010101\n"
      "s_mov_b32 exec_hi, 0x1010101\n"
      "global_store_dwordx4 v41, v[212:215], s[52:53] offset:0 sc1\n"
      "s_mov_b64 exec, -1\n"
      "v_mfma_f32_16x16x32_bf16 v[64:67], a[64:67], a[0:3], v[64:67]\n"
      "v_mfma_f32_16x16x32_bf16 v[64:67], a[68:71], a[4:7], v[64:67]\n"
      "v_mfma_f32_16x16x32_bf16 v[64:67], a[176:179], a[112:115], v[64:67]\n"
      "v_mfma_f32_16x16x32_bf16 v[64:67], a[180:183], a[116:119], v[64:67]\n"
      "v_mfma_f32_16x16x32_bf16 v[96:99], a[72:75], a[0:3], v[96:99]\n"
      "v_mfma_f32_16x16x32_bf16 v[96:99], a[76:79], a[4:7], v[96:99]\n"
      "v_mfma_f32_16x16x32_bf16 v[96:99], a[184:187], a[112:115], v[96:99]\n"
      "v_mfma_f32_16x16x32_bf16 v[96:99], a[188:191], a[116:119], v[96:99]\n"
      "v_mfma_f32_16x16x32_bf16 v[68:71], a[64:67], a[8:11], v[68:71]\n"
      "global_load_lds_dwordx4 v33, s[38:39] offset:1024\n"
      "s_nop 1\n"
      "v_fmaak_f32 v48, v64, v25, 0xb5c1a2a0\n"
      "v_fmaak_f32 v49, v65, v25, 0xb5c1a2a0\n"
      "v_fmaak_f32 v50, v66, v25, 0xb5c1a2a0\n"
      "v_fmaak_f32 v51, v67, v25, 0xb5c1a2a0\n"
      "v_exp_f32_e64 v48, v48 clamp\n"
      "v_mfma_f32_16x16x32_bf16 v[68:71], a[68:71], a[12:15], v[68:71]\n"
      "v_max_f32 v64, s94, v64\n"
      "v_exp_f32_e64 v49, v49 clamp\n"
      "v_max_f32 v65, s94, v65\n"
      "v_exp_f32_e64 v50, v50 clamp\n"
      "v_max_f32 v66, s94, v66\n"
      "v_mfma_f32_16x16x32_bf16 v[68:71], a[176:179], a[120:123], v[68:71]\n"
      "v_exp_f32_e64 v51, v51 clamp\n"
      "v_max_f32 v67, s94, v67\n"
      "v_add_f32 v64, v64, v48\n"
      "v_add_f32 v65, v65, v49\n"
      "v_add_f32 v66, v66, v50\n"
      "v_mfma_f32_16x16x32_bf16 v[68:71], a[180:183], a[124:127], v[68:71]\n"
      "global_load_lds_dwordx4 v34, s[38:39] offset:2048\n"
      "v_add_f32 v67, v67, v51\n"
      "v_fmaak_f32 v52, v96, v25, 0xb5c1a2a0\n"
      "v_fmaak_f32 v53, v97, v25, 0xb5c1a2a0\n"
      "v_fmaak_f32 v54, v98, v25, 0xb5c1a2a0\n"
      "v_fmaak_f32 v55, v99, v25, 0xb5c1a2a0\n"
      "v_mfma_f32_16x16x32_bf16 v[100:103], a[72:75], a[8:11], v[100:103]\n"
      "v_exp_f32_e64 v52, v52 clamp\n"
      "v_max_f32 v96, s94, v96\n"
      "v_exp_f32_e64 v53, v53 clamp\n"
      "v_max_f32 v97, s94, v97\n"
      "v_exp_f32_e64 v54, v54 clamp\n"
      "v_mfma_f32_16x16x32_bf16 v[100:103], a[76:79], a[12:15], v[100:103]\n"
      "v_max_f32 v98, s94, v98\n"
      "v_exp_f32_e64 v55, v55 clamp\n"
      "v_max_f32 v99, s94, v99\n"
      "v_add_f32 v96, v96, v52\n"
      "v_add_f32 v97, v97, v53\n"
      "v_mfma_f32_16x16x32_bf16 v[100:103], a[184:187], a[120:123], v[100:103]\n"
      "global_load_lds_dwordx4 v35, s[38:39] offset:3072\n"
      "v_add_f32 v98, v98, v54\n"
      "v_add_f32 v99, v99, v55\n"
      "v_cvt_pk_bf16_f32 v64, v64, v65\n"
      "v_cvt_pk_bf16_f32 v65, v66, v67\n"
      "v_cvt_pk_bf16_f32 v66, v96, v97\n"
      "v_mfma_f32_16x16x32_bf16 v[100:103], a[188:191], a[124:127], v[100:103]\n"
      "v_cvt_pk_bf16_f32 v67, v98, v99\n"
      "v_mfma_f32_16x16x32_bf16 v[72:75], a[64:67], a[16:19], v[72:75]\n"
      "s_add_u32 s3, s0, 4\n"
      "s_and_b32 s3, s3, 7\n"
      "s_lshl_b32 s3, s3, 18\n"
      "s_add_u32 s38, s42, s3\n"
      "s_addc_u32 s39, s43, 0\n"
      "s_add_u32 m0, s99, 0x8000\n"
      "s_nop 0\n"
      "global_load_lds_dwordx4 v32, s[38:39] offset:0\n"
      "v_fmaak_f32 v44, v68, v25, 0xb5c1a2a0\n"
      "v_fmaak_f32 v45, v69, v25, 0xb5c1a2a0\n"
      "v_fmaak_f32 v46, v70, v25, 0xb5c1a2a0\n"
      "v_fmaak_f32 v47, v71, v25, 0xb5c1a2a0\n"
      "v_exp_f32_e64 v44, v44 clamp\n"
      "v_mfma_f32_16x16x32_bf16 v[72:75], a[68:71], a[20:23], v[72:75]\n"
      "v_max_f32 v68, s94, v68\n"
      "v_exp_f32_e64 v45, v45 clamp\n"
      "v_max_f32 v69, s94, v69\n"
      "v_exp_f32_e64 v46, v46 clamp\n"
      "v_max_f32 v70, s94, v70\n"
      "v_mfma_f32_16x16x32_bf16 v[72:75], a[176:179], a[128:131], v[72:75]\n"
      "v_exp_f32_e64 v47, v47 clamp\n"
      "v_max_f32 v71, s94, v71\n"
      "v_add_f32 v68, v68, v44\n"
      "v_add_f32 v69, v69, v45\n"
      "v_add_f32 v70, v70, v46\n"
      "v_mfma_f32_16x16x32_bf16 v[72:75], a[180:183], a[132:135], v[72:75]\n"
      "global_load_lds_dwordx4 v33, s[38:39] offset:1024\n"
      "v_add_f32 v71, v71, v47\n"
      "v_fmaak_f32 v48, v100, v25, 0xb5c1a2a0\n"
      "v_fmaak_f32 v49, v101, v25, 0xb5c1a2a0\n"
      "v_fmaak_f32 v50, v102, v25, 0xb5c1a2a0\n"
      "v_fmaak_f32 v51, v103, v25, 0xb5c1a2a0\n"
      "v_mfma_f32_16x16x32_bf16 v[104:107], a[72:75], a[16:19], v[104:107]\n"
      "v_exp_f32_e64 v48, v48 clamp\n"
      "v_max_f32 v100, s94, v100\n"
      "v_exp_f32_e64 v49, v49 clamp\n"
      "v_max_f32 v101, s94, v101\n"
      "v_exp_f32_e64 v50, v50 clamp\n"
      "v_mfma_f32_16x16x32_bf16 v[104:107], a[76:79], a[20:23], v[104:107]\n"
      "v_max_f32 v102, s94, v102\n"
      "v_exp_f32_e64 v51, v51 clamp\n"
      "v_max_f32 v103, s94, v103\n"
      "v_add_f32 v100, v100, v48\n"
      "v_add_f32 v101, v101, v49\n"
      "v_mfma_f32_16x16x32_bf16 v[104:107], a[184:187], a[128:131], v[104:107]\n"
      "global_load_lds_dwordx4 v34, s[38:39] offset:2048\n"
      "v_add_f32 v102, v102, v50\n"
      "v_add_f32 v103, v103, v51\n"
      "v_cvt_pk_bf16_f32 v68, v68, v69\n"
      "v_cvt_pk_bf16_f32 v69, v70, v71\n"
      "v_cvt_pk_bf16_f32 v70, v100, v101\n"
      "v_mfma_f32_16x16x32_bf16 v[104:107], a[188:191], a[132:135], v[104:107]\n"
      "v_cvt_pk_bf16_f32 v71, v102, v103\n"
      "v_mfma_f32_16x16x32_bf16 v[76:79], a[64:67], a[24:27], v[76:79]\n"
      "global_load_lds_dwordx4 v35, s[38:39] offset:3072\n"
      "v_fmaak_f32 v52, v72, v25, 0xb5c1a2a0\n"
      "v_fmaak_f32 v53, v73, v25, 0xb5c1a2a0\n"
      "v_fmaak_f32 v54, v74, v25, 0xb5c1a2a0\n"
      "v_fmaak_f32 v55, v75, v25, 0xb5c1a2a0\n"
      "v_exp_f32_e64 v52, v52 clamp\n"
      "v_mfma_f32_16x16x32_bf16 v[76:79], a[68:71], a[28:31], v[76:79]\n"
      "v_max_f32 v72, s94, v72\n"
      "v_exp_f32_e64 v53, v53 clamp\n"
      "v_max_f32 v73, s94, v73\n"
      "v_exp_f32_e64 v54, v54 clamp\n"
      "v_max_f32 v74, s94, v74\n"
      "v_mfma_f32_16x16x32_bf16 v[76:79], a[176:179], a[136:139], v[76:79]\n"
      "v_exp_f32_e64 v55, v55 clamp\n"
      "v_max_f32 v75, s94, v75\n"
      "v_add_f32 v72, v72, v52\n"
      "v_add_f32 v73, v73, v53\n"
      "v_add_f32 v74, v74, v54\n"
      "v_mfma_f32_16x16x32_bf16 v[76:79], a[180:183], a[140:143], v[76:79]\n"
      "s_add_u32 s38, s38, 0x10000\n"
      "s_addc_u32 s39, s39, 0\n"
      "s_add_u32 m0, s99, 0x9000\n"
      "s_nop 0\n"
      "global_load_lds_dwordx4 v32, s[38:39] offset:0\n"
      "v_add_f32 v75, v75, v55\n"
      "v_fmaak_f32 v44, v104, v25, 0xb5c1a2a0\n"
      "v_fmaak_f32 v45, v105, v25, 0xb5c1a2a0\n"
      "v_fmaak_f32 v46, v106, v25, 0xb5c1a2a0\n"
      "v_fmaak_f32 v47, v107, v25, 0xb5c1a2a0\n"
      "v_mfma_f32_16x16x32_bf16 v[108:111], a[72:75], a[24:27], v[108:111]\n"
      "v_exp_f32_e64 v44, v44 clamp\n"
      "v_max_f32 v104, s94, v104\n"
      "v_exp_f32_e64 v45, v45 clamp\n"
      "v_max_f32 v105, s94, v105\n"
      "v_exp_f32_e64 v46, v46 clamp\n"
      "v_mfma_f32_16x16x32_bf16 v[108:111], a[76:79], a[28:31], v[108:111]\n"
      "v_max_f32 v106, s94, v106\n"
      "v_exp_f32_e64 v47, v47 clamp\n"
      "v_max_f32 v107, s94, v107\n"
      "v_add_f32 v104, v104, v44\n"
      "v_add_f32 v105, v105, v45\n"
      "v_mfma_f32_16x16x32_bf16 v[108:111], a[184:187], a[136:139], v[108:111]\n"
      "global_load_lds_dwordx4 v33, s[38:39] offset:1024\n"
      "v_add_f32 v106, v106, v46\n"
      "v_add_f32 v107, v107, v47\n"
      "v_cvt_pk_bf16_f32 v72, v72, v73\n"
      "v_cvt_pk_bf16_f32 v73, v74, v75\n"
      "v_cvt_pk_bf16_f32 v74, v104, v105\n"
      "v_mfma_f32_16x16x32_bf16 v[108:111], a[188:191], a[140:143], v[108:111]\n"
      "v_cvt_pk_bf16_f32 v75, v106, v107\n"
      "v_mfma_f32_16x16x32_bf16 v[80:83], a[64:67], a[32:35], v[80:83]\n"
      "global_load_lds_dwordx4 v34, s[38:39] offset:2048\n"
      "v_fmaak_f32 v48, v76, v25, 0xb5c1a2a0\n"
      "v_fmaak_f32 v49, v77, v25, 0xb5c1a2a0\n"
      "v_fmaak_f32 v50, v78, v25, 0xb5c1a2a0\n"
      "v_fmaak_f32 v51, v79, v25, 0xb5c1a2a0\n"
      "v_exp_f32_e64 v48, v48 clamp\n"
      "v_mfma_f32_16x16x32_bf16 v[80:83], a[68:71], a[36:39], v[80:83]\n"
      "v_max_f32 v76, s94, v76\n"
      "v_exp_f32_e64 v49, v49 clamp\n"
      "v_max_f32 v77, s94, v77\n"
      "v_exp_f32_e64 v50, v50 clamp\n"
      "v_max_f32 v78, s94, v78\n"
      "v_mfma_f32_16x16x32_bf16 v[80:83], a[176:179], a[144:147], v[80:83]\n"
      "v_exp_f32_e64 v51, v51 clamp\n"
      "global_load_dwordx4 v[208:211], v37, s[54:55] offset:0 sc1\n"
      "global_load_dwordx4 v[192:195], v36, s[78:79] offset:0 sc1\n"
      "global_load_dwordx4 v[196:199], v36, s[80:81] offset:0 sc1\n"
      "global_load_dwordx4 v[200:203], v36, s[82:83] offset:0 sc1\n"
      "global_load_dwordx4 v[204:207], v36, s[84:85] offset:0 sc1\n"
      "v_max_f32 v79, s94, v79\n"
      "v_add_f32 v76, v76, v48\n"
      "v_add_f32 v77, v77, v49\n"
      "v_add_f32 v78, v78, v50\n"
      "v_mfma_f32_16x16x32_bf16 v[80:83], a[180:183], a[148:151], v[80:83]\n"
      "global_load_lds_dwordx4 v35, s[38:39] offset:3072\n"
      "v_add_f32 v79, v79, v51\n"
      "v_fmaak_f32 v52, v108, v25, 0xb5c1a2a0\n"
      "v_fmaak_f32 v53, v109, v25, 0xb5c1a2a0\n"
      "v_fmaak_f32 v54, v110, v25, 0xb5c1a2a0\n"
      "v_fmaak_f32 v55, v111, v25, 0xb5c1a2a0\n"
      "v_mfma_f32_16x16x32_bf16 v[112:115], a[72:75], a[32:35], v[112:115]\n"
      "v_exp_f32_e64 v52, v52 clamp\n"
      "v_max_f32 v108, s94, v108\n"
      "v_exp_f32_e64 v53, v53 clamp\n"
      "v_max_f32 v109, s94, v109\n"
      "v_exp_f32_e64 v54, v54 clamp\n"
      "v_mfma_f32_16x16x32_bf16 v[112:115], a[76:79], a[36:39], v[112:115]\n"
      "v_max_f32 v110, s94, v110\n"
      "v_exp_f32_e64 v55, v55 clamp\n"
      "v_max_f32 v111, s94, v111\n"
      "v_add_f32 v108, v108, v52\n"
      "v_add_f32 v109, v109, v53\n"
      "v_mfma_f32_16x16x32_bf16 v[112:115], a[184:187], a[144:147], v[112:115]\n"
      "global_load_dwordx4 v[8:11], v24, s[26:27] offset:0\n"
      "v_add_f32 v110, v110, v54\n"
      "v_add_f32 v111, v111, v55\n"
      "v_cvt_pk_bf16_f32 v76, v76, v77\n"
      "v_cvt_pk_bf16_f32 v77, v78, v79\n"
      "v_cvt_pk_bf16_f32 v78, v108, v109\n"
      "v_mfma_f32_16x16x32_bf16 v[112:115], a[188:191], a[148:151], v[112:115]\n"
      "v_cvt_pk_bf16_f32 v79, v110, v111\n"
      "v_mfma_f32_16x16x32_bf16 v[84:87], a[64:67], a[40:43], v[84:87]\n"
      "global_load_dwordx4 v[12:15], v24, s[26:27] offset:1024\n"
      "v_fmaak_f32 v44, v80, v25, 0xb5c1a2a0\n"
      "v_fmaak_f32 v45, v81, v25, 0xb5c1a2a0\n"
      "v_fmaak_f32 v46, v82, v25, 0xb5c1a2a0\n"
      "v_fmaak_f32 v47, v83, v25, 0xb5c1a2a0\n"
      "v_exp_f32_e64 v44, v44 clamp\n"
      "v_mfma_f32_16x16x32_bf16 v[84:87], a[68:71], a[44:47], v[84:87]\n"
      "v_max_f32 v80, s94, v80\n"
      "v_exp_f32_e64 v45, v45 clamp\n"
      "v_max_f32 v81, s94, v81\n"
      "v_exp_f32_e64 v46, v46 clamp\n"
      "v_max_f32 v82, s94, v82\n"
      "v_mfma_f32_16x16x32_bf16 v[84:87], a[176:179], a[152:155], v[84:87]\n"
      "v_exp_f32_e64 v47, v47 clamp\n"
      "v_max_f32 v83, s94, v83\n"
      "v_add_f32 v80, v80, v44\n"
      "v_add_f32 v81, v81, v45\n"
      "v_add_f32 v82, v82, v46\n"
      "v_mfma_f32_16x16x32_bf16 v[84:87], a[180:183], a[156:159], v[84:87]\n"
      "v_add_f32 v83, v83, v47\n"
      "v_fmaak_f32 v48, v112, v25, 0xb5c1a2a0\n"
      "v_fmaak_f32 v49, v113, v25, 0xb5c1a2a0\n"
      "v_fmaak_f32 v50, v114, v25, 0xb5c1a2a0\n"
      "v_fmaak_f32 v51, v115, v25, 0xb5c1a2a0\n"
      "v_mfma_f32_16x16x32_bf16 v[116:119], a[72:75], a[40:43], v[116:119]\n"
      "v_exp_f32_e64 v48, v48 clamp\n"
      "v_max_f32 v112, s94, v112\n"
      "v_exp_f32_e64 v49, v49 clamp\n"
      "v_max_f32 v113, s94, v113\n"
      "v_exp_f32_e64 v50, v50 clamp\n"
      "v_mfma_f32_16x16x32_bf16 v[116:119], a[76:79], a[44:47], v[116:119]\n"
      "v_max_f32 v114, s94, v114\n"
      "v_exp_f32_e64 v51, v51 clamp\n"
      "v_max_f32 v115, s94, v115\n"
      "v_add_f32 v112, v112, v48\n"
      "v_add_f32 v113, v113, v49\n"
      "v_mfma_f32_16x16x32_bf16 v[116:119], a[184:187], a[152:155], v[116:119]\n"
      "v_add_f32 v114, v114, v50\n"
      "v_add_f32 v115, v115, v51\n"
      "v_cvt_pk_bf16_f32 v80, v80, v81\n"
      "v_cvt_pk_bf16_f32 v81, v82, v83\n"
      "v_cvt_pk_bf16_f32 v82, v112, v113\n"
      "v_mfma_f32_16x16x32_bf16 v[116:119], a[188:191], a[156:159], v[116:119]\n"
      "v_cvt_pk_bf16_f32 v83, v114, v115\n"
      "v_mfma_f32_16x16x32_bf16 v[88:91], a[64:67], a[48:51], v[88:91]\n"
      "v_fmaak_f32 v52, v84, v25, 0xb5c1a2a0\n"
      "v_fmaak_f32 v53, v85, v25, 0xb5c1a2a0\n"
      "v_fmaak_f32 v54, v86, v25, 0xb5c1a2a0\n"
      "v_fmaak_f32 v55, v87, v25, 0xb5c1a2a0\n"
      "v_exp_f32_e64 v52, v52 clamp\n"
      "v_mfma_f32_16x16x32_bf16 v[88:91], a[68:71], a[52:55], v[88:91]\n"
      "v_max_f32 v84, s94, v84\n"
      "v_exp_f32_e64 v53, v53 clamp\n"
      "v_max_f32 v85, s94, v85\n"
      "v_exp_f32_e64 v54, v54 clamp\n"
      "v_max_f32 v86, s94, v86\n"
      "v_mfma_f32_16x16x32_bf16 v[88:91], a[176:179], a[160:163], v[88:91]\n"
      "v_exp_f32_e64 v55, v55 clamp\n"
      "v_max_f32 v87, s94, v87\n"
      "v_add_f32 v84, v84, v52\n"
      "v_add_f32 v85, v85, v53\n"
      "v_add_f32 v86, v86, v54\n"
      "v_mfma_f32_16x16x32_bf16 v[88:91], a[180:183], a[164:167], v[88:91]\n"
      "v_add_f32 v87, v87, v55\n"
      "v_fmaak_f32 v44, v116, v25, 0xb5c1a2a0\n"
      "v_fmaak_f32 v45, v117, v25, 0xb5c1a2a0\n"
      "v_fmaak_f32 v46, v118, v25, 0xb5c1a2a0\n"
      "v_fmaak_f32 v47, v119, v25, 0xb5c1a2a0\n"
      "v_mfma_f32_16x16x32_bf16 v[120:123], a[72:75], a[48:51], v[120:123]\n"
      "v_exp_f32_e64 v44, v44 clamp\n"
      "v_max_f32 v116, s94, v116\n"
      "v_exp_f32_e64 v45, v45 clamp\n"
      "v_max_f32 v117, s94, v117\n"
      "v_exp_f32_e64 v46, v46 clamp\n"
      "v_mfma_f32_16x16x32_bf16 v[120:123], a[76:79], a[52:55], v[120:123]\n"
      "v_max_f32 v118, s94, v118\n"
      "v_exp_f32_e64 v47, v47 clamp\n"
      "v_max_f32 v119, s94, v119\n"
      "v_add_f32 v116, v116, v44\n"
      "v_add_f32 v117, v117, v45\n"
      "v_mfma_f32_16x16x32_bf16 v[120:123], a[184:187], a[160:163], v[120:123]\n"
      "v_add_f32 v118, v118, v46\n"
      "v_add_f32 v119, v119, v47\n"
      "v_cvt_pk_bf16_f32 v84, v84, v85\n"
      "v_cvt_pk_bf16_f32 v85, v86, v87\n"
      "s_waitcnt vmcnt(3)\n"
      "v_xor_b32 v192, s88, v192\n"
      "v_xor_b32 v194, s88, v194\n"
      "v_xor_b32 v196, s88, v196\n"
      "v_xor_b32 v198, s88, v198\n"
      "v_xor_b32 v200, s88, v200\n"
      "v_xor_b32 v202, s88, v202\n"
      "v_xor_b32 v204, s88, v204\n"
      "v_xor_b32 v206, s88, v206\n"
      "v_xor_b32 v208, s88, v208\n"
      "v_xor_b32 v209, s88, v209\n"
      "v_xor_b32 v210, s88, v210\n"
      "v_xor_b32 v211, s88, v211\n"
      "v_or_b32 v216, v192, v194\n"
      "v_or3_b32 v216, v216, v196, v198\n"
      "v_or3_b32 v216, v216, v200, v202\n"
      "v_or3_b32 v216, v216, v204, v206\n"
      "v_or3_b32 v216, v216, v208, v209\n"
      "v_or3_b32 v216, v216, v210, v211\n"
      "v_and_b32 v216, s86, v216\n"
      "v_cmp_ne_u32 vcc, 0, v216\n"
      "s_nop 0\n"
      "s_cbranch_vccnz .Lgm_slow0\n"
      ".Lgm_back0:\n"
      "v_permlane32_swap_b32 v208, v209\n"
      "v_permlane32_swap_b32 v210, v211\n"
      "v_add_f32 v192, v192, v196\n"
      "v_add_f32 v193, v193, v197\n"
      "v_add_f32 v194, v194, v198\n"
      "v_add_f32 v195, v195, v199\n"
      "v_add_f32 v208, v208, v209\n"
      "v_add_f32 v210, v210, v211\n"
      "v_add_f32 v200, v200, v204\n"
      "v_add_f32 v201, v201, v205\n"
      "v_add_f32 v202, v202, v206\n"
      "v_add_f32 v203, v203, v207\n"
      "v_permlane16_swap_b32 v208, v210\n"
      "v_add_f32 v192, v192, v200\n"
      "v_add_f32 v193, v193, v201\n"
      "v_add_f32 v194, v194, v202\n"
      "v_add_f32 v195, v195, v203\n"
      "v_add_f32 v208, v208, v210\n"
      "v_permlane32_swap_b32 v192, v193\n"
      "v_permlane32_swap_b32 v194, v195\n"
      "v_add_f32_dpp v208, v208, v208 quad_perm:[1,0,3,2] row_mask:0xf bank_mask:0xf\n"
      "v_add_f32 v192, v192, v193\n"
      "v_add_f32 v194, v194, v195\n"
      "v_add_f32_dpp v208, v208, v208 quad_perm:[2,3,0,1] row_mask:0xf bank_mask:0xf\n"
      "v_cvt_pk_bf16_f32 v208, v208, v208\n"
      "v_permlane16_swap_b32 v192, v194\n"
      "v_bfi_b32 v208, s87, v26, v208\n"
      "v_add_f32 v192, v192, v194\n"
      "s_mov_b32 exec_lo, 0x10001\n"
      "s_mov_b32 exec_hi, 0x10001\n"
      "global_store_dword v39, v208, s[58:59] offset:0 sc1\n"
      "s_mov_b64 exec, -1\n"
      "v_cvt_pk_bf16_f32 v192, v192, v192\n"
      "v_bfi_b32 v192, s87, v26, v192\n"
      "global_store_dword v38, v192, s[56:57] offset:0 sc1\n"
      "v_cvt_pk_bf16_f32 v86, v116, v117\n"
      "v_mfma_f32_16x16x32_bf16 v[120:123], a[188:191], a[164:167], v[120:123]\n"
      "v_cvt_pk_bf16_f32 v87, v118, v119\n"
      "v_mfma_f32_16x16x32_bf16 v[92:95], a[64:67], a[56:59], v[92:95]\n"
      "v_fmaak_f32 v48, v88, v25, 0xb5c1a2a0\n"
      "v_fmaak_f32 v49, v89, v25, 0xb5c1a2a0\n"
      "v_fmaak_f32 v50, v90, v25, 0xb5c1a2a0\n"
      "v_fmaak_f32 v51, v91, v25, 0xb5c1a2a0\n"
      "v_exp_f32_e64 v48, v48 clamp\n"
      "v_mfma_f32_16x16x32_bf16 v[92:95], a[68:71], a[60:63], v[92:95]\n"
      "v_max_f32 v88, s94, v88\n"
      "v_exp_f32_e64 v49, v49 clamp\n"
      "v_max_f32 v89, s94, v89\n"
      "v_exp_f32_e64 v50, v50 clamp\n"
      "v_max_f32 v90, s94, v90\n"
      "v_mfma_f32_16x16x32_bf16 v[92:95], a[176:179], a[168:171], v[92:95]\n"
      "v_exp_f32_e64 v51, v51 clamp\n"
      "v_max_f32 v91, s94, v91\n"
      "v_add_f32 v88, v88, v48\n"
      "v_add_f32 v89, v89, v49\n"
      "v_add_f32 v90, v90, v50\n"
      "v_mfma_f32_16x16x32_bf16 v[92:95], a[180:183], a[172:175], v[92:95]\n"
      "v_add_f32 v91, v91, v51\n"
      "v_fmaak_f32 v52, v120, v25, 0xb5c1a2a0\n"
      "v_fmaak_f32 v53, v121, v25, 0xb5c1a2a0\n"
      "v_fmaak_f32 v54, v122, v25, 0xb5c1a2a0\n"
      "v_fmaak_f32 v55, v123, v25, 0xb5c1a2a0\n"
      "v_mfma_f32_16x16x32_bf16 v[124:127], a[72:75], a[56:59], v[124:127]\n"
      "v_exp_f32_e64 v52, v52 clamp\n"
      "v_max_f32 v120, s94, v120\n"
      "v_exp_f32_e64 v53, v53 clamp\n"
      "v_max_f32 v121, s94, v121\n"
      "v_exp_f32_e64 v54, v54 clamp\n"
      "v_mfma_f32_16x16x32_bf16 v[124:127], a[76:79], a[60:63], v[124:127]\n"
      "v_max_f32 v122, s94, v122\n"
      "v_exp_f32_e64 v55, v55 clamp\n"
      "v_max_f32 v123, s94, v123\n"
      "v_add_f32 v120, v120, v52\n"
      "v_add_f32 v121, v121, v53\n"
      "v_mfma_f32_16x16x32_bf16 v[124:127], a[184:187], a[168:171], v[124:127]\n"
      "v_add_f32 v122, v122, v54\n"
      "v_add_f32 v123, v123, v55\n"
      "v_cvt_pk_bf16_f32 v88, v88, v89\n"
      "v_cvt_pk_bf16_f32 v89, v90, v91\n"
      "v_cvt_pk_bf16_f32 v90, v120, v121\n"
      "v_mfma_f32_16x16x32_bf16 v[124:127], a[188:191], a[172:175], v[124:127]\n"
      "v_cvt_pk_bf16_f32 v91, v122, v123\n"
      "v_fmaak_f32 v44, v92, v25, 0xb5c1a2a0\n"
      "v_fmaak_f32 v45, v93, v25, 0xb5c1a2a0\n"
      "v_fmaak_f32 v46, v94, v25, 0xb5c1a2a0\n"
      "v_fmaak_f32 v47, v95, v25, 0xb5c1a2a0\n"
      "v_exp_f32_e64 v44, v44 clamp\n"
      "v_max_f32 v92, s94, v92\n"
      "v_exp_f32_e64 v45, v45 clamp\n"
      "v_max_f32 v93, s94, v93\n"
      "v_exp_f32_e64 v46, v46 clamp\n"
      "v_max_f32 v94, s94, v94\n"
      "v_exp_f32_e64 v47, v47 clamp\n"
      "v_max_f32 v95, s94, v95\n"
      "v_add_f32 v92, v92, v44\n"
      "v_add_f32 v93, v93, v45\n"
      "v_add_f32 v94, v94, v46\n"
      "v_add_f32 v95, v95, v47\n"
      "v_fmaak_f32 v48, v124, v25, 0xb5c1a2a0\n"
      "v_fmaak_f32 v49, v125, v25, 0xb5c1a2a0\n"
      "v_fmaak_f32 v50, v126, v25, 0xb5c1a2a0\n"
      "v_fmaak_f32 v51, v127, v25, 0xb5c1a2a0\n"
      "v_exp_f32_e64 v48, v48 clamp\n"
      "v_max_f32 v124, s94, v124\n"
      "v_exp_f32_e64 v49, v49 clamp\n"
      "v_max_f32 v125, s94, v125\n"
      "v_exp_f32_e64 v50, v50 clamp\n"
      "v_max_f32 v126, s94, v126\n"
      "v_exp_f32_e64 v51, v51 clamp\n"
      "v_max_f32 v127, s94, v127\n"
      "v_add_f32 v124, v124, v48\n"
      "v_add_f32 v125, v125, v49\n"
      "v_add_f32 v126, v126, v50\n"
      "v_add_f32 v127, v127, v51\n"
      "v_cvt_pk_bf16_f32 v92, v92, v93\n"
      "v_cvt_pk_bf16_f32 v93, v94, v95\n"
      "v_cvt_pk_bf16_f32 v94, v124, v125\n"
      "v_cvt_pk_bf16_f32 v95, v126, v127\n"
      "s_waitcnt vmcnt(2)\n"
      "v_mov_b32 v16, 0\n"
      "v_mov_b32 v17, 0\n"
      "v_mov_b32 v18, 0\n"
      "v_mov_b32 v19, 0\n"
      "v_accvgpr_read_b32 v224, a224\n"
      "v_accvgpr_read_b32 v225, a225\n"
      "v_accvgpr_read_b32 v226, a226\n"
      "v_accvgpr_read_b32 v227, a227\n"
      "v_accvgpr_read_b32 v228, a228\n"
      "v_accvgpr_read_b32 v229, a229\n"
      "v_accvgpr_read_b32 v230, a230\n"
      "v_accvgpr_read_b32 v231, a231\n"
      "v_accvgpr_read_b32 v232, a232\n"
      "v_accvgpr_read_b32 v233, a233\n"
      "v_accvgpr_read_b32 v234, a234\n"
      "v_accvgpr_read_b32 v235, a235\n"
      "v_accvgpr_read_b32 v236, a236\n"
      "v_accvgpr_read_b32 v237, a237\n"
      "v_accvgpr_read_b32 v238, a238\n"
      "v_accvgpr_read_b32 v239, a239\n"
      "v_accvgpr_read_b32 v240, a240\n"
      "v_accvgpr_read_b32 v241, a241\n"
      "v_accvgpr_read_b32 v242, a242\n"
      "v_accvgpr_read_b32 v243, a243\n"
      "v_accvgpr_read_b32 v244, a244\n"
      "v_accvgpr_read_b32 v245, a245\n"
      "v_accvgpr_read_b32 v246, a246\n"
      "v_accvgpr_read_b32 v247, a247\n"
      "v_accvgpr_read_b32 v248, a248\n"
      "v_accvgpr_read_b32 v249, a249\n"
      "v_accvgpr_read_b32 v250, a250\n"
      "v_accvgpr_read_b32 v251, a251\n"
      "v_accvgpr_read_b32 v252, a252\n"
      "v_accvgpr_read_b32 v253, a253\n"
      "v_accvgpr_read_b32 v254, a254\n"
      "v_accvgpr_read_b32 v255, a255\n"
      "s_barrier\n"
      "ds_read_b64_tr_b16 a[128:129], v58 offset:0\n"
      "ds_read_b64_tr_b16 a[130:131], v58 offset:1024\n"
      "ds_read_b64_tr_b16 a[132:133], v58 offset:4096\n"
      "ds_read_b64_tr_b16 a[134:135], v58 offset:5120\n"
      "ds_read_b64_tr_b16 a[136:137], v59 offset:0\n"
      "ds_read_b64_tr_b16 a[138:139], v59 offset:1024\n"
      "ds_read_b64_tr_b16 a[140:141], v59 offset:4096\n"
      "ds_read_b64_tr_b16 a[142:143], v59 offset:5120\n"
      "ds_read_b64_tr_b16 a[144:145], v58 offset:512\n"
      "ds_read_b64_tr_b16 a[146:147], v58 offset:1536\n"
      "ds_read_b64_tr_b16 a[148:149], v58 offset:4608\n"
      "ds_read_b64_tr_b16 a[150:151], v58 offset:5632\n"
      "ds_read_b64_tr_b16 a[152:153], v59 offset:512\n"
      "ds_read_b64_tr_b16 a[154:155], v59 offset:1536\n"
      "ds_read_b64_tr_b16 a[156:157], v59 offset:4608\n"
      "ds_read_b64_tr_b16 a[158:159], v59 offset:5632\n"
      "ds_read_b64_tr_b16 a[160:161], v58 offset:8192\n"
      "ds_read_b64_tr_b16 a[162:163], v58 offset:9216\n"
      "ds_read_b64_tr_b16 a[164:165], v58 offset:12288\n"
      "ds_read_b64_tr_b16 a[166:167], v58 offset:13312\n"
      "ds_read_b64_tr_b16 a[168:169], v59 offset:8192\n"
      "global_load_dwordx4 v[192:195], v24, s[56:57] offset:0 sc1\n"
      "global_load_dwordx4 v[196:199], v24, s[56:57] offset:1024 sc1\n"
      "global_load_dwordx4 v[200:203], v24, s[56:57] offset:2048 sc1\n"
      "global_load_dwordx4 v[204:207], v24, s[56:57] offset:3072 sc1\n"
      "global_load_dwordx4 v[208:211], v28, s[58:59] offset:0 sc1\n"
      "global_load_dwordx4 v[212:215], v28, s[58:59] offset:64 sc1\n"
      "ds_read_b64_tr_b16 a[170:171], v59 offset:9216\n"
      "ds_read_b64_tr_b16 a[172:173], v59 offset:12288\n"
      "ds_read_b64_tr_b16 a[174:175], v59 offset:13312\n"
      "ds_read_b64_tr_b16 a[176:177], v58 offset:8704\n"
      "ds_read_b64_tr_b16 a[178:179], v58 offset:9728\n"
      "ds_read_b64_tr_b16 a[180:181], v58 offset:12800\n"
      "ds_read_b64_tr_b16 a[182:183], v58 offset:13824\n"
      "ds_read_b64_tr_b16 a[184:185], v59 offset:8704\n"
      "ds_read_b64_tr_b16 a[186:187], v59 offset:9728\n"
      "ds_read_b64_tr_b16 a[188:189], v59 offset:12800\n"
      "ds_read_b64_tr_b16 a[190:191], v59 offset:13824\n"
      "ds_read_b64_tr_b16 a[192:193], v58 offset:32768\n"
      "ds_read_b64_tr_b16 a[194:195], v58 offset:33792\n"
      "ds_read_b64_tr_b16 a[196:197], v58 offset:36864\n"
      "ds_read_b64_tr_b16 a[198:199], v58 offset:37888\n"
      "ds_read_b64_tr_b16 a[200:201], v59 offset:32768\n"
      "ds_read_b64_tr_b16 a[202:203], v59 offset:33792\n"
      "ds_read_b64_tr_b16 a[204:205], v59 offset:36864\n"
      "ds_read_b64_tr_b16 a[206:207], v59 offset:37888\n"
      "ds_read_b64_tr_b16 a[208:209], v58 offset:33280\n"
      "ds_read_b64_tr_b16 a[210:211], v58 offset:34304\n"
      "ds_read_b64_tr_b16 a[212:213], v58 offset:37376\n"
      "ds_read_b64_tr_b16 a[214:215], v58 offset:38400\n"
      "ds_read_b64_tr_b16 a[216:217], v59 offset:33280\n"
      "ds_read_b64_tr_b16 a[218:219], v59 offset:34304\n"
      "ds_read_b64_tr_b16 a[220:221], v59 offset:37376\n"
      ".Lgm_w7:\n"
      "s_cbranch_execz .Lgm_r1\n"
      "ds_read_b64_tr_b16 a[222:223], v59 offset:38400\n"
      "ds_read_b64_tr_b16 a[224:225], v58 offset:40960\n"
      "ds_read_b64_tr_b16 a[226:227], v58 offset:41984\n"
      "ds_read_b64_tr_b16 a[228:229], v58 offset:45056\n"
      "ds_read_b64_tr_b16 a[230:231], v58 offset:46080\n"
      "ds_read_b64_tr_b16 a[232:233], v59 offset:40960\n"
      "ds_read_b64_tr_b16 a[234:235], v59 offset:41984\n"
      "ds_read_b64_tr_b16 a[236:237], v59 offset:45056\n"
      "ds_read_b64_tr_b16 a[238:239], v59 offset:46080\n"
      "ds_read_b64_tr_b16 a[240:241], v58 offset:41472\n"
      "ds_read_b64_tr_b16 a[242:243], v58 offset:42496\n"
      "ds_read_b64_tr_b16 a[244:245], v58 offset:45568\n"
      "ds_read_b64_tr_b16 a[246:247], v58 offset:46592\n"
      "ds_read_b64_tr_b16 a[248:249], v59 offset:41472\n"
      "ds_read_b64_tr_b16 a[250:251], v59 offset:42496\n"
      "ds_read_b64_tr_b16 a[252:253], v59 offset:45568\n"
      "ds_read_b64_tr_b16 a[254:255], v59 offset:46592\n"
      "s_waitcnt lgkmcnt(0)\n"
      "s_barrier\n"
      "s_mov_b64 exec, 0\n"
      "s_cmp_eq_u32 s4, 0\n"
      "s_cbranch_scc1 .Lgm_w8\n"
      "s_cmp_eq_u32 s4, 1\n"
      "s_cbranch_scc1 .Lgm_w9\n"
      ".Lgm_r2:\n"
      "s_mov_b64 exec, -1\n"
      "s_waitcnt vmcnt(0)\n"
      "v_xor_b32 v216, v192, v26\n"
      "v_bitop3_b32 v216, v216, v193, v26 bitop3:0xf6\n"
      "v_bitop3_b32 v216, v216, v194, v26 bitop3:0xf6\n"
      "v_bitop3_b32 v216, v216, v195, v26 bitop3:0xf6\n"
      "v_bitop3_b32 v216, v216, v196, v26 bitop3:0xf6\n"
      "v_bitop3_b32 v216, v216, v197, v26 bitop3:0xf6\n"
      "v_bitop3_b32 v216, v216, v198, v26 bitop3:0xf6\n"
      "v_bitop3_b32 v216, v216, v199, v26 bitop3:0xf6\n"
      "v_bitop3_b32 v216, v216, v200, v26 bitop3:0xf6\n"
      "v_bitop3_b32 v216, v216, v201, v26 bitop3:0xf6\n"
      "v_bitop3_b32 v216, v216, v202, v26 bitop3:0xf6\n"
      "v_bitop3_b32 v216, v216, v203, v26 bitop3:0xf6\n"
      "v_bitop3_b32 v216, v216, v204, v26 bitop3:0xf6\n"
      "v_bitop3_b32 v216, v216, v205, v26 bitop3:0xf6\n"
      "v_bitop3_b32 v216, v216, v206, v26 bitop3:0xf6\n"
      "v_bitop3_b32 v216, v216, v207, v26 bitop3:0xf6\n"
      "v_bitop3_b32 v216, v216, v208, v26 bitop3:0xf6\n"
      "v_bitop3_b32 v216, v216, v209, v26 bitop3:0xf6\n"
      "v_bitop3_b32 v216, v216, v210, v26 bitop3:0xf6\n"
      "v_bitop3_b32 v216, v216, v211, v26 bitop3:0xf6\n"
      "v_bitop3_b32 v216, v216, v212, v26 bitop3:0xf6\n"
      "v_bitop3_b32 v216, v216, v213, v26 bitop3:0xf6\n"
      "v_bitop3_b32 v216, v216, v214, v26 bitop3:0xf6\n"
      "v_bitop3_b32 v216, v216, v215, v26 bitop3:0xf6\n"
      "v_cmp_ne_u16 vcc, 0, v216\n"
      "s_nop 0\n"
      "s_cbranch_vccnz .Lgm_slow1\n"
      ".Lgm_back1:\n"
      "v_perm_b32 v52, v209, v208, s89\n"
      "v_perm_b32 v53, v211, v210, s89\n"
      "v_perm_b32 v54, v213, v212, s89\n"
      "v_perm_b32 v55, v215, v214, s89\n"
      "v_perm_b32 v44, v193, v192, s89\n"
      "v_perm_b32 v45, v195, v194, s89\n"
      "v_perm_b32 v46, v197, v196, s89\n"
      "v_perm_b32 v47, v199, v198, s89\n"
      "v_perm_b32 v48, v201, v200, s89\n"
      "v_perm_b32 v49, v203, v202, s89\n"
      "v_perm_b32 v50, v205, v204, s89\n"
      "v_perm_b32 v51, v207, v206, s89\n"
      "v_mfma_f32_16x16x32_bf16 v[96:99], v[52:55], v[64:67], v[20:23]\n"
      "v_mfma_f32_16x16x32_bf16 v[128:131], v[44:47], v[64:67], 0\n"
      "v_mfma_f32_16x16x32_bf16 v[160:163], v[48:51], v[64:67], 0\n"
      "v_mfma_f32_16x16x32_bf16 v[100:103], v[52:55], v[68:71], v[20:23]\n"
      "v_mfma_f32_16x16x32_bf16 v[132:135], v[44:47], v[68:71], 0\n"
      "v_mfma_f32_16x16x32_bf16 v[164:167], v[48:51], v[68:71], 0\n"
      "v_mfma_f32_16x16x32_bf16 v[104:107], v[52:55], v[72:75], v[20:23]\n"
      "s_nop 1\n"
      "v_rcp_f32 v96, v96\n"
      "v_mfma_f32_16x16x32_bf16 v[136:139], v[44:47], v[72:75], 0\n"
      "v_mfma_f32_16x16x32_bf16 v[168:171], v[48:51], v[72:75], 0\n"
      "v_mfma_f32_16x16x32_bf16 v[108:111], v[52:55], v[76:79], v[20:23]\n"
      "v_rcp_f32 v100, v100\n"
      "v_mul_f32 v128, v128, v96\n"
      "v_mul_f32 v129, v129, v96\n"
      "v_mul_f32 v130, v130, v96\n"
      "v_mul_f32 v131, v131, v96\n"
      "v_mul_f32 v160, v160, v96\n"
      "v_mul_f32 v161, v161, v96\n"
      "v_mul_f32 v162, v162, v96\n"
      "v_mul_f32 v163, v163, v96\n"
      "v_cvt_pk_bf16_f32 v128, v128, v129\n"
      "v_cvt_pk_bf16_f32 v129, v130, v131\n"
      "v_cvt_pk_bf16_f32 v130, v160, v161\n"
      "v_cvt_pk_bf16_f32 v131, v162, v163\n"
      "global_store_dwordx4 v24, v[128:131], s[72:73] offset:0 sc1\n"
      "v_mfma_f32_16x16x32_bf16 v[140:143], v[44:47], v[76:79], 0\n"
      "v_mfma_f32_16x16x32_bf16 v[172:175], v[48:51], v[76:79], 0\n"
      "v_mfma_f32_16x16x32_bf16 v[112:115], v[52:55], v[80:83], v[20:23]\n"
      "v_rcp_f32 v104, v104\n"
      "s_add_u32 s72, s72, 0x10000\n"
      "s_addc_u32 s73, s73, 0\n"
      "v_mul_f32 v132, v132, v100\n"
      "v_mul_f32 v133, v133, v100\n"
      "v_mul_f32 v134, v134, v100\n"
      "v_mul_f32 v135, v135, v100\n"
      "v_mul_f32 v164, v164, v100\n"
      "v_mul_f32 v165, v165, v100\n"
      "v_mul_f32 v166, v166, v100\n"
      "v_mul_f32 v167, v167, v100\n"
      "v_cvt_pk_bf16_f32 v132, v132, v133\n"
      "v_cvt_pk_bf16_f32 v133, v134, v135\n"
      "v_cvt_pk_bf16_f32 v134, v164, v165\n"
      "v_cvt_pk_bf16_f32 v135, v166, v167\n"
      "global_store_dwordx4 v24, v[132:135], s[72:73] offset:0 sc1\n"
      "v_mfma_f32_16x16x32_bf16 v[144:147], v[44:47], v[80:83], 0\n"
      "v_mfma_f32_16x16x32_bf16 v[176:179], v[48:51], v[80:83], 0\n"
      "v_mfma_f32_16x16x32_bf16 v[116:119], v[52:55], v[84:87], v[20:23]\n"
      "v_rcp_f32 v108, v108\n"
      "s_add_u32 s72, s72, 0x10000\n"
      "s_addc_u32 s73, s73, 0\n"
      "v_mul_f32 v136, v136, v104\n"
      "v_mul_f32 v137, v137, v104\n"
      "v_mul_f32 v138, v138, v104\n"
      "v_mul_f32 v139, v139, v104\n"
      "v_mul_f32 v168, v168, v104\n"
      "v_mul_f32 v169, v169, v104\n"
      "v_mul_f32 v170, v170, v104\n"
      "v_mul_f32 v171, v171, v104\n"
      "v_cvt_pk_bf16_f32 v136, v136, v137\n"
      "v_cvt_pk_bf16_f32 v137, v138, v139\n"
      "v_cvt_pk_bf16_f32 v138, v168, v169\n"
      "v_cvt_pk_bf16_f32 v139, v170, v171\n"
      "global_store_dwordx4 v24, v[136:139], s[72:73] offset:0 sc1\n"
      "v_mfma_f32_16x16x32_bf16 v[148:151], v[44:47], v[84:87], 0\n"
      "v_mfma_f32_16x16x32_bf16 v[180:183], v[48:51], v[84:87], 0\n"
      "v_mfma_f32_16x16x32_bf16 v[120:123], v[52:55], v[88:91], v[20:23]\n"
      "v_rcp_f32 v112, v112\n"
      "s_add_u32 s72, s72, 0x10000\n"
      "s_addc_u32 s73, s73, 0\n"
      "v_mul_f32 v140, v140, v108\n"
      "v_mul_f32 v141, v141, v108\n"
      "v_mul_f32 v142, v142, v108\n"
      "v_mul_f32 v143, v143, v108\n"
      "v_mul_f32 v172, v172, v108\n"
      "v_mul_f32 v173, v173, v108\n"
      "v_mul_f32 v174, v174, v108\n"
      "v_mul_f32 v175, v175, v108\n"
      "v_cvt_pk_bf16_f32 v140, v140, v141\n"
      "v_cvt_pk_bf16_f32 v141, v142, v143\n"
      "v_cvt_pk_bf16_f32 v142, v172, v173\n"
      "v_cvt_pk_bf16_f32 v143, v174, v175\n"
      "global_store_dwordx4 v24, v[140:143], s[72:73] offset:0 sc1\n"
      "v_mfma_f32_16x16x32_bf16 v[152:155], v[44:47], v[88:91], 0\n"
      "v_mfma_f32_16x16x32_bf16 v[184:187], v[48:51], v[88:91], 0\n"
      "v_mfma_f32_16x16x32_bf16 v[124:127], v[52:55], v[92:95], v[20:23]\n"
      "v_rcp_f32 v116, v116\n"
      "s_add_u32 s72, s72, 0x10000\n"
      "s_addc_u32 s73, s73, 0\n"
      "v_mul_f32 v144, v144, v112\n"
      "v_mul_f32 v145, v145, v112\n"
      "v_mul_f32 v146, v146, v112\n"
      "v_mul_f32 v147, v147, v112\n"
      "v_mul_f32 v176, v176, v112\n"
      "v_mul_f32 v177, v177, v112\n"
      "v_mul_f32 v178, v178, v112\n"
      "v_mul_f32 v179, v179, v112\n"
      "v_cvt_pk_bf16_f32 v144, v144, v145\n"
      "v_cvt_pk_bf16_f32 v145, v146, v147\n"
      "v_cvt_pk_bf16_f32 v146, v176, v177\n"
      "v_cvt_pk_bf16_f32 v147, v178, v179\n"
      "global_store_dwordx4 v24, v[144:147], s[72:73] offset:0 sc1\n"
      "v_mfma_f32_16x16x32_bf16 v[156:159], v[44:47], v[92:95], 0\n"
      "v_mfma_f32_16x16x32_bf16 v[188:191], v[48:51], v[92:95], 0\n"
      "v_rcp_f32 v120, v120\n"
      "s_add_u32 s72, s72, 0x10000\n"
      "s_addc_u32 s73, s73, 0\n"
      "v_mul_f32 v148, v148, v116\n"
      "v_mul_f32 v149, v149, v116\n"
      "v_mul_f32 v150, v150, v116\n"
      "v_mul_f32 v151, v151, v116\n"
      "v_mul_f32 v180, v180, v116\n"
      "v_mul_f32 v181, v181, v116\n"
      "v_mul_f32 v182, v182, v116\n"
      "v_mul_f32 v183, v183, v116\n"
      "v_cvt_pk_bf16_f32 v148, v148, v149\n"
      "v_cvt_pk_bf16_f32 v149, v150, v151\n"
      "v_cvt_pk_bf16_f32 v150, v180, v181\n"
      "v_cvt_pk_bf16_f32 v151, v182, v183\n"
      "global_store_dwordx4 v24, v[148:151], s[72:73] offset:0 sc1\n"
      "v_rcp_f32 v124, v124\n"
      "s_add_u32 s72, s72, 0x10000\n"
      "s_addc_u32 s73, s73, 0\n"
      "v_mul_f32 v152, v152, v120\n"
      "v_mul_f32 v153, v153, v120\n"
      "v_mul_f32 v154, v154, v120\n"
      "v_mul_f32 v155, v155, v120\n"
      "v_mul_f32 v184, v184, v120\n"
      "v_mul_f32 v185, v185, v120\n"
      "v_mul_f32 v186, v186, v120\n"
      "v_mul_f32 v187, v187, v120\n"
      "v_cvt_pk_bf16_f32 v152, v152, v153\n"
      "v_cvt_pk_bf16_f32 v153, v154, v155\n"
      "v_cvt_pk_bf16_f32 v154, v184, v185\n"
      "v_cvt_pk_bf16_f32 v155, v186, v187\n"
      "global_store_dwordx4 v24, v[152:155], s[72:73] offset:0 sc1\n"
      "s_add_u32 s72, s72, 0x10000\n"
      "s_addc_u32 s73, s73, 0\n"
      "v_mul_f32 v156, v156, v124\n"
      "v_mul_f32 v157, v157, v124\n"
      "v_mul_f32 v158, v158, v124\n"
      "v_mul_f32 v159, v159, v124\n"
      "v_mul_f32 v188, v188, v124\n"
      "v_mul_f32 v189, v189, v124\n"
      "v_mul_f32 v190, v190, v124\n"
      "v_mul_f32 v191, v191, v124\n"
      "v_cvt_pk_bf16_f32 v156, v156, v157\n"
      "v_cvt_pk_bf16_f32 v157, v158, v159\n"
      "v_cvt_pk_bf16_f32 v158, v188, v189\n"
      "v_cvt_pk_bf16_f32 v159, v190, v191\n"
      "global_store_dwordx4 v24, v[156:159], s[72:73] offset:0 sc1\n"
      "v_dot2c_f32_bf16 v16, v224, v8\n"
      "v_dot2c_f32_bf16 v17, v232, v8\n"
      "v_dot2c_f32_bf16 v18, v240, v8\n"
      "v_dot2c_f32_bf16 v19, v248, v8\n"
      "v_dot2c_f32_bf16 v16, v225, v9\n"
      "v_dot2c_f32_bf16 v17, v233, v9\n"
      "v_dot2c_f32_bf16 v18, v241, v9\n"
      "v_dot2c_f32_bf16 v19, v249, v9\n"
      "v_dot2c_f32_bf16 v16, v226, v10\n"
      "v_dot2c_f32_bf16 v17, v234, v10\n"
      "v_dot2c_f32_bf16 v18, v242, v10\n"
      "v_dot2c_f32_bf16 v19, v250, v10\n"
      "v_dot2c_f32_bf16 v16, v227, v11\n"
      "v_dot2c_f32_bf16 v17, v235, v11\n"
      "v_dot2c_f32_bf16 v18, v243, v11\n"
      "v_dot2c_f32_bf16 v19, v251, v11\n"
      "v_dot2c_f32_bf16 v16, v228, v12\n"
      "v_dot2c_f32_bf16 v17, v236, v12\n"
      "v_dot2c_f32_bf16 v18, v244, v12\n"
      "v_dot2c_f32_bf16 v19, v252, v12\n"
      "v_dot2c_f32_bf16 v16, v229, v13\n"
      "v_dot2c_f32_bf16 v17, v237, v13\n"
      "v_dot2c_f32_bf16 v18, v245, v13\n"
      "v_dot2c_f32_bf16 v19, v253, v13\n"
      "v_dot2c_f32_bf16 v16, v230, v14\n"
      "v_dot2c_f32_bf16 v17, v238, v14\n"
      "v_dot2c_f32_bf16 v18, v246, v14\n"
      "v_dot2c_f32_bf16 v19, v254, v14\n"
      "v_dot2c_f32_bf16 v16, v231, v15\n"
      "v_dot2c_f32_bf16 v17, v239, v15\n"
      "v_dot2c_f32_bf16 v18, v247, v15\n"
      "v_dot2c_f32_bf16 v19, v255, v15\n"
      "s_nop 0\n"
      "v_permlane32_swap_b32 v16, v17\n"
      "s_nop 0\n"
      "v_permlane32_swap_b32 v18, v19\n"
      "v_add_f32 v16, v16, v17\n"
      "v_add_f32 v18, v18, v19\n"
      "s_nop 1\n"
      "v_permlane16_swap_b32 v16, v18\n"
      "v_add_f32 v16, v16, v18\n"
      "s_nop 1\n"
      "v_add_f32_dpp v16, v16, v16 row_ror:8 row_mask:0xf bank_mask:0xf\n"
      "s_nop 1\n"
      "v_add_f32_dpp v16, v16, v16 row_ror:4 row_mask:0xf bank_mask:0xf\n"
      "s_nop 1\n"
      "v_add_f32_dpp v16, v16, v16 row_ror:2 row_mask:0xf bank_mask:0xf\n"
      "s_nop 1\n"
      "v_add_f32_dpp v16, v16, v16 row_ror:1 row_mask:0xf bank_mask:0xf\n"
      "v_mul_f32 v16, s96, v16\n"
      "v_exp_f32 v16, v16\n"
      "s_nop 0\n"
      "v_add_f32 v16, 1.0, v16\n"
      "v_rcp_f32 v16, v16\n"
      "s_mov_b32 exec_lo, 0x10001\n"
      "s_mov_b32 exec_hi, 0x10001\n"
      "global_store_dword v42, v16, s[68:69] offset:0 sc1\n"
      "s_mov_b64 exec, -1\n"
      "s_add_u32 s1, s0, 0\n"
      "s_and_b32 s1, s1, 7\n"
      "s_lshl_b32 s1, s1, 12\n"
      "s_add_u32 s46, s44, s1\n"
      "s_addc_u32 s47, s45, 0\n"
      "v_add_u32 v2, 0x10000, v24\n"
      "v_add_u32 v3, 0x20000, v24\n"
      "v_add_u32 v4, 0x30000, v24\n"
      "v_and_b32 v237, 0xf0, v24\n"
      "v_lshrrev_b32 v237, 2, v237\n"
      "v_and_b32 v236, 7, v31\n"
      "s_lshl_b32 s90, s0, 2\n"
      "v_add_u32 v236, s90, v236\n"
      "v_and_b32 v236, 31, v236\n"
      "v_lshlrev_b32 v236, 7, v236\n"
      "ds_read_b64_tr_b16 v[128:129], v60 offset:0\n"
      "ds_read_b64_tr_b16 v[130:131], v60 offset:1024\n"
      "ds_read_b64_tr_b16 v[132:133], v60 offset:4096\n"
      "ds_read_b64_tr_b16 v[134:135], v60 offset:5120\n"
      "ds_read_b64_tr_b16 v[136:137], v61 offset:0\n"
      "ds_read_b64_tr_b16 v[138:139], v61 offset:1024\n"
      "ds_read_b64_tr_b16 v[140:141], v61 offset:4096\n"
      "ds_read_b64_tr_b16 v[142:143], v61 offset:5120\n"
      "ds_read_b64_tr_b16 v[144:145], v60 offset:512\n"
      "ds_read_b64_tr_b16 v[146:147], v60 offset:1536\n"
      "ds_read_b64_tr_b16 v[148:149], v60 offset:4608\n"
      "ds_read_b64_tr_b16 v[150:151], v60 offset:5632\n"
      "ds_read_b64_tr_b16 v[152:153], v61 offset:512\n"
      "ds_read_b64_tr_b16 v[154:155], v61 offset:1536\n"
      "ds_read_b64_tr_b16 v[156:157], v61 offset:4608\n"
      "ds_read_b64_tr_b16 v[158:159], v61 offset:5632\n"
      "ds_read_b64_tr_b16 v[160:161], v60 offset:8192\n"
      "ds_read_b64_tr_b16 v[162:163], v60 offset:9216\n"
      "ds_read_b64_tr_b16 v[164:165], v60 offset:12288\n"
      "ds_read_b64_tr_b16 v[166:167], v60 offset:13312\n"
      "ds_read_b64_tr_b16 v[168:169], v61 offset:8192\n"
      "ds_read_b64_tr_b16 v[170:171], v61 offset:9216\n"
      "ds_read_b64_tr_b16 v[172:173], v61 offset:12288\n"
      "ds_read_b64_tr_b16 v[174:175], v61 offset:13312\n"
      "ds_read_b64_tr_b16 v[176:177], v60 offset:8704\n"
      "ds_read_b64_tr_b16 v[178:179], v60 offset:9728\n"
      "ds_read_b64_tr_b16 v[180:181], v60 offset:12800\n"
      "ds_read_b64_tr_b16 v[182:183], v60 offset:13824\n"
      "ds_read_b64_tr_b16 v[184:185], v61 offset:8704\n"
      "ds_read_b64_tr_b16 v[186:187], v61 offset:9728\n"
      "ds_read_b64_tr_b16 v[188:189], v61 offset:12800\n"
      "ds_read_b64_tr_b16 v[190:191], v61 offset:13824\n"
      "s_waitcnt lgkmcnt(0)\n"
      "s_barrier\n"
      "s_waitcnt vmcnt(0)\n"
      "s_mov_b64 exec, 1\n"
      "global_store_dword v27, v26, s[60:61] offset:0 sc1\n"
      "s_mov_b64 exec, -1\n"
      "s_add_u32 s3, s0, 5\n"
      "s_and_b32 s3, s3, 7\n"
      "s_lshl_b32 s3, s3, 18\n"
      "s_add_u32 s38, s42, s3\n"
      "s_addc_u32 s39, s43, 0\n"
      "s_add_u32 m0, s99, 0x10000\n"
      "s_nop 0\n"
      "global_load_lds_dwordx4 v32, s[38:39] offset:0\n"
      "global_load_lds_dwordx4 v33, s[38:39] offset:1024\n"
      "global_load_lds_dwordx4 v34, s[38:39] offset:2048\n"
      "global_load_lds_dwordx4 v35, s[38:39] offset:3072\n"
      "s_add_u32 s38, s38, 0x10000\n"
      "s_addc_u32 s39, s39, 0\n"
      "s_add_u32 m0, s99, 0x11000\n"
      "s_nop 0\n"
      "global_load_lds_dwordx4 v32, s[38:39] offset:0\n"
      "global_load_lds_dwordx4 v33, s[38:39] offset:1024\n"
      "global_load_lds_dwordx4 v34, s[38:39] offset:2048\n"
      "global_load_lds_dwordx4 v35, s[38:39] offset:3072\n"
      "s_add_u32 s3, s0, 6\n"
      "s_and_b32 s3, s3, 7\n"
      "s_lshl_b32 s3, s3, 18\n"
      "s_add_u32 s38, s42, s3\n"
      "s_addc_u32 s39, s43, 0\n"
      "s_add_u32 m0, s99, 0x18000\n"
      "s_nop 0\n"
      "global_load_lds_dwordx4 v32, s[38:39] offset:0\n"
      "global_load_lds_dwordx4 v33, s[38:39] offset:1024\n"
      "global_load_lds_dwordx4 v34, s[38:39] offset:2048\n"
      "global_load_lds_dwordx4 v35, s[38:39] offset:3072\n"
      "s_add_u32 s38, s38, 0x10000\n"
      "s_addc_u32 s39, s39, 0\n"
      "s_add_u32 m0, s99, 0x19000\n"
      "s_nop 0\n"
      "global_load_lds_dwordx4 v32, s[38:39] offset:0\n"
      "global_load_lds_dwordx4 v33, s[38:39] offset:1024\n"
      "global_load_lds_dwordx4 v34, s[38:39] offset:2048\n"
      "global_load_lds_dwordx4 v35, s[38:39] offset:3072\n"
      "s_add_u32 s3, s0, 7\n"
      "s_and_b32 s3, s3, 7\n"
      "s_lshl_b32 s3, s3, 18\n"
      "s_add_u32 s38, s42, s3\n"
      "s_addc_u32 s39, s43, 0\n"
      "s_add_u32 m0, s99, 0x20000\n"
      "s_nop 0\n"
      "global_load_lds_dwordx4 v32, s[38:39] offset:0\n"
      "global_load_lds_dwordx4 v33, s[38:39] offset:1024\n"
      "global_load_lds_dwordx4 v34, s[38:39] offset:2048\n"
      "global_load_lds_dwordx4 v35, s[38:39] offset:3072\n"
      "s_add_u32 s38, s38, 0x10000\n"
      "s_addc_u32 s39, s39, 0\n"
      "s_add_u32 m0, s99, 0x21000\n"
      "s_nop 0\n"
      "global_load_lds_dwordx4 v32, s[38:39] offset:0\n"
      "global_load_lds_dwordx4 v33, s[38:39] offset:1024\n"
      "global_load_lds_dwordx4 v34, s[38:39] offset:2048\n"
      "global_load_lds_dwordx4 v35, s[38:39] offset:3072\n"
      "global_load_dword v238, v236, s[62:63] offset:0 sc1\n"
      "s_waitcnt vmcnt(0)\n"
      "v_cmp_ne_u32 vcc, s13, v238\n"
      "s_nop 0\n"
      "s_cbranch_vccnz .Lgm_slow3\n"
      ".Lgm_back3:\n"
      "global_load_dwordx4 a[0:3], v24, s[46:47] offset:0 sc1\n"
      "s_add_u32 s1, s0, 1\n"
      "s_and_b32 s1, s1, 7\n"
      "s_lshl_b32 s1, s1, 12\n"
      "global_load_dwordx4 a[16:19], v24, s[46:47] offset:1024 sc1\n"
      "global_load_dwordx4 a[4:7], v2, s[46:47] offset:0 sc1\n"
      "global_load_dwordx4 a[20:23], v2, s[46:47] offset:1024 sc1\n"
      "global_load_dwordx4 a[8:11], v3, s[46:47] offset:0 sc1\n"
      "global_load_dwordx4 a[24:27], v3, s[46:47] offset:1024 sc1\n"
      "global_load_dwordx4 a[12:15], v4, s[46:47] offset:0 sc1\n"
      "global_load_dwordx4 a[28:31], v4, s[46:47] offset:1024 sc1\n"
      "global_load_dwordx4 a[32:35], v24, s[46:47] offset:2048 sc1\n"
      "global_load_dwordx4 a[48:51], v24, s[46:47] offset:3072 sc1\n"
      "global_load_dwordx4 a[36:39], v2, s[46:47] offset:2048 sc1\n"
      "global_load_dwordx4 a[52:55], v2, s[46:47] offset:3072 sc1\n"
      "global_load_dwordx4 a[40:43], v3, s[46:47] offset:2048 sc1\n"
      "global_load_dwordx4 a[56:59], v3, s[46:47] offset:3072 sc1\n"
      "global_load_dwordx4 a[44:47], v4, s[46:47] offset:2048 sc1\n"
      "global_load_dwordx4 a[60:63], v4, s[46:47] offset:3072 sc1\n"
      "s_add_u32 s46, s44, s1\n"
      "s_addc_u32 s47, s45, 0\n"
      "global_load_dwordx4 a[64:67], v24, s[46:47] offset:0 sc1\n"
      "s_add_u32 s1, s0, 2\n"
      "s_and_b32 s1, s1, 7\n"
      "s_lshl_b32 s1, s1, 12\n"
      "global_load_dwordx4 a[80:83], v24, s[46:47] offset:1024 sc1\n"
      "global_load_dwordx4 a[68:71], v2, s[46:47] offset:0 sc1\n"
      "global_load_dwordx4 a[84:87], v2, s[46:47] offset:1024 sc1\n"
      "global_load_dwordx4 a[72:75], v3, s[46:47] offset:0 sc1\n"
      "global_load_dwordx4 a[88:91], v3, s[46:47] offset:1024 sc1\n"
      "s_waitcnt vmcnt(14)\n"
      "v_mfma_f32_16x16x32_bf16 v[64:67], a[128:131], a[0:3], 0\n"
      "v_mfma_f32_16x16x32_bf16 v[64:67], a[132:135], a[16:19], v[64:67]\n"
      "v_mfma_f32_16x16x32_bf16 v[80:83], a[136:139], a[0:3], 0\n"
      "v_mfma_f32_16x16x32_bf16 v[80:83], a[140:143], a[16:19], v[80:83]\n"
      "global_load_dwordx4 a[76:79], v4, s[46:47] offset:0 sc1\n"
      "v_mfma_f32_16x16x32_bf16 v[68:71], a[128:131], a[4:7], 0\n"
      "v_mfma_f32_16x16x32_bf16 v[68:71], a[132:135], a[20:23], v[68:71]\n"
      "v_mfma_f32_16x16x32_bf16 v[84:87], a[136:139], a[4:7], 0\n"
      "v_mfma_f32_16x16x32_bf16 v[84:87], a[140:143], a[20:23], v[84:87]\n"
      "global_load_dwordx4 a[92:95], v4, s[46:47] offset:1024 sc1\n"
      "v_mfma_f32_16x16x32_bf16 v[72:75], a[128:131], a[8:11], 0\n"
      "global_load_dword v238, v40, s[62:63] offset:0 sc1\n"
      "v_mfma_f32_16x16x32_bf16 v[72:75], a[132:135], a[24:27], v[72:75]\n"
      "v_mfma_f32_16x16x32_bf16 v[88:91], a[136:139], a[8:11], 0\n"
      "v_mfma_f32_16x16x32_bf16 v[88:91], a[140:143], a[24:27], v[88:91]\n"
      "global_load_dwordx4 a[96:99], v24, s[46:47] offset:2048 sc1\n"
      "v_mfma_f32_16x16x32_bf16 v[76:79], a[128:131], a[12:15], 0\n"
      "v_mfma_f32_16x16x32_bf16 v[76:79], a[132:135], a[28:31], v[76:79]\n"
      "v_mfma_f32_16x16x32_bf16 v[92:95], a[136:139], a[12:15], 0\n"
      "v_mfma_f32_16x16x32_bf16 v[92:95], a[140:143], a[28:31], v[92:95]\n"
      "global_load_dwordx4 a[112:115], v24, s[46:47] offset:3072 sc1\n"
      "v_mfma_f32_16x16x32_bf16 v[96:99], a[144:147], a[0:3], 0\n"
      "v_mfma_f32_16x16x32_bf16 v[96:99], a[148:151], a[16:19], v[96:99]\n"
      "v_mfma_f32_16x16x32_bf16 v[112:115], a[152:155], a[0:3], 0\n"
      "v_mfma_f32_16x16x32_bf16 v[112:115], a[156:159], a[16:19], v[112:115]\n"
      "global_load_dwordx4 a[100:103], v2, s[46:47] offset:2048 sc1\n"
      "v_mfma_f32_16x16x32_bf16 v[100:103], a[144:147], a[4:7], 0\n"
      "v_mfma_f32_16x16x32_bf16 v[100:103], a[148:151], a[20:23], v[100:103]\n"
      "v_mfma_f32_16x16x32_bf16 v[116:119], a[152:155], a[4:7], 0\n"
      "v_mfma_f32_16x16x32_bf16 v[116:119], a[156:159], a[20:23], v[116:119]\n"
      "global_load_dwordx4 a[116:119], v2, s[46:47] offset:3072 sc1\n"
      "v_mfma_f32_16x16x32_bf16 v[104:107], a[144:147], a[8:11], 0\n"
      "v_mfma_f32_16x16x32_bf16 v[104:107], a[148:151], a[24:27], v[104:107]\n"
      "v_mfma_f32_16x16x32_bf16 v[120:123], a[152:155], a[8:11], 0\n"
      "v_mfma_f32_16x16x32_bf16 v[120:123], a[156:159], a[24:27], v[120:123]\n"
      "global_load_dwordx4 a[104:107], v3, s[46:47] offset:2048 sc1\n"
      "v_mfma_f32_16x16x32_bf16 v[108:111], a[144:147], a[12:15], 0\n"
      "v_mfma_f32_16x16x32_bf16 v[108:111], a[148:151], a[28:31], v[108:111]\n"
      "v_mfma_f32_16x16x32_bf16 v[124:127], a[152:155], a[12:15], 0\n"
      "v_mfma_f32_16x16x32_bf16 v[124:127], a[156:159], a[28:31], v[124:127]\n"
      "global_load_dwordx4 a[120:123], v3, s[46:47] offset:3072 sc1\n"
      "s_waitcnt vmcnt(15)\n"
      "v_mfma_f32_16x16x32_bf16 v[64:67], a[160:163], a[32:35], v[64:67]\n"
      "v_mfma_f32_16x16x32_bf16 v[64:67], a[164:167], a[48:51], v[64:67]\n"
      "v_mfma_f32_16x16x32_bf16 v[80:83], a[168:171], a[32:35], v[80:83]\n"
      "v_mfma_f32_16x16x32_bf16 v[80:83], a[172:175], a[48:51], v[80:83]\n"
      "global_load_dwordx4 a[108:111], v4, s[46:47] offset:2048 sc1\n"
      "v_mfma_f32_16x16x32_bf16 v[68:71], a[160:163], a[36:39], v[68:71]\n"
      "v_mfma_f32_16x16x32_bf16 v[68:71], a[164:167], a[52:55], v[68:71]\n"
      "v_mfma_f32_16x16x32_bf16 v[84:87], a[168:171], a[36:39], v[84:87]\n"
      "v_mfma_f32_16x16x32_bf16 v[84:87], a[172:175], a[52:55], v[84:87]\n"
      "global_load_dwordx4 a[124:127], v4, s[46:47] offset:3072 sc1\n"
      "v_mfma_f32_16x16x32_bf16 v[72:75], a[160:163], a[40:43], v[72:75]\n"
      "s_waitcnt vmcnt(8)\n"
      "v_cmp_ne_u32 vcc, s13, v238\n"
      "s_nop 0\n"
      "s_cbranch_vccnz .Lgm_slow2\n"
      ".Lgm_back2:\n"
      "v_mfma_f32_16x16x32_bf16 v[72:75], a[164:167], a[56:59], v[72:75]\n"
      "s_add_u32 s46, s44, s1\n"
      "s_addc_u32 s47, s45, 0\n"
      "v_mfma_f32_16x16x32_bf16 v[88:91], a[168:171], a[40:43], v[88:91]\n"
      "v_mfma_f32_16x16x32_bf16 v[88:91], a[172:175], a[56:59], v[88:91]\n"
      "global_load_dwordx4 a[128:131], v24, s[46:47] offset:0 sc1\n"
      "s_add_u32 s1, s0, 3\n"
      "v_mfma_f32_16x16x32_bf16 v[76:79], a[160:163], a[44:47], v[76:79]\n"
      "s_and_b32 s1, s1, 7\n"
      "s_lshl_b32 s1, s1, 12\n"
      "v_mfma_f32_16x16x32_bf16 v[76:79], a[164:167], a[60:63], v[76:79]\n"
      "v_mfma_f32_16x16x32_bf16 v[92:95], a[168:171], a[44:47], v[92:95]\n"
      "v_mfma_f32_16x16x32_bf16 v[92:95], a[172:175], a[60:63], v[92:95]\n"
      "global_load_dwordx4 a[144:147], v24, s[46:47] offset:1024 sc1\n"
      "v_mfma_f32_16x16x32_bf16 v[96:99], a[176:179], a[32:35], v[96:99]\n"
      "v_mfma_f32_16x16x32_bf16 v[96:99], a[180:183], a[48:51], v[96:99]\n"
      "v_mfma_f32_16x16x32_bf16 v[112:115], a[184:187], a[32:35], v[112:115]\n"
      "v_mfma_f32_16x16x32_bf16 v[112:115], a[188:191], a[48:51], v[112:115]\n"
      "global_load_dwordx4 a[132:135], v2, s[46:47] offset:0 sc1\n"
      "v_mfma_f32_16x16x32_bf16 v[100:103], a[176:179], a[36:39], v[100:103]\n"
      "v_mfma_f32_16x16x32_bf16 v[100:103], a[180:183], a[52:55], v[100:103]\n"
      "v_mfma_f32_16x16x32_bf16 v[116:119], a[184:187], a[36:39], v[116:119]\n"
      "v_mfma_f32_16x16x32_bf16 v[116:119], a[188:191], a[52:55], v[116:119]\n"
      "global_load_dwordx4 a[148:151], v2, s[46:47] offset:1024 sc1\n"
      "v_mfma_f32_16x16x32_bf16 v[104:107], a[176:179], a[40:43], v[104:107]\n"
      "v_mfma_f32_16x16x32_bf16 v[104:107], a[180:183], a[56:59], v[104:107]\n"
      "v_mfma_f32_16x16x32_bf16 v[120:123], a[184:187], a[40:43], v[120:123]\n"
      "v_mfma_f32_16x16x32_bf16 v[120:123], a[188:191], a[56:59], v[120:123]\n"
      "global_load_dwordx4 a[136:139], v3, s[46:47] offset:0 sc1\n"
      "v_mfma_f32_16x16x32_bf16 v[108:111], a[176:179], a[44:47], v[108:111]\n"
      "v_mfma_f32_16x16x32_bf16 v[108:111], a[180:183], a[60:63], v[108:111]\n"
      "v_mfma_f32_16x16x32_bf16 v[124:127], a[184:187], a[44:47], v[124:127]\n"
      "v_mfma_f32_16x16x32_bf16 v[124:127], a[188:191], a[60:63], v[124:127]\n"
      "global_load_dwordx4 a[152:155], v3, s[46:47] offset:1024 sc1\n"
      "v_mfma_f32_16x16x32_bf16 v[64:67], a[192:195], a[64:67], v[64:67]\n"
      "v_mfma_f32_16x16x32_bf16 v[64:67], a[196:199], a[80:83], v[64:67]\n"
      "v_mfma_f32_16x16x32_bf16 v[80:83], a[200:203], a[64:67], v[80:83]\n"
      "v_mfma_f32_16x16x32_bf16 v[80:83], a[204:207], a[80:83], v[80:83]\n"
      "global_load_dwordx4 a[140:143], v4, s[46:47] offset:0 sc1\n"
      "v_mfma_f32_16x16x32_bf16 v[68:71], a[192:195], a[68:71], v[68:71]\n"
      "v_mfma_f32_16x16x32_bf16 v[68:71], a[196:199], a[84:87], v[68:71]\n"
      "v_mfma_f32_16x16x32_bf16 v[84:87], a[200:203], a[68:71], v[84:87]\n"
      "v_mfma_f32_16x16x32_bf16 v[84:87], a[204:207], a[84:87], v[84:87]\n"
      "global_load_dwordx4 a[156:159], v4, s[46:47] offset:1024 sc1\n"
      "v_mfma_f32_16x16x32_bf16 v[72:75], a[192:195], a[72:75], v[72:75]\n"
      "v_mfma_f32_16x16x32_bf16 v[72:75], a[196:199], a[88:91], v[72:75]\n"
      "v_mfma_f32_16x16x32_bf16 v[88:91], a[200:203], a[72:75], v[88:91]\n"
      "v_mfma_f32_16x16x32_bf16 v[88:91], a[204:207], a[88:91], v[88:91]\n"
      "global_load_dwordx4 a[160:163], v24, s[46:47] offset:2048 sc1\n"
      "v_mfma_f32_16x16x32_bf16 v[76:79], a[192:195], a[76:79], v[76:79]\n"
      "v_mfma_f32_16x16x32_bf16 v[76:79], a[196:199], a[92:95], v[76:79]\n"
      "v_mfma_f32_16x16x32_bf16 v[92:95], a[200:203], a[76:79], v[92:95]\n"
      "v_mfma_f32_16x16x32_bf16 v[92:95], a[204:207], a[92:95], v[92:95]\n"
      "global_load_dwordx4 a[176:179], v24, s[46:47] offset:3072 sc1\n"
      "v_mfma_f32_16x16x32_bf16 v[96:99], a[208:211], a[64:67], v[96:99]\n"
      "v_mfma_f32_16x16x32_bf16 v[96:99], a[212:215], a[80:83], v[96:99]\n"
      "v_mfma_f32_16x16x32_bf16 v[112:115], a[216:219], a[64:67], v[112:115]\n"
      "v_mfma_f32_16x16x32_bf16 v[112:115], a[220:223], a[80:83], v[112:115]\n"
      "global_load_dwordx4 a[164:167], v2, s[46:47] offset:2048 sc1\n"
      "v_mfma_f32_16x16x32_bf16 v[100:103], a[208:211], a[68:71], v[100:103]\n"
      "v_mfma_f32_16x16x32_bf16 v[100:103], a[212:215], a[84:87], v[100:103]\n"
      "v_mfma_f32_16x16x32_bf16 v[116:119], a[216:219], a[68:71], v[116:119]\n"
      "v_mfma_f32_16x16x32_bf16 v[116:119], a[220:223], a[84:87], v[116:119]\n"
      "global_load_dwordx4 a[180:183], v2, s[46:47] offset:3072 sc1\n"
      "v_mfma_f32_16x16x32_bf16 v[104:107], a[208:211], a[72:75], v[104:107]\n"
      "v_mfma_f32_16x16x32_bf16 v[104:107], a[212:215], a[88:91], v[104:107]\n"
      "v_mfma_f32_16x16x32_bf16 v[120:123], a[216:219], a[72:75], v[120:123]\n"
      "v_mfma_f32_16x16x32_bf16 v[120:123], a[220:223], a[88:91], v[120:123]\n"
      "global_load_dwordx4 a[168:171], v3, s[46:47] offset:2048 sc1\n"
      "v_mfma_f32_16x16x32_bf16 v[108:111], a[208:211], a[76:79], v[108:111]\n"
      "v_mfma_f32_16x16x32_bf16 v[108:111], a[212:215], a[92:95], v[108:111]\n"
      "v_mfma_f32_16x16x32_bf16 v[124:127], a[216:219], a[76:79], v[124:127]\n"
      "v_mfma_f32_16x16x32_bf16 v[124:127], a[220:223], a[92:95], v[124:127]\n"
      "global_load_dwordx4 a[184:187], v3, s[46:47] offset:3072 sc1\n"
      "s_waitcnt vmcnt(14)\n"
      "v_mfma_f32_16x16x32_bf16 v[64:67], a[224:227], a[96:99], v[64:67]\n"
      "v_mfma_f32_16x16x32_bf16 v[64:67], a[228:231], a[112:115], v[64:67]\n"
      "v_mfma_f32_16x16x32_bf16 v[80:83], a[232:235], a[96:99], v[80:83]\n"
      "v_mfma_f32_16x16x32_bf16 v[80:83], a[236:239], a[112:115], v[80:83]\n"
      "global_load_dwordx4 a[172:175], v4, s[46:47] offset:2048 sc1\n"
      "v_mfma_f32_16x16x32_bf16 v[68:71], a[224:227], a[100:103], v[68:71]\n"
      "v_mfma_f32_16x16x32_bf16 v[68:71], a[228:231], a[116:119], v[68:71]\n"
      "v_mfma_f32_16x16x32_bf16 v[84:87], a[232:235], a[100:103], v[84:87]\n"
      "v_mfma_f32_16x16x32_bf16 v[84:87], a[236:239], a[116:119], v[84:87]\n"
      "global_load_dwordx4 a[188:191], v4, s[46:47] offset:3072 sc1\n"
      "v_mfma_f32_16x16x32_bf16 v[72:75], a[224:227], a[104:107], v[72:75]\n"
      "s_add_u32 s46, s44, s1\n"
      "s_addc_u32 s47, s45, 0\n"
      "v_mfma_f32_16x16x32_bf16 v[72:75], a[228:231], a[120:123], v[72:75]\n"
      "v_mfma_f32_16x16x32_bf16 v[88:91], a[232:235], a[104:107], v[88:91]\n"
      "v_mfma_f32_16x16x32_bf16 v[88:91], a[236:239], a[120:123], v[88:91]\n"
      "global_load_dwordx4 a[192:195], v24, s[46:47] offset:0 sc1\n"
      "s_add_u32 s1, s0, 4\n"
      "v_mfma_f32_16x16x32_bf16 v[76:79], a[224:227], a[108:111], v[76:79]\n"
      "s_and_b32 s1, s1, 7\n"
      "s_lshl_b32 s1, s1, 12\n"
      "v_mfma_f32_16x16x32_bf16 v[76:79], a[228:231], a[124:127], v[76:79]\n"
      "v_mfma_f32_16x16x32_bf16 v[92:95], a[232:235], a[108:111], v[92:95]\n"
      "v_mfma_f32_16x16x32_bf16 v[92:95], a[236:239], a[124:127], v[92:95]\n"
      "global_load_dwordx4 a[208:211], v24, s[46:47] offset:1024 sc1\n"
      "v_mfma_f32_16x16x32_bf16 v[96:99], a[240:243], a[96:99], v[96:99]\n"
      "v_mfma_f32_16x16x32_bf16 v[96:99], a[244:247], a[112:115], v[96:99]\n"
      "v_mfma_f32_16x16x32_bf16 v[112:115], a[248:251], a[96:99], v[112:115]\n"
      "v_mfma_f32_16x16x32_bf16 v[112:115], a[252:255], a[112:115], v[112:115]\n"
      "global_load_dwordx4 a[196:199], v2, s[46:47] offset:0 sc1\n"
      "v_mfma_f32_16x16x32_bf16 v[100:103], a[240:243], a[100:103], v[100:103]\n"
      "v_mfma_f32_16x16x32_bf16 v[100:103], a[244:247], a[116:119], v[100:103]\n"
      "v_mfma_f32_16x16x32_bf16 v[116:119], a[248:251], a[100:103], v[116:119]\n"
      "v_mfma_f32_16x16x32_bf16 v[116:119], a[252:255], a[116:119], v[116:119]\n"
      "global_load_dwordx4 a[212:215], v2, s[46:47] offset:1024 sc1\n"
      "v_mfma_f32_16x16x32_bf16 v[104:107], a[240:243], a[104:107], v[104:107]\n"
      "v_mfma_f32_16x16x32_bf16 v[104:107], a[244:247], a[120:123], v[104:107]\n"
      "v_mfma_f32_16x16x32_bf16 v[120:123], a[248:251], a[104:107], v[120:123]\n"
      "v_mfma_f32_16x16x32_bf16 v[120:123], a[252:255], a[120:123], v[120:123]\n"
      "global_load_dwordx4 a[200:203], v3, s[46:47] offset:0 sc1\n"
      "v_mfma_f32_16x16x32_bf16 v[108:111], a[240:243], a[108:111], v[108:111]\n"
      "v_mfma_f32_16x16x32_bf16 v[108:111], a[244:247], a[124:127], v[108:111]\n"
      "v_mfma_f32_16x16x32_bf16 v[124:127], a[248:251], a[108:111], v[124:127]\n"
      "v_mfma_f32_16x16x32_bf16 v[124:127], a[252:255], a[124:127], v[124:127]\n"
      "global_load_dwordx4 a[216:219], v3, s[46:47] offset:1024 sc1\n"
      "s_waitcnt vmcnt(14)\n"
      "v_mfma_f32_16x16x32_bf16 v[64:67], v[128:131], a[128:131], v[64:67]\n"
      "v_mfma_f32_16x16x32_bf16 v[64:67], v[132:135], a[144:147], v[64:67]\n"
      "v_mfma_f32_16x16x32_bf16 v[80:83], v[136:139], a[128:131], v[80:83]\n"
      "v_mfma_f32_16x16x32_bf16 v[80:83], v[140:143], a[144:147], v[80:83]\n"
      "global_load_dwordx4 a[204:207], v4, s[46:47] offset:0 sc1\n"
      "v_mfma_f32_16x16x32_bf16 v[68:71], v[128:131], a[132:135], v[68:71]\n"
      "v_mfma_f32_16x16x32_bf16 v[68:71], v[132:135], a[148:151], v[68:71]\n"
      "v_mfma_f32_16x16x32_bf16 v[84:87], v[136:139], a[132:135], v[84:87]\n"
      "v_mfma_f32_16x16x32_bf16 v[84:87], v[140:143], a[148:151], v[84:87]\n"
      "global_load_dwordx4 a[220:223], v4, s[46:47] offset:1024 sc1\n"
      "v_mfma_f32_16x16x32_bf16 v[72:75], v[128:131], a[136:139], v[72:75]\n"
      "v_mfma_f32_16x16x32_bf16 v[72:75], v[132:135], a[152:155], v[72:75]\n"
      "v_mfma_f32_16x16x32_bf16 v[88:91], v[136:139], a[136:139], v[88:91]\n"
      "v_mfma_f32_16x16x32_bf16 v[88:91], v[140:143], a[152:155], v[88:91]\n"
      "global_load_dwordx4 a[224:227], v24, s[46:47] offset:2048 sc1\n"
      "v_mfma_f32_16x16x32_bf16 v[76:79], v[128:131], a[140:143], v[76:79]\n"
      "v_mfma_f32_16x16x32_bf16 v[76:79], v[132:135], a[156:159], v[76:79]\n"
      "v_mfma_f32_16x16x32_bf16 v[92:95], v[136:139], a[140:143], v[92:95]\n"
      "v_mfma_f32_16x16x32_bf16 v[92:95], v[140:143], a[156:159], v[92:95]\n"
      "global_load_dwordx4 a[240:243], v24, s[46:47] offset:3072 sc1\n"
      ".Lgm_w8:\n"
      "s_cbranch_execz .Lgm_r2\n"
      "v_mfma_f32_16x16x32_bf16 v[96:99], v[144:147], a[128:131], v[96:99]\n"
      "v_mfma_f32_16x16x32_bf16 v[96:99], v[148:151], a[144:147], v[96:99]\n"
      "v_mfma_f32_16x16x32_bf16 v[112:115], v[152:155], a[128:131], v[112:115]\n"
      "v_mfma_f32_16x16x32_bf16 v[112:115], v[156:159], a[144:147], v[112:115]\n"
      "global_load_dwordx4 a[228:231], v2, s[46:47] offset:2048 sc1\n"
      "v_mfma_f32_16x16x32_bf16 v[100:103], v[144:147], a[132:135], v[100:103]\n"
      "v_mfma_f32_16x16x32_bf16 v[100:103], v[148:151], a[148:151], v[100:103]\n"
      "v_mfma_f32_16x16x32_bf16 v[116:119], v[152:155], a[132:135], v[116:119]\n"
      "v_mfma_f32_16x16x32_bf16 v[116:119], v[156:159], a[148:151], v[116:119]\n"
      "global_load_dwordx4 a[244:247], v2, s[46:47] offset:3072 sc1\n"
      "v_mfma_f32_16x16x32_bf16 v[104:107], v[144:147], a[136:139], v[104:107]\n"
      "v_mfma_f32_16x16x32_bf16 v[104:107], v[148:151], a[152:155], v[104:107]\n"
      "v_mfma_f32_16x16x32_bf16 v[120:123], v[152:155], a[136:139], v[120:123]\n"
      "v_mfma_f32_16x16x32_bf16 v[120:123], v[156:159], a[152:155], v[120:123]\n"
      "global_load_dwordx4 a[232:235], v3, s[46:47] offset:2048 sc1\n"
      "v_mfma_f32_16x16x32_bf16 v[108:111], v[144:147], a[140:143], v[108:111]\n"
      "v_mfma_f32_16x16x32_bf16 v[108:111], v[148:151], a[156:159], v[108:111]\n"
      "v_mfma_f32_16x16x32_bf16 v[124:127], v[152:155], a[140:143], v[124:127]\n"
      "v_mfma_f32_16x16x32_bf16 v[124:127], v[156:159], a[156:159], v[124:127]\n"
      "global_load_dwordx4 a[248:251], v3, s[46:47] offset:3072 sc1\n"
      "s_waitcnt vmcnt(14)\n"
      "v_mfma_f32_16x16x32_bf16 v[64:67], v[160:163], a[160:163], v[64:67]\n"
      "v_mfma_f32_16x16x32_bf16 v[64:67], v[164:167], a[176:179], v[64:67]\n"
      "v_mfma_f32_16x16x32_bf16 v[80:83], v[168:171], a[160:163], v[80:83]\n"
      "v_mfma_f32_16x16x32_bf16 v[80:83], v[172:175], a[176:179], v[80:83]\n"
      "global_load_dwordx4 a[236:239], v4, s[46:47] offset:2048 sc1\n"
      "v_mfma_f32_16x16x32_bf16 v[68:71], v[160:163], a[164:167], v[68:71]\n"
      "v_mfma_f32_16x16x32_bf16 v[68:71], v[164:167], a[180:183], v[68:71]\n"
      "v_mfma_f32_16x16x32_bf16 v[84:87], v[168:171], a[164:167], v[84:87]\n"
      "v_mfma_f32_16x16x32_bf16 v[84:87], v[172:175], a[180:183], v[84:87]\n"
      "global_load_dwordx4 a[252:255], v4, s[46:47] offset:3072 sc1\n"
      "v_mfma_f32_16x16x32_bf16 v[72:75], v[160:163], a[168:171], v[72:75]\n"
      "s_add_u32 s46, s44, s1\n"
      "s_addc_u32 s47, s45, 0\n"
      "v_mfma_f32_16x16x32_bf16 v[72:75], v[164:167], a[184:187], v[72:75]\n"
      "v_mfma_f32_16x16x32_bf16 v[88:91], v[168:171], a[168:171], v[88:91]\n"
      "v_mfma_f32_16x16x32_bf16 v[88:91], v[172:175], a[184:187], v[88:91]\n"
      "global_load_dwordx4 a[0:3], v24, s[46:47] offset:0 sc1\n"
      "s_add_u32 s1, s0, 5\n"
      "v_mfma_f32_16x16x32_bf16 v[76:79], v[160:163], a[172:175], v[76:79]\n"
      "s_and_b32 s1, s1, 7\n"
      "s_lshl_b32 s1, s1, 12\n"
      "v_mfma_f32_16x16x32_bf16 v[76:79], v[164:167], a[188:191], v[76:79]\n"
      "v_mfma_f32_16x16x32_bf16 v[92:95], v[168:171], a[172:175], v[92:95]\n"
      "v_mfma_f32_16x16x32_bf16 v[92:95], v[172:175], a[188:191], v[92:95]\n"
      "global_load_dwordx4 a[16:19], v24, s[46:47] offset:1024 sc1\n"
      "v_mfma_f32_16x16x32_bf16 v[96:99], v[176:179], a[160:163], v[96:99]\n"
      "v_mfma_f32_16x16x32_bf16 v[96:99], v[180:183], a[176:179], v[96:99]\n"
      "ds_read_b64_tr_b16 v[192:193], v56 offset:0\n"
      "v_mfma_f32_16x16x32_bf16 v[112:115], v[184:187], a[160:163], v[112:115]\n"
      "ds_read_b64_tr_b16 v[194:195], v56 offset:1024\n"
      "v_mfma_f32_16x16x32_bf16 v[112:115], v[188:191], a[176:179], v[112:115]\n"
      "global_load_dwordx4 a[4:7], v2, s[46:47] offset:0 sc1\n"
      "v_mfma_f32_16x16x32_bf16 v[100:103], v[176:179], a[164:167], v[100:103]\n"
      "ds_read_b64_tr_b16 v[196:197], v56 offset:4096\n"
      "v_mfma_f32_16x16x32_bf16 v[100:103], v[180:183], a[180:183], v[100:103]\n"
      "ds_read_b64_tr_b16 v[198:199], v56 offset:5120\n"
      "v_mfma_f32_16x16x32_bf16 v[116:119], v[184:187], a[164:167], v[116:119]\n"
      "ds_read_b64_tr_b16 v[200:201], v57 offset:0\n"
      "v_mfma_f32_16x16x32_bf16 v[116:119], v[188:191], a[180:183], v[116:119]\n"
      "global_load_dwordx4 a[20:23], v2, s[46:47] offset:1024 sc1\n"
      "v_mfma_f32_16x16x32_bf16 v[104:107], v[176:179], a[168:171], v[104:107]\n"
      "ds_read_b64_tr_b16 v[202:203], v57 offset:1024\n"
      "v_mfma_f32_16x16x32_bf16 v[104:107], v[180:183], a[184:187], v[104:107]\n"
      "ds_read_b64_tr_b16 v[204:205], v57 offset:4096\n"
      "v_mfma_f32_16x16x32_bf16 v[120:123], v[184:187], a[168:171], v[120:123]\n"
      "ds_read_b64_tr_b16 v[206:207], v57 offset:5120\n"
      "v_mfma_f32_16x16x32_bf16 v[120:123], v[188:191], a[184:187], v[120:123]\n"
      "global_load_dwordx4 a[8:11], v3, s[46:47] offset:0 sc1\n"
      "v_mfma_f32_16x16x32_bf16 v[108:111], v[176:179], a[172:175], v[108:111]\n"
      "v_mfma_f32_16x16x32_bf16 v[108:111], v[180:183], a[188:191], v[108:111]\n"
      "v_mfma_f32_16x16x32_bf16 v[124:127], v[184:187], a[172:175], v[124:127]\n"
      "v_mfma_f32_16x16x32_bf16 v[124:127], v[188:191], a[188:191], v[124:127]\n"
      "global_load_dwordx4 a[24:27], v3, s[46:47] offset:1024 sc1\n"
      "s_waitcnt vmcnt(14) lgkmcnt(0)\n"
      "v_mfma_f32_16x16x32_bf16 v[64:67], v[192:195], a[192:195], v[64:67]\n"
      "v_mfma_f32_16x16x32_bf16 v[64:67], v[196:199], a[208:211], v[64:67]\n"
      "ds_read_b64_tr_b16 v[208:209], v56 offset:512\n"
      "v_mfma_f32_16x16x32_bf16 v[80:83], v[200:203], a[192:195], v[80:83]\n"
      "ds_read_b64_tr_b16 v[210:211], v56 offset:1536\n"
      "v_mfma_f32_16x16x32_bf16 v[80:83], v[204:207], a[208:211], v[80:83]\n"
      "global_load_dwordx4 a[12:15], v4, s[46:47] offset:0 sc1\n"
      "v_mfma_f32_16x16x32_bf16 v[68:71], v[192:195], a[196:199], v[68:71]\n"
      "ds_read_b64_tr_b16 v[212:213], v56 offset:4608\n"
      "v_mfma_f32_16x16x32_bf16 v[68:71], v[196:199], a[212:215], v[68:71]\n"
      "ds_read_b64_tr_b16 v[214:215], v56 offset:5632\n"
      "v_mfma_f32_16x16x32_bf16 v[84:87], v[200:203], a[196:199], v[84:87]\n"
      "ds_read_b64_tr_b16 v[216:217], v57 offset:512\n"
      "v_mfma_f32_16x16x32_bf16 v[84:87], v[204:207], a[212:215], v[84:87]\n"
      "global_load_dwordx4 a[28:31], v4, s[46:47] offset:1024 sc1\n"
      "v_mfma_f32_16x16x32_bf16 v[72:75], v[192:195], a[200:203], v[72:75]\n"
      "ds_read_b64_tr_b16 v[218:219], v57 offset:1536\n"
      "v_mfma_f32_16x16x32_bf16 v[72:75], v[196:199], a[216:219], v[72:75]\n"
      "ds_read_b64_tr_b16 v[220:221], v57 offset:4608\n"
      "v_mfma_f32_16x16x32_bf16 v[88:91], v[200:203], a[200:203], v[88:91]\n"
      "ds_read_b64_tr_b16 v[222:223], v57 offset:5632\n"
      "v_mfma_f32_16x16x32_bf16 v[88:91], v[204:207], a[216:219], v[88:91]\n"
      "global_load_dwordx4 a[32:35], v24, s[46:47] offset:2048 sc1\n"
      "v_mfma_f32_16x16x32_bf16 v[76:79], v[192:195], a[204:207], v[76:79]\n"
      "global_load_dword v224, v237, s[64:65] offset:0 sc1\n"
      "v_mfma_f32_16x16x32_bf16 v[76:79], v[196:199], a[220:223], v[76:79]\n"
      "global_load_dword v225, v237, s[64:65] offset:64 sc1\n"
      "v_mfma_f32_16x16x32_bf16 v[92:95], v[200:203], a[204:207], v[92:95]\n"
      "global_load_dword v226, v237, s[64:65] offset:128 sc1\n"
      "v_mfma_f32_16x16x32_bf16 v[92:95], v[204:207], a[220:223], v[92:95]\n"
      "global_load_dwordx4 a[48:51], v24, s[46:47] offset:3072 sc1\n"
      "s_waitcnt lgkmcnt(0)\n"
      "v_mfma_f32_16x16x32_bf16 v[96:99], v[208:211], a[192:195], v[96:99]\n"
      "global_load_dword v227, v237, s[64:65] offset:192 sc1\n"
      "s_add_u32 s90, s99, 0x8000\n"
      "v_mfma_f32_16x16x32_bf16 v[96:99], v[212:215], a[208:211], v[96:99]\n"
      "ds_read_b64_tr_b16 v[192:193], v56 offset:8192\n"
      "v_mul_u32_u24 v234, 20, v237\n"
      "v_mfma_f32_16x16x32_bf16 v[112:115], v[216:219], a[192:195], v[112:115]\n"
      "ds_read_b64_tr_b16 v[194:195], v56 offset:9216\n"
      "v_lshrrev_b32 v236, 1, v28\n"
      "v_mfma_f32_16x16x32_bf16 v[112:115], v[220:223], a[208:211], v[112:115]\n"
      "global_load_dwordx4 a[36:39], v2, s[46:47] offset:2048 sc1\n"
      "v_add3_u32 v234, v234, v236, s90\n"
      "v_mfma_f32_16x16x32_bf16 v[100:103], v[208:211], a[196:199], v[100:103]\n"
      "ds_read_b64_tr_b16 v[196:197], v56 offset:12288\n"
      "v_lshrrev_b32 v235, 6, v24\n"
      "v_mfma_f32_16x16x32_bf16 v[100:103], v[212:215], a[212:215], v[100:103]\n"
      "ds_read_b64_tr_b16 v[198:199], v56 offset:13312\n"
      "v_and_b32 v237, 48, v24\n"
      "v_mfma_f32_16x16x32_bf16 v[116:119], v[216:219], a[196:199], v[116:119]\n"
      "ds_read_b64_tr_b16 v[200:201], v57 offset:8192\n"
      "v_lshl_or_b32 v236, v235, 11, v237\n"
      "v_mfma_f32_16x16x32_bf16 v[116:119], v[220:223], a[212:215], v[116:119]\n"
      "global_load_dwordx4 a[52:55], v2, s[46:47] offset:3072 sc1\n"
      "v_mul_u32_u24 v235, 0x50, v235\n"
      "v_mfma_f32_16x16x32_bf16 v[104:107], v[208:211], a[200:203], v[104:107]\n"
      "ds_read_b64_tr_b16 v[202:203], v57 offset:9216\n"
      "v_add3_u32 v235, v235, v237, s90\n"
      "v_mfma_f32_16x16x32_bf16 v[104:107], v[212:215], a[216:219], v[104:107]\n"
      "ds_read_b64_tr_b16 v[204:205], v57 offset:12288\n"
      "s_add_u32 s90, s99, 0\n"
      "v_mfma_f32_16x16x32_bf16 v[120:123], v[216:219], a[200:203], v[120:123]\n"
      "ds_read_b64_tr_b16 v[206:207], v57 offset:13312\n"
      "v_add_u32 v232, s90, v24\n"
      "v_mfma_f32_16x16x32_bf16 v[120:123], v[220:223], a[216:219], v[120:123]\n"
      "global_load_dwordx4 a[40:43], v3, s[46:47] offset:2048 sc1\n"
      "v_xor_b32 v233, 0x4000, v232\n"
      "v_mfma_f32_16x16x32_bf16 v[108:111], v[208:211], a[204:207], v[108:111]\n"
      "s_add_u32 s80, s66, 0x8000\n"
      "s_addc_u32 s81, s67, 0\n"
      "v_mfma_f32_16x16x32_bf16 v[108:111], v[212:215], a[220:223], v[108:111]\n"
      "s_add_u32 s82, s66, 0x10000\n"
      "s_addc_u32 s83, s67, 0\n"
      "v_mfma_f32_16x16x32_bf16 v[124:127], v[216:219], a[204:207], v[124:127]\n"
      "s_add_u32 s84, s66, 0x18000\n"
      "s_addc_u32 s85, s67, 0\n"
      "v_mfma_f32_16x16x32_bf16 v[124:127], v[220:223], a[220:223], v[124:127]\n"
      "global_load_dwordx4 a[56:59], v3, s[46:47] offset:3072 sc1\n"
      "s_waitcnt vmcnt(18) lgkmcnt(0)\n"
      "v_mfma_f32_16x16x32_bf16 v[64:67], v[192:195], a[224:227], v[64:67]\n"
      "v_mfma_f32_16x16x32_bf16 v[64:67], v[196:199], a[240:243], v[64:67]\n"
      "ds_read_b64_tr_b16 v[208:209], v56 offset:8704\n"
      "v_mfma_f32_16x16x32_bf16 v[80:83], v[200:203], a[224:227], v[80:83]\n"
      "ds_read_b64_tr_b16 v[210:211], v56 offset:9728\n"
      "v_mfma_f32_16x16x32_bf16 v[80:83], v[204:207], a[240:243], v[80:83]\n"
      "global_load_dwordx4 a[44:47], v4, s[46:47] offset:2048 sc1\n"
      "v_mfma_f32_16x16x32_bf16 v[68:71], v[192:195], a[228:231], v[68:71]\n"
      "ds_read_b64_tr_b16 v[212:213], v56 offset:12800\n"
      "v_mfma_f32_16x16x32_bf16 v[68:71], v[196:199], a[244:247], v[68:71]\n"
      "ds_read_b64_tr_b16 v[214:215], v56 offset:13824\n"
      "v_mfma_f32_16x16x32_bf16 v[84:87], v[200:203], a[228:231], v[84:87]\n"
      "ds_read_b64_tr_b16 v[216:217], v57 offset:8704\n"
      "v_mfma_f32_16x16x32_bf16 v[84:87], v[204:207], a[244:247], v[84:87]\n"
      "global_load_dwordx4 a[60:63], v4, s[46:47] offset:3072 sc1\n"
      "v_mfma_f32_16x16x32_bf16 v[72:75], v[192:195], a[232:235], v[72:75]\n"
      "ds_read_b64_tr_b16 v[218:219], v57 offset:9728\n"
      "v_mfma_f32_16x16x32_bf16 v[72:75], v[196:199], a[248:251], v[72:75]\n"
      "ds_read_b64_tr_b16 v[220:221], v57 offset:12800\n"
      "v_mfma_f32_16x16x32_bf16 v[88:91], v[200:203], a[232:235], v[88:91]\n"
      "ds_read_b64_tr_b16 v[222:223], v57 offset:13824\n"
      "v_mfma_f32_16x16x32_bf16 v[88:91], v[204:207], a[248:251], v[88:91]\n"
      "s_add_u32 s46, s44, s1\n"
      "s_addc_u32 s47, s45, 0\n"
      "v_mfma_f32_16x16x32_bf16 v[76:79], v[192:195], a[236:239], v[76:79]\n"
      "v_mfma_f32_16x16x32_bf16 v[76:79], v[196:199], a[252:255], v[76:79]\n"
      "v_mfma_f32_16x16x32_bf16 v[92:95], v[200:203], a[236:239], v[92:95]\n"
      "v_mfma_f32_16x16x32_bf16 v[92:95], v[204:207], a[252:255], v[92:95]\n"
      "global_load_dwordx4 a[64:67], v24, s[46:47] offset:0 sc1\n"
      "s_waitcnt lgkmcnt(0)\n"
      "v_mfma_f32_16x16x32_bf16 v[96:99], v[208:211], a[224:227], v[96:99]\n"
      "s_add_u32 s1, s0, 6\n"
      "s_and_b32 s1, s1, 7\n"
      "v_mfma_f32_16x16x32_bf16 v[96:99], v[212:215], a[240:243], v[96:99]\n"
      "ds_read_b64_tr_b16 v[192:193], v56 offset:32768\n"
      "s_lshl_b32 s1, s1, 12\n"
      "v_mfma_f32_16x16x32_bf16 v[112:115], v[216:219], a[224:227], v[112:115]\n"
      "ds_read_b64_tr_b16 v[194:195], v56 offset:33792\n"
      "v_mfma_f32_16x16x32_bf16 v[112:115], v[220:223], a[240:243], v[112:115]\n"
      "global_load_dwordx4 a[80:83], v24, s[46:47] offset:1024 sc1\n"
      "v_mfma_f32_16x16x32_bf16 v[100:103], v[208:211], a[228:231], v[100:103]\n"
      "ds_read_b64_tr_b16 v[196:197], v56 offset:36864\n"
      "v_mfma_f32_16x16x32_bf16 v[100:103], v[212:215], a[244:247], v[100:103]\n"
      "ds_read_b64_tr_b16 v[198:199], v56 offset:37888\n"
      "v_mfma_f32_16x16x32_bf16 v[116:119], v[216:219], a[228:231], v[116:119]\n"
      "ds_read_b64_tr_b16 v[200:201], v57 offset:32768\n"
      "v_mfma_f32_16x16x32_bf16 v[116:119], v[220:223], a[244:247], v[116:119]\n"
      "global_load_dwordx4 a[68:71], v2, s[46:47] offset:0 sc1\n"
      "v_mfma_f32_16x16x32_bf16 v[104:107], v[208:211], a[232:235], v[104:107]\n"
      "ds_read_b64_tr_b16 v[202:203], v57 offset:33792\n"
      "v_mfma_f32_16x16x32_bf16 v[104:107], v[212:215], a[248:251], v[104:107]\n"
      "ds_read_b64_tr_b16 v[204:205], v57 offset:36864\n"
      "v_mfma_f32_16x16x32_bf16 v[120:123], v[216:219], a[232:235], v[120:123]\n"
      "ds_read_b64_tr_b16 v[206:207], v57 offset:37888\n"
      "v_mfma_f32_16x16x32_bf16 v[120:123], v[220:223], a[248:251], v[120:123]\n"
      "global_load_dwordx4 a[84:87], v2, s[46:47] offset:1024 sc1\n"
      "v_mfma_f32_16x16x32_bf16 v[108:111], v[208:211], a[236:239], v[108:111]\n"
      "v_mfma_f32_16x16x32_bf16 v[108:111], v[212:215], a[252:255], v[108:111]\n"
      "v_mfma_f32_16x16x32_bf16 v[124:127], v[216:219], a[236:239], v[124:127]\n"
      "v_mfma_f32_16x16x32_bf16 v[124:127], v[220:223], a[252:255], v[124:127]\n"
      "global_load_dwordx4 a[72:75], v3, s[46:47] offset:0 sc1\n"
      "s_waitcnt vmcnt(17) lgkmcnt(0)\n"
      "v_mfma_f32_16x16x32_bf16 v[64:67], v[192:195], a[0:3], v[64:67]\n"
      "v_mfma_f32_16x16x32_bf16 v[64:67], v[196:199], a[16:19], v[64:67]\n"
      "ds_read_b64_tr_b16 v[208:209], v56 offset:33280\n"
      "v_mfma_f32_16x16x32_bf16 v[80:83], v[200:203], a[0:3], v[80:83]\n"
      "ds_read_b64_tr_b16 v[210:211], v56 offset:34304\n"
      "v_mfma_f32_16x16x32_bf16 v[80:83], v[204:207], a[16:19], v[80:83]\n"
      "global_load_dwordx4 a[88:91], v3, s[46:47] offset:1024 sc1\n"
      "v_mfma_f32_16x16x32_bf16 v[68:71], v[192:195], a[4:7], v[68:71]\n"
      "ds_read_b64_tr_b16 v[212:213], v56 offset:37376\n"
      "v_mfma_f32_16x16x32_bf16 v[68:71], v[196:199], a[20:23], v[68:71]\n"
      "ds_read_b64_tr_b16 v[214:215], v56 offset:38400\n"
      "v_mfma_f32_16x16x32_bf16 v[84:87], v[200:203], a[4:7], v[84:87]\n"
      "ds_read_b64_tr_b16 v[216:217], v57 offset:33280\n"
      "v_mfma_f32_16x16x32_bf16 v[84:87], v[204:207], a[20:23], v[84:87]\n"
      "global_load_dwordx4 a[76:79], v4, s[46:47] offset:0 sc1\n"
      "v_mfma_f32_16x16x32_bf16 v[72:75], v[192:195], a[8:11], v[72:75]\n"
      "ds_read_b64_tr_b16 v[218:219], v57 offset:34304\n"
      "v_mfma_f32_16x16x32_bf16 v[72:75], v[196:199], a[24:27], v[72:75]\n"
      "ds_read_b64_tr_b16 v[220:221], v57 offset:37376\n"
      "v_mfma_f32_16x16x32_bf16 v[88:91], v[200:203], a[8:11], v[88:91]\n"
      "ds_read_b64_tr_b16 v[222:223], v57 offset:38400\n"
      "v_mfma_f32_16x16x32_bf16 v[88:91], v[204:207], a[24:27], v[88:91]\n"
      "global_load_dwordx4 a[92:95], v4, s[46:47] offset:1024 sc1\n"
      "v_mfma_f32_16x16x32_bf16 v[76:79], v[192:195], a[12:15], v[76:79]\n"
      "v_mfma_f32_16x16x32_bf16 v[76:79], v[196:199], a[28:31], v[76:79]\n"
      "v_mfma_f32_16x16x32_bf16 v[92:95], v[200:203], a[12:15], v[92:95]\n"
      "v_mfma_f32_16x16x32_bf16 v[92:95], v[204:207], a[28:31], v[92:95]\n"
      "global_load_dwordx4 a[96:99], v24, s[46:47] offset:2048 sc1\n"
      "s_waitcnt lgkmcnt(0)\n"
      "v_mfma_f32_16x16x32_bf16 v[96:99], v[208:211], a[0:3], v[96:99]\n"
      "v_mfma_f32_16x16x32_bf16 v[96:99], v[212:215], a[16:19], v[96:99]\n"
      "ds_read_b64_tr_b16 v[192:193], v56 offset:40960\n"
      "v_mfma_f32_16x16x32_bf16 v[112:115], v[216:219], a[0:3], v[112:115]\n"
      "ds_read_b64_tr_b16 v[194:195], v56 offset:41984\n"
      "v_mfma_f32_16x16x32_bf16 v[112:115], v[220:223], a[16:19], v[112:115]\n"
      "global_load_dwordx4 a[112:115], v24, s[46:47] offset:3072 sc1\n"
      "v_mfma_f32_16x16x32_bf16 v[100:103], v[208:211], a[4:7], v[100:103]\n"
      "ds_read_b64_tr_b16 v[196:197], v56 offset:45056\n"
      "v_mfma_f32_16x16x32_bf16 v[100:103], v[212:215], a[20:23], v[100:103]\n"
      "ds_read_b64_tr_b16 v[198:199], v56 offset:46080\n"
      "v_mfma_f32_16x16x32_bf16 v[116:119], v[216:219], a[4:7], v[116:119]\n"
      "ds_read_b64_tr_b16 v[200:201], v57 offset:40960\n"
      "v_mfma_f32_16x16x32_bf16 v[116:119], v[220:223], a[20:23], v[116:119]\n"
      "global_load_dwordx4 a[100:103], v2, s[46:47] offset:2048 sc1\n"
      "v_mfma_f32_16x16x32_bf16 v[104:107], v[208:211], a[8:11], v[104:107]\n"
      "ds_read_b64_tr_b16 v[202:203], v57 offset:41984\n"
      "v_mfma_f32_16x16x32_bf16 v[104:107], v[212:215], a[24:27], v[104:107]\n"
      "ds_read_b64_tr_b16 v[204:205], v57 offset:45056\n"
      "v_mfma_f32_16x16x32_bf16 v[120:123], v[216:219], a[8:11], v[120:123]\n"
      "ds_read_b64_tr_b16 v[206:207], v57 offset:46080\n"
      "v_mfma_f32_16x16x32_bf16 v[120:123], v[220:223], a[24:27], v[120:123]\n"
      "global_load_dwordx4 a[116:119], v2, s[46:47] offset:3072 sc1\n"
      "v_mfma_f32_16x16x32_bf16 v[108:111], v[208:211], a[12:15], v[108:111]\n"
      "v_mfma_f32_16x16x32_bf16 v[108:111], v[212:215], a[28:31], v[108:111]\n"
      "v_mfma_f32_16x16x32_bf16 v[124:127], v[216:219], a[12:15], v[124:127]\n"
      "v_mfma_f32_16x16x32_bf16 v[124:127], v[220:223], a[28:31], v[124:127]\n"
      "global_load_dwordx4 a[104:107], v3, s[46:47] offset:2048 sc1\n"
      "s_waitcnt vmcnt(13) lgkmcnt(0)\n"
      "v_mfma_f32_16x16x32_bf16 v[64:67], v[192:195], a[32:35], v[64:67]\n"
      "v_mfma_f32_16x16x32_bf16 v[64:67], v[196:199], a[48:51], v[64:67]\n"
      "ds_read_b64_tr_b16 v[208:209], v56 offset:41472\n"
      "v_mfma_f32_16x16x32_bf16 v[80:83], v[200:203], a[32:35], v[80:83]\n"
      "ds_read_b64_tr_b16 v[210:211], v56 offset:42496\n"
      "v_mfma_f32_16x16x32_bf16 v[80:83], v[204:207], a[48:51], v[80:83]\n"
      "global_load_dwordx4 a[120:123], v3, s[46:47] offset:3072 sc1\n"
      "v_mfma_f32_16x16x32_bf16 v[68:71], v[192:195], a[36:39], v[68:71]\n"
      "ds_read_b64_tr_b16 v[212:213], v56 offset:45568\n"
      "v_mfma_f32_16x16x32_bf16 v[68:71], v[196:199], a[52:55], v[68:71]\n"
      "ds_read_b64_tr_b16 v[214:215], v56 offset:46592\n"
      "v_mfma_f32_16x16x32_bf16 v[84:87], v[200:203], a[36:39], v[84:87]\n"
      "ds_read_b64_tr_b16 v[216:217], v57 offset:41472\n"
      "v_mfma_f32_16x16x32_bf16 v[84:87], v[204:207], a[52:55], v[84:87]\n"
      "global_load_dwordx4 a[108:111], v4, s[46:47] offset:2048 sc1\n"
      "v_mfma_f32_16x16x32_bf16 v[72:75], v[192:195], a[40:43], v[72:75]\n"
      "ds_read_b64_tr_b16 v[218:219], v57 offset:42496\n"
      "v_mfma_f32_16x16x32_bf16 v[72:75], v[196:199], a[56:59], v[72:75]\n"
      "ds_read_b64_tr_b16 v[220:221], v57 offset:45568\n"
      "v_mfma_f32_16x16x32_bf16 v[88:91], v[200:203], a[40:43], v[88:91]\n"
      "ds_read_b64_tr_b16 v[222:223], v57 offset:46592\n"
      "v_mfma_f32_16x16x32_bf16 v[88:91], v[204:207], a[56:59], v[88:91]\n"
      "global_load_dwordx4 a[124:127], v4, s[46:47] offset:3072 sc1\n"
      "v_mfma_f32_16x16x32_bf16 v[76:79], v[192:195], a[44:47], v[76:79]\n"
      "s_add_u32 s46, s44, s1\n"
      "s_addc_u32 s47, s45, 0\n"
      "v_mfma_f32_16x16x32_bf16 v[76:79], v[196:199], a[60:63], v[76:79]\n"
      "v_mfma_f32_16x16x32_bf16 v[92:95], v[200:203], a[44:47], v[92:95]\n"
      "v_mfma_f32_16x16x32_bf16 v[92:95], v[204:207], a[60:63], v[92:95]\n"
      "global_load_dwordx4 a[128:131], v24, s[46:47] offset:0 sc1\n"
      "s_waitcnt lgkmcnt(0)\n"
      "v_mfma_f32_16x16x32_bf16 v[96:99], v[208:211], a[32:35], v[96:99]\n"
      "s_barrier\n"
      "s_add_u32 s1, s0, 7\n"
      "s_and_b32 s1, s1, 7\n"
      "v_mfma_f32_16x16x32_bf16 v[96:99], v[212:215], a[48:51], v[96:99]\n"
      "ds_read_b64_tr_b16 v[192:193], v58 offset:0\n"
      "s_lshl_b32 s1, s1, 12\n"
      "v_mfma_f32_16x16x32_bf16 v[112:115], v[216:219], a[32:35], v[112:115]\n"
      "ds_read_b64_tr_b16 v[194:195], v58 offset:1024\n"
      "v_mfma_f32_16x16x32_bf16 v[112:115], v[220:223], a[48:51], v[112:115]\n"
      "global_load_dwordx4 a[144:147], v24, s[46:47] offset:1024 sc1\n"
      "v_mfma_f32_16x16x32_bf16 v[100:103], v[208:211], a[36:39], v[100:103]\n"
      "ds_read_b64_tr_b16 v[196:197], v58 offset:4096\n"
      "v_mfma_f32_16x16x32_bf16 v[100:103], v[212:215], a[52:55], v[100:103]\n"
      "ds_read_b64_tr_b16 v[198:199], v58 offset:5120\n"
      "v_mfma_f32_16x16x32_bf16 v[116:119], v[216:219], a[36:39], v[116:119]\n"
      "ds_read_b64_tr_b16 v[200:201], v59 offset:0\n"
      "v_mfma_f32_16x16x32_bf16 v[116:119], v[220:223], a[52:55], v[116:119]\n"
      "global_load_dwordx4 a[132:135], v2, s[46:47] offset:0 sc1\n"
      "v_mfma_f32_16x16x32_bf16 v[104:107], v[208:211], a[40:43], v[104:107]\n"
      "ds_read_b64_tr_b16 v[202:203], v59 offset:1024\n"
      "v_mfma_f32_16x16x32_bf16 v[104:107], v[212:215], a[56:59], v[104:107]\n"
      "ds_read_b64_tr_b16 v[204:205], v59 offset:4096\n"
      "v_mfma_f32_16x16x32_bf16 v[120:123], v[216:219], a[40:43], v[120:123]\n"
      "ds_read_b64_tr_b16 v[206:207], v59 offset:5120\n"
      "v_mfma_f32_16x16x32_bf16 v[120:123], v[220:223], a[56:59], v[120:123]\n"
      "global_load_dwordx4 a[148:151], v2, s[46:47] offset:1024 sc1\n"
      "v_mfma_f32_16x16x32_bf16 v[108:111], v[208:211], a[44:47], v[108:111]\n"
      "v_mfma_f32_16x16x32_bf16 v[108:111], v[212:215], a[60:63], v[108:111]\n"
      "v_mfma_f32_16x16x32_bf16 v[124:127], v[216:219], a[44:47], v[124:127]\n"
      "v_mfma_f32_16x16x32_bf16 v[124:127], v[220:223], a[60:63], v[124:127]\n"
      "global_load_dwordx4 a[136:139], v3, s[46:47] offset:0 sc1\n"
      "s_waitcnt vmcnt(13) lgkmcnt(0)\n"
      "v_mfma_f32_16x16x32_bf16 v[64:67], v[192:195], a[64:67], v[64:67]\n"
      "v_mfma_f32_16x16x32_bf16 v[64:67], v[196:199], a[80:83], v[64:67]\n"
      "ds_read_b64_tr_b16 v[208:209], v58 offset:512\n"
      "v_mfma_f32_16x16x32_bf16 v[80:83], v[200:203], a[64:67], v[80:83]\n"
      "ds_read_b64_tr_b16 v[210:211], v58 offset:1536\n"
      "v_mfma_f32_16x16x32_bf16 v[80:83], v[204:207], a[80:83], v[80:83]\n"
      "global_load_dwordx4 a[152:155], v3, s[46:47] offset:1024 sc1\n"
      "v_mfma_f32_16x16x32_bf16 v[68:71], v[192:195], a[68:71], v[68:71]\n"
      "ds_read_b64_tr_b16 v[212:213], v58 offset:4608\n"
      "v_mfma_f32_16x16x32_bf16 v[68:71], v[196:199], a[84:87], v[68:71]\n"
      "ds_read_b64_tr_b16 v[214:215], v58 offset:5632\n"
      "v_mfma_f32_16x16x32_bf16 v[84:87], v[200:203], a[68:71], v[84:87]\n"
      "ds_read_b64_tr_b16 v[216:217], v59 offset:512\n"
      "v_mfma_f32_16x16x32_bf16 v[84:87], v[204:207], a[84:87], v[84:87]\n"
      "global_load_dwordx4 a[140:143], v4, s[46:47] offset:0 sc1\n"
      "v_mfma_f32_16x16x32_bf16 v[72:75], v[192:195], a[72:75], v[72:75]\n"
      "ds_read_b64_tr_b16 v[218:219], v59 offset:1536\n"
      "v_mfma_f32_16x16x32_bf16 v[72:75], v[196:199], a[88:91], v[72:75]\n"
      "ds_read_b64_tr_b16 v[220:221], v59 offset:4608\n"
      "v_mfma_f32_16x16x32_bf16 v[88:91], v[200:203], a[72:75], v[88:91]\n"
      "ds_read_b64_tr_b16 v[222:223], v59 offset:5632\n"
      "v_mfma_f32_16x16x32_bf16 v[88:91], v[204:207], a[88:91], v[88:91]\n"
      "global_load_dwordx4 a[156:159], v4, s[46:47] offset:1024 sc1\n"
      "v_mfma_f32_16x16x32_bf16 v[76:79], v[192:195], a[76:79], v[76:79]\n"
      "v_mfma_f32_16x16x32_bf16 v[76:79], v[196:199], a[92:95], v[76:79]\n"
      "v_mfma_f32_16x16x32_bf16 v[92:95], v[200:203], a[76:79], v[92:95]\n"
      "v_mfma_f32_16x16x32_bf16 v[92:95], v[204:207], a[92:95], v[92:95]\n"
      "global_load_dwordx4 a[160:163], v24, s[46:47] offset:2048 sc1\n"
      "s_waitcnt lgkmcnt(0)\n"
      "v_mfma_f32_16x16x32_bf16 v[96:99], v[208:211], a[64:67], v[96:99]\n"
      "v_mfma_f32_16x16x32_bf16 v[96:99], v[212:215], a[80:83], v[96:99]\n"
      "ds_read_b64_tr_b16 v[192:193], v58 offset:8192\n"
      "v_mfma_f32_16x16x32_bf16 v[112:115], v[216:219], a[64:67], v[112:115]\n"
      "ds_read_b64_tr_b16 v[194:195], v58 offset:9216\n"
      "v_mfma_f32_16x16x32_bf16 v[112:115], v[220:223], a[80:83], v[112:115]\n"
      "global_load_dwordx4 a[176:179], v24, s[46:47] offset:3072 sc1\n"
      "v_mfma_f32_16x16x32_bf16 v[100:103], v[208:211], a[68:71], v[100:103]\n"
      "ds_read_b64_tr_b16 v[196:197], v58 offset:12288\n"
      "v_mfma_f32_16x16x32_bf16 v[100:103], v[212:215], a[84:87], v[100:103]\n"
      "ds_read_b64_tr_b16 v[198:199], v58 offset:13312\n"
      "v_mfma_f32_16x16x32_bf16 v[116:119], v[216:219], a[68:71], v[116:119]\n"
      "ds_read_b64_tr_b16 v[200:201], v59 offset:8192\n"
      "v_mfma_f32_16x16x32_bf16 v[116:119], v[220:223], a[84:87], v[116:119]\n"
      "global_load_dwordx4 a[164:167], v2, s[46:47] offset:2048 sc1\n"
      "v_mfma_f32_16x16x32_bf16 v[104:107], v[208:211], a[72:75], v[104:107]\n"
      "ds_read_b64_tr_b16 v[202:203], v59 offset:9216\n"
      "v_mfma_f32_16x16x32_bf16 v[104:107], v[212:215], a[88:91], v[104:107]\n"
      "ds_read_b64_tr_b16 v[204:205], v59 offset:12288\n"
      "v_mfma_f32_16x16x32_bf16 v[120:123], v[216:219], a[72:75], v[120:123]\n"
      "ds_read_b64_tr_b16 v[206:207], v59 offset:13312\n"
      "v_mfma_f32_16x16x32_bf16 v[120:123], v[220:223], a[88:91], v[120:123]\n"
      "global_load_dwordx4 a[180:183], v2, s[46:47] offset:3072 sc1\n"
      "v_mfma_f32_16x16x32_bf16 v[108:111], v[208:211], a[76:79], v[108:111]\n"
      "v_mfma_f32_16x16x32_bf16 v[108:111], v[212:215], a[92:95], v[108:111]\n"
      "v_mfma_f32_16x16x32_bf16 v[124:127], v[216:219], a[76:79], v[124:127]\n"
      "v_mfma_f32_16x16x32_bf16 v[124:127], v[220:223], a[92:95], v[124:127]\n"
      "global_load_dwordx4 a[168:171], v3, s[46:47] offset:2048 sc1\n"
      "s_waitcnt vmcnt(13) lgkmcnt(0)\n"
      "v_mfma_f32_16x16x32_bf16 v[64:67], v[192:195], a[96:99], v[64:67]\n"
      "v_mfma_f32_16x16x32_bf16 v[64:67], v[196:199], a[112:115], v[64:67]\n"
      "ds_read_b64_tr_b16 v[208:209], v58 offset:8704\n"
      "v_mfma_f32_16x16x32_bf16 v[80:83], v[200:203], a[96:99], v[80:83]\n"
      "ds_read_b64_tr_b16 v[210:211], v58 offset:9728\n"
      "v_mfma_f32_16x16x32_bf16 v[80:83], v[204:207], a[112:115], v[80:83]\n"
      "global_load_dwordx4 a[184:187], v3, s[46:47] offset:3072 sc1\n"
      "v_mfma_f32_16x16x32_bf16 v[68:71], v[192:195], a[100:103], v[68:71]\n"
      "ds_read_b64_tr_b16 v[212:213], v58 offset:12800\n"
      "v_mfma_f32_16x16x32_bf16 v[68:71], v[196:199], a[116:119], v[68:71]\n"
      "ds_read_b64_tr_b16 v[214:215], v58 offset:13824\n"
      "v_mfma_f32_16x16x32_bf16 v[84:87], v[200:203], a[100:103], v[84:87]\n"
      "ds_read_b64_tr_b16 v[216:217], v59 offset:8704\n"
      "v_mfma_f32_16x16x32_bf16 v[84:87], v[204:207], a[116:119], v[84:87]\n"
      "global_load_dwordx4 a[172:175], v4, s[46:47] offset:2048 sc1\n"
      "v_mfma_f32_16x16x32_bf16 v[72:75], v[192:195], a[104:107], v[72:75]\n"
      "ds_read_b64_tr_b16 v[218:219], v59 offset:9728\n"
      "v_mfma_f32_16x16x32_bf16 v[72:75], v[196:199], a[120:123], v[72:75]\n"
      "ds_read_b64_tr_b16 v[220:221], v59 offset:12800\n"
      "v_mfma_f32_16x16x32_bf16 v[88:91], v[200:203], a[104:107], v[88:91]\n"
      "ds_read_b64_tr_b16 v[222:223], v59 offset:13824\n"
      "v_mfma_f32_16x16x32_bf16 v[88:91], v[204:207], a[120:123], v[88:91]\n"
      "global_load_dwordx4 a[188:191], v4, s[46:47] offset:3072 sc1\n"
      "v_mfma_f32_16x16x32_bf16 v[76:79], v[192:195], a[108:111], v[76:79]\n"
      "s_add_u32 s46, s44, s1\n"
      "s_addc_u32 s47, s45, 0\n"
      "v_mfma_f32_16x16x32_bf16 v[76:79], v[196:199], a[124:127], v[76:79]\n"
      "v_mfma_f32_16x16x32_bf16 v[92:95], v[200:203], a[108:111], v[92:95]\n"
      "v_mfma_f32_16x16x32_bf16 v[92:95], v[204:207], a[124:127], v[92:95]\n"
      "global_load_dwordx4 a[192:195], v24, s[46:47] offset:0 sc1\n"
      "s_waitcnt lgkmcnt(0)\n"
      "v_mfma_f32_16x16x32_bf16 v[96:99], v[208:211], a[96:99], v[96:99]\n"
      "s_barrier\n"
      "v_mfma_f32_16x16x32_bf16 v[96:99], v[212:215], a[112:115], v[96:99]\n"
      "ds_read_b64_tr_b16 v[192:193], v58 offset:32768\n"
      "v_mfma_f32_16x16x32_bf16 v[112:115], v[216:219], a[96:99], v[112:115]\n"
      "ds_read_b64_tr_b16 v[194:195], v58 offset:33792\n"
      "v_mfma_f32_16x16x32_bf16 v[112:115], v[220:223], a[112:115], v[112:115]\n"
      "global_load_dwordx4 a[208:211], v24, s[46:47] offset:1024 sc1\n"
      "v_mfma_f32_16x16x32_bf16 v[100:103], v[208:211], a[100:103], v[100:103]\n"
      "ds_read_b64_tr_b16 v[196:197], v58 offset:36864\n"
      "v_mfma_f32_16x16x32_bf16 v[100:103], v[212:215], a[116:119], v[100:103]\n"
      "ds_read_b64_tr_b16 v[198:199], v58 offset:37888\n"
      "v_mfma_f32_16x16x32_bf16 v[116:119], v[216:219], a[100:103], v[116:119]\n"
      "ds_read_b64_tr_b16 v[200:201], v59 offset:32768\n"
      "v_mfma_f32_16x16x32_bf16 v[116:119], v[220:223], a[116:119], v[116:119]\n"
      "global_load_dwordx4 a[224:227], v24, s[46:47] offset:2048 sc1\n"
      "v_mfma_f32_16x16x32_bf16 v[104:107], v[208:211], a[104:107], v[104:107]\n"
      "ds_read_b64_tr_b16 v[202:203], v59 offset:33792\n"
      "v_mfma_f32_16x16x32_bf16 v[104:107], v[212:215], a[120:123], v[104:107]\n"
      "ds_read_b64_tr_b16 v[204:205], v59 offset:36864\n"
      "v_mfma_f32_16x16x32_bf16 v[120:123], v[216:219], a[104:107], v[120:123]\n"
      "ds_read_b64_tr_b16 v[206:207], v59 offset:37888\n"
      "v_mfma_f32_16x16x32_bf16 v[120:123], v[220:223], a[120:123], v[120:123]\n"
      "global_load_dwordx4 a[240:243], v24, s[46:47] offset:3072 sc1\n"
      "v_mfma_f32_16x16x32_bf16 v[108:111], v[208:211], a[108:111], v[108:111]\n"
      "v_mfma_f32_16x16x32_bf16 v[108:111], v[212:215], a[124:127], v[108:111]\n"
      "v_mfma_f32_16x16x32_bf16 v[124:127], v[216:219], a[108:111], v[124:127]\n"
      "v_mfma_f32_16x16x32_bf16 v[124:127], v[220:223], a[124:127], v[124:127]\n"
      "global_load_dwordx4 a[196:199], v2, s[46:47] offset:0 sc1\n"
      "s_waitcnt vmcnt(13) lgkmcnt(0)\n"
      "v_mfma_f32_16x16x32_bf16 v[64:67], v[192:195], a[128:131], v[64:67]\n"
      "v_mfma_f32_16x16x32_bf16 v[64:67], v[196:199], a[144:147], v[64:67]\n"
      "ds_read_b64_tr_b16 v[208:209], v58 offset:33280\n"
      "v_mfma_f32_16x16x32_bf16 v[80:83], v[200:203], a[128:131], v[80:83]\n"
      "ds_read_b64_tr_b16 v[210:211], v58 offset:34304\n"
      "v_mfma_f32_16x16x32_bf16 v[80:83], v[204:207], a[144:147], v[80:83]\n"
      "global_load_dwordx4 a[212:215], v2, s[46:47] offset:1024 sc1\n"
      "v_mfma_f32_16x16x32_bf16 v[68:71], v[192:195], a[132:135], v[68:71]\n"
      "ds_read_b64_tr_b16 v[212:213], v58 offset:37376\n"
      "v_mfma_f32_16x16x32_bf16 v[68:71], v[196:199], a[148:151], v[68:71]\n"
      "ds_read_b64_tr_b16 v[214:215], v58 offset:38400\n"
      "v_mfma_f32_16x16x32_bf16 v[84:87], v[200:203], a[132:135], v[84:87]\n"
      "ds_read_b64_tr_b16 v[216:217], v59 offset:33280\n"
      "v_mfma_f32_16x16x32_bf16 v[84:87], v[204:207], a[148:151], v[84:87]\n"
      "global_load_dwordx4 a[228:231], v2, s[46:47] offset:2048 sc1\n"
      "v_mfma_f32_16x16x32_bf16 v[72:75], v[192:195], a[136:139], v[72:75]\n"
      "ds_read_b64_tr_b16 v[218:219], v59 offset:34304\n"
      "v_mfma_f32_16x16x32_bf16 v[72:75], v[196:199], a[152:155], v[72:75]\n"
      "ds_read_b64_tr_b16 v[220:221], v59 offset:37376\n"
      "v_mfma_f32_16x16x32_bf16 v[88:91], v[200:203], a[136:139], v[88:91]\n"
      "ds_read_b64_tr_b16 v[222:223], v59 offset:38400\n"
      "v_mfma_f32_16x16x32_bf16 v[88:91], v[204:207], a[152:155], v[88:91]\n"
      "global_load_dwordx4 a[244:247], v2, s[46:47] offset:3072 sc1\n"
      "v_mfma_f32_16x16x32_bf16 v[76:79], v[192:195], a[140:143], v[76:79]\n"
      "v_mfma_f32_16x16x32_bf16 v[76:79], v[196:199], a[156:159], v[76:79]\n"
      "v_mfma_f32_16x16x32_bf16 v[92:95], v[200:203], a[140:143], v[92:95]\n"
      "v_mfma_f32_16x16x32_bf16 v[92:95], v[204:207], a[156:159], v[92:95]\n"
      "global_load_dwordx4 a[200:203], v3, s[46:47] offset:0 sc1\n"
      "s_waitcnt lgkmcnt(0)\n"
      "v_mfma_f32_16x16x32_bf16 v[96:99], v[208:211], a[128:131], v[96:99]\n"
      "v_mfma_f32_16x16x32_bf16 v[96:99], v[212:215], a[144:147], v[96:99]\n"
      "ds_read_b64_tr_b16 v[192:193], v58 offset:40960\n"
      "v_mfma_f32_16x16x32_bf16 v[112:115], v[216:219], a[128:131], v[112:115]\n"
      "ds_read_b64_tr_b16 v[194:195], v58 offset:41984\n"
      "v_mfma_f32_16x16x32_bf16 v[112:115], v[220:223], a[144:147], v[112:115]\n"
      "global_load_dwordx4 a[216:219], v3, s[46:47] offset:1024 sc1\n"
      "v_mfma_f32_16x16x32_bf16 v[100:103], v[208:211], a[132:135], v[100:103]\n"
      "ds_read_b64_tr_b16 v[196:197], v58 offset:45056\n"
      "v_mfma_f32_16x16x32_bf16 v[100:103], v[212:215], a[148:151], v[100:103]\n"
      "ds_read_b64_tr_b16 v[198:199], v58 offset:46080\n"
      "v_mfma_f32_16x16x32_bf16 v[116:119], v[216:219], a[132:135], v[116:119]\n"
      "ds_read_b64_tr_b16 v[200:201], v59 offset:40960\n"
      "v_mfma_f32_16x16x32_bf16 v[116:119], v[220:223], a[148:151], v[116:119]\n"
      "global_load_dwordx4 a[232:235], v3, s[46:47] offset:2048 sc1\n"
      "v_mfma_f32_16x16x32_bf16 v[104:107], v[208:211], a[136:139], v[104:107]\n"
      "ds_read_b64_tr_b16 v[202:203], v59 offset:41984\n"
      "v_mfma_f32_16x16x32_bf16 v[104:107], v[212:215], a[152:155], v[104:107]\n"
      "ds_read_b64_tr_b16 v[204:205], v59 offset:45056\n"
      "v_mfma_f32_16x16x32_bf16 v[120:123], v[216:219], a[136:139], v[120:123]\n"
      "ds_read_b64_tr_b16 v[206:207], v59 offset:46080\n"
      "v_mfma_f32_16x16x32_bf16 v[120:123], v[220:223], a[152:155], v[120:123]\n"
      "global_load_dwordx4 a[248:251], v3, s[46:47] offset:3072 sc1\n"
      "v_mfma_f32_16x16x32_bf16 v[108:111], v[208:211], a[140:143], v[108:111]\n"
      "v_mfma_f32_16x16x32_bf16 v[108:111], v[212:215], a[156:159], v[108:111]\n"
      "v_mfma_f32_16x16x32_bf16 v[124:127], v[216:219], a[140:143], v[124:127]\n"
      "v_mfma_f32_16x16x32_bf16 v[124:127], v[220:223], a[156:159], v[124:127]\n"
      "global_load_dwordx4 a[204:207], v4, s[46:47] offset:0 sc1\n"
      "global_load_dwordx4 a[220:223], v4, s[46:47] offset:1024 sc1\n"
      "global_load_dwordx4 a[236:239], v4, s[46:47] offset:2048 sc1\n"
      "global_load_dwordx4 a[252:255], v4, s[46:47] offset:3072 sc1\n"
      "s_waitcnt vmcnt(16) lgkmcnt(0)\n"
      "v_mfma_f32_16x16x32_bf16 v[64:67], v[192:195], a[160:163], v[64:67]\n"
      "v_mfma_f32_16x16x32_bf16 v[64:67], v[196:199], a[176:179], v[64:67]\n"
      "ds_read_b64_tr_b16 v[208:209], v58 offset:41472\n"
      "v_mfma_f32_16x16x32_bf16 v[80:83], v[200:203], a[160:163], v[80:83]\n"
      "ds_read_b64_tr_b16 v[210:211], v58 offset:42496\n"
      "v_mfma_f32_16x16x32_bf16 v[80:83], v[204:207], a[176:179], v[80:83]\n"
      "v_mfma_f32_16x16x32_bf16 v[68:71], v[192:195], a[164:167], v[68:71]\n"
      "ds_read_b64_tr_b16 v[212:213], v58 offset:45568\n"
      "v_mfma_f32_16x16x32_bf16 v[68:71], v[196:199], a[180:183], v[68:71]\n"
      "ds_read_b64_tr_b16 v[214:215], v58 offset:46592\n"
      "v_mfma_f32_16x16x32_bf16 v[84:87], v[200:203], a[164:167], v[84:87]\n"
      "ds_read_b64_tr_b16 v[216:217], v59 offset:41472\n"
      "v_mfma_f32_16x16x32_bf16 v[84:87], v[204:207], a[180:183], v[84:87]\n"
      "v_mfma_f32_16x16x32_bf16 v[72:75], v[192:195], a[168:171], v[72:75]\n"
      "ds_read_b64_tr_b16 v[218:219], v59 offset:42496\n"
      ".Lgm_w9:\n"
      "s_cbranch_execz .Lgm_r2\n"
      "v_mfma_f32_16x16x32_bf16 v[72:75], v[196:199], a[184:187], v[72:75]\n"
      "ds_read_b64_tr_b16 v[220:221], v59 offset:45568\n"
      "v_mfma_f32_16x16x32_bf16 v[88:91], v[200:203], a[168:171], v[88:91]\n"
      "ds_read_b64_tr_b16 v[222:223], v59 offset:46592\n"
      "v_mfma_f32_16x16x32_bf16 v[88:91], v[204:207], a[184:187], v[88:91]\n"
      "v_mfma_f32_16x16x32_bf16 v[76:79], v[192:195], a[172:175], v[76:79]\n"
      "v_mfma_f32_16x16x32_bf16 v[76:79], v[196:199], a[188:191], v[76:79]\n"
      "v_mfma_f32_16x16x32_bf16 v[92:95], v[200:203], a[172:175], v[92:95]\n"
      "v_mfma_f32_16x16x32_bf16 v[92:95], v[204:207], a[188:191], v[92:95]\n"
      "s_waitcnt lgkmcnt(0)\n"
      "v_mfma_f32_16x16x32_bf16 v[96:99], v[208:211], a[160:163], v[96:99]\n"
      "v_mfma_f32_16x16x32_bf16 v[96:99], v[212:215], a[176:179], v[96:99]\n"
      "ds_read_b64_tr_b16 v[192:193], v60 offset:512\n"
      "v_mfma_f32_16x16x32_bf16 v[112:115], v[216:219], a[160:163], v[112:115]\n"
      "ds_read_b64_tr_b16 v[194:195], v60 offset:1536\n"
      "v_mfma_f32_16x16x32_bf16 v[112:115], v[220:223], a[176:179], v[112:115]\n"
      "v_mfma_f32_16x16x32_bf16 v[100:103], v[208:211], a[164:167], v[100:103]\n"
      "ds_read_b64_tr_b16 v[196:197], v60 offset:4608\n"
      "v_mfma_f32_16x16x32_bf16 v[100:103], v[212:215], a[180:183], v[100:103]\n"
      "ds_read_b64_tr_b16 v[198:199], v60 offset:5632\n"
      "v_mfma_f32_16x16x32_bf16 v[116:119], v[216:219], a[164:167], v[116:119]\n"
      "ds_read_b64_tr_b16 v[200:201], v60 offset:8704\n"
      "v_mfma_f32_16x16x32_bf16 v[116:119], v[220:223], a[180:183], v[116:119]\n"
      "v_mfma_f32_16x16x32_bf16 v[104:107], v[208:211], a[168:171], v[104:107]\n"
      "ds_read_b64_tr_b16 v[202:203], v60 offset:9728\n"
      "v_mfma_f32_16x16x32_bf16 v[104:107], v[212:215], a[184:187], v[104:107]\n"
      "ds_read_b64_tr_b16 v[204:205], v60 offset:12800\n"
      "v_mfma_f32_16x16x32_bf16 v[120:123], v[216:219], a[168:171], v[120:123]\n"
      "ds_read_b64_tr_b16 v[206:207], v60 offset:13824\n"
      "v_mfma_f32_16x16x32_bf16 v[120:123], v[220:223], a[184:187], v[120:123]\n"
      "v_mfma_f32_16x16x32_bf16 v[108:111], v[208:211], a[172:175], v[108:111]\n"
      "v_mfma_f32_16x16x32_bf16 v[108:111], v[212:215], a[188:191], v[108:111]\n"
      "v_mfma_f32_16x16x32_bf16 v[124:127], v[216:219], a[172:175], v[124:127]\n"
      "v_mfma_f32_16x16x32_bf16 v[124:127], v[220:223], a[188:191], v[124:127]\n"
      "s_waitcnt vmcnt(0) lgkmcnt(0)\n"
      "v_mfma_f32_16x16x32_bf16 v[96:99], v[192:195], a[192:195], v[96:99]\n"
      "v_mfma_f32_16x16x32_bf16 v[96:99], v[196:199], a[208:211], v[96:99]\n"
      "ds_read_b64_tr_b16 v[208:209], v61 offset:512\n"
      "v_mfma_f32_16x16x32_bf16 v[96:99], v[200:203], a[224:227], v[96:99]\n"
      "ds_read_b64_tr_b16 v[210:211], v61 offset:1536\n"
      "v_mfma_f32_16x16x32_bf16 v[96:99], v[204:207], a[240:243], v[96:99]\n"
      "v_mfma_f32_16x16x32_bf16 v[100:103], v[192:195], a[196:199], v[100:103]\n"
      "ds_read_b64_tr_b16 v[212:213], v61 offset:4608\n"
      "v_mfma_f32_16x16x32_bf16 v[100:103], v[196:199], a[212:215], v[100:103]\n"
      "ds_read_b64_tr_b16 v[214:215], v61 offset:5632\n"
      "v_mfma_f32_16x16x32_bf16 v[100:103], v[200:203], a[228:231], v[100:103]\n"
      "ds_read_b64_tr_b16 v[216:217], v61 offset:8704\n"
      "v_mfma_f32_16x16x32_bf16 v[100:103], v[204:207], a[244:247], v[100:103]\n"
      "v_mfma_f32_16x16x32_bf16 v[104:107], v[192:195], a[200:203], v[104:107]\n"
      "ds_read_b64_tr_b16 v[218:219], v61 offset:9728\n"
      "v_mul_f32 v96, v96, v224\n"
      "v_mul_f32 v97, v97, v224\n"
      "v_mfma_f32_16x16x32_bf16 v[104:107], v[196:199], a[216:219], v[104:107]\n"
      "ds_read_b64_tr_b16 v[220:221], v61 offset:12800\n"
      "v_mul_f32 v98, v98, v224\n"
      "v_mul_f32 v99, v99, v224\n"
      "v_mfma_f32_16x16x32_bf16 v[104:107], v[200:203], a[232:235], v[104:107]\n"
      "ds_read_b64_tr_b16 v[222:223], v61 offset:13824\n"
      "v_mul_f32 v100, v100, v225\n"
      "v_mul_f32 v101, v101, v225\n"
      "v_mfma_f32_16x16x32_bf16 v[104:107], v[204:207], a[248:251], v[104:107]\n"
      "ds_write_b128 v232, v[96:99] offset:0\n"
      "v_mfma_f32_16x16x32_bf16 v[108:111], v[192:195], a[204:207], v[108:111]\n"
      "v_mul_f32 v102, v102, v225\n"
      "v_mul_f32 v103, v103, v225\n"
      "v_mfma_f32_16x16x32_bf16 v[108:111], v[196:199], a[220:223], v[108:111]\n"
      "v_mfma_f32_16x16x32_bf16 v[108:111], v[200:203], a[236:239], v[108:111]\n"
      "v_mfma_f32_16x16x32_bf16 v[108:111], v[204:207], a[252:255], v[108:111]\n"
      "ds_write_b128 v232, v[100:103] offset:1024\n"
      "s_waitcnt lgkmcnt(2)\n"
      "v_mfma_f32_16x16x32_bf16 v[112:115], v[208:211], a[192:195], v[112:115]\n"
      "v_mul_f32 v104, v104, v226\n"
      "v_mul_f32 v105, v105, v226\n"
      "v_mul_f32 v106, v106, v226\n"
      "v_mfma_f32_16x16x32_bf16 v[112:115], v[212:215], a[208:211], v[112:115]\n"
      "ds_read_b64_tr_b16 v[192:193], v60 offset:0\n"
      "v_mul_f32 v107, v107, v226\n"
      "v_mfma_f32_16x16x32_bf16 v[112:115], v[216:219], a[224:227], v[112:115]\n"
      "ds_read_b64_tr_b16 v[194:195], v60 offset:1024\n"
      "v_mul_f32 v108, v108, v227\n"
      "v_mul_f32 v109, v109, v227\n"
      "v_mfma_f32_16x16x32_bf16 v[112:115], v[220:223], a[240:243], v[112:115]\n"
      "ds_write_b128 v232, v[104:107] offset:2048\n"
      "v_mfma_f32_16x16x32_bf16 v[116:119], v[208:211], a[196:199], v[116:119]\n"
      "ds_read_b64_tr_b16 v[196:197], v60 offset:4096\n"
      "v_mul_f32 v110, v110, v227\n"
      "v_mul_f32 v111, v111, v227\n"
      "v_mfma_f32_16x16x32_bf16 v[116:119], v[212:215], a[212:215], v[116:119]\n"
      "ds_read_b64_tr_b16 v[198:199], v60 offset:5120\n"
      "v_mfma_f32_16x16x32_bf16 v[116:119], v[216:219], a[228:231], v[116:119]\n"
      "ds_read_b64_tr_b16 v[200:201], v60 offset:8192\n"
      "v_mul_f32 v112, v112, v224\n"
      "v_mul_f32 v113, v113, v224\n"
      "v_mfma_f32_16x16x32_bf16 v[116:119], v[220:223], a[244:247], v[116:119]\n"
      "ds_write_b128 v232, v[108:111] offset:3072\n"
      "v_mfma_f32_16x16x32_bf16 v[120:123], v[208:211], a[200:203], v[120:123]\n"
      "ds_read_b64_tr_b16 v[202:203], v60 offset:9216\n"
      "v_mul_f32 v114, v114, v224\n"
      "v_mul_f32 v115, v115, v224\n"
      "v_mfma_f32_16x16x32_bf16 v[120:123], v[212:215], a[216:219], v[120:123]\n"
      "ds_read_b64_tr_b16 v[204:205], v60 offset:12288\n"
      "v_mfma_f32_16x16x32_bf16 v[120:123], v[216:219], a[232:235], v[120:123]\n"
      "ds_read_b64_tr_b16 v[206:207], v60 offset:13312\n"
      "v_mul_f32 v116, v116, v225\n"
      "v_mul_f32 v117, v117, v225\n"
      "v_mfma_f32_16x16x32_bf16 v[120:123], v[220:223], a[248:251], v[120:123]\n"
      "ds_write_b128 v232, v[112:115] offset:4096\n"
      "v_mfma_f32_16x16x32_bf16 v[124:127], v[208:211], a[204:207], v[124:127]\n"
      "v_mul_f32 v118, v118, v225\n"
      "v_mul_f32 v119, v119, v225\n"
      "v_mfma_f32_16x16x32_bf16 v[124:127], v[212:215], a[220:223], v[124:127]\n"
      "v_mfma_f32_16x16x32_bf16 v[124:127], v[216:219], a[236:239], v[124:127]\n"
      "v_mfma_f32_16x16x32_bf16 v[124:127], v[220:223], a[252:255], v[124:127]\n"
      "ds_write_b128 v232, v[116:119] offset:5120\n"
      "s_waitcnt lgkmcnt(2)\n"
      "v_mfma_f32_16x16x32_bf16 v[64:67], v[192:195], a[192:195], v[64:67]\n"
      "v_mul_f32 v120, v120, v226\n"
      "v_mul_f32 v121, v121, v226\n"
      "v_mul_f32 v122, v122, v226\n"
      "v_mfma_f32_16x16x32_bf16 v[64:67], v[196:199], a[208:211], v[64:67]\n"
      "ds_read_b64_tr_b16 v[208:209], v61 offset:0\n"
      "v_mul_f32 v123, v123, v226\n"
      "v_mfma_f32_16x16x32_bf16 v[64:67], v[200:203], a[224:227], v[64:67]\n"
      "ds_read_b64_tr_b16 v[210:211], v61 offset:1024\n"
      "v_mul_f32 v124, v124, v227\n"
      "v_mul_f32 v125, v125, v227\n"
      "v_mfma_f32_16x16x32_bf16 v[64:67], v[204:207], a[240:243], v[64:67]\n"
      "ds_write_b128 v232, v[120:123] offset:6144\n"
      "v_mfma_f32_16x16x32_bf16 v[68:71], v[192:195], a[196:199], v[68:71]\n"
      "ds_read_b64_tr_b16 v[212:213], v61 offset:4096\n"
      "v_mul_f32 v126, v126, v227\n"
      "v_mul_f32 v127, v127, v227\n"
      "v_mfma_f32_16x16x32_bf16 v[68:71], v[196:199], a[212:215], v[68:71]\n"
      "ds_read_b64_tr_b16 v[214:215], v61 offset:5120\n"
      "v_mfma_f32_16x16x32_bf16 v[68:71], v[200:203], a[228:231], v[68:71]\n"
      "ds_read_b64_tr_b16 v[216:217], v61 offset:8192\n"
      "v_mfma_f32_16x16x32_bf16 v[68:71], v[204:207], a[244:247], v[68:71]\n"
      "ds_write_b128 v232, v[124:127] offset:7168\n"
      "v_mfma_f32_16x16x32_bf16 v[72:75], v[192:195], a[200:203], v[72:75]\n"
      "ds_read_b64_tr_b16 v[218:219], v61 offset:9216\n"
      "v_mfma_f32_16x16x32_bf16 v[72:75], v[196:199], a[216:219], v[72:75]\n"
      "ds_read_b64_tr_b16 v[220:221], v61 offset:12288\n"
      "v_mfma_f32_16x16x32_bf16 v[72:75], v[200:203], a[232:235], v[72:75]\n"
      "ds_read_b64_tr_b16 v[222:223], v61 offset:13312\n"
      "v_mfma_f32_16x16x32_bf16 v[72:75], v[204:207], a[248:251], v[72:75]\n"
      "v_mfma_f32_16x16x32_bf16 v[76:79], v[192:195], a[204:207], v[76:79]\n"
      "v_mfma_f32_16x16x32_bf16 v[76:79], v[196:199], a[220:223], v[76:79]\n"
      "v_mfma_f32_16x16x32_bf16 v[76:79], v[200:203], a[236:239], v[76:79]\n"
      "v_mfma_f32_16x16x32_bf16 v[76:79], v[204:207], a[252:255], v[76:79]\n"
      "s_waitcnt lgkmcnt(0)\n"
      "v_mfma_f32_16x16x32_bf16 v[80:83], v[208:211], a[192:195], v[80:83]\n"
      "s_barrier\n"
      "ds_read_b128 v[96:99], v233 offset:0\n"
      "ds_read_b128 v[100:103], v233 offset:1024\n"
      "v_mfma_f32_16x16x32_bf16 v[80:83], v[212:215], a[208:211], v[80:83]\n"
      "ds_read_b128 v[104:107], v233 offset:2048\n"
      "ds_read_b128 v[108:111], v233 offset:3072\n"
      "v_mfma_f32_16x16x32_bf16 v[80:83], v[216:219], a[224:227], v[80:83]\n"
      "ds_read_b128 v[112:115], v233 offset:4096\n"
      "ds_read_b128 v[116:119], v233 offset:5120\n"
      "v_mfma_f32_16x16x32_bf16 v[80:83], v[220:223], a[240:243], v[80:83]\n"
      "ds_read_b128 v[120:123], v233 offset:6144\n"
      "ds_read_b128 v[124:127], v233 offset:7168\n"
      "v_mfma_f32_16x16x32_bf16 v[84:87], v[208:211], a[196:199], v[84:87]\n"
      "v_mfma_f32_16x16x32_bf16 v[84:87], v[212:215], a[212:215], v[84:87]\n"
      "v_mfma_f32_16x16x32_bf16 v[84:87], v[216:219], a[228:231], v[84:87]\n"
      "v_mfma_f32_16x16x32_bf16 v[84:87], v[220:223], a[244:247], v[84:87]\n"
      "s_waitcnt lgkmcnt(4)\n"
      "v_fmac_f32 v96, v64, v224\n"
      "v_fmac_f32 v97, v65, v224\n"
      "v_fmac_f32 v98, v66, v224\n"
      "v_fmac_f32 v99, v67, v224\n"
      "v_cvt_pk_bf16_f32 v240, v96, v97\n"
      "v_cvt_pk_bf16_f32 v241, v98, v99\n"
      "v_mfma_f32_16x16x32_bf16 v[88:91], v[208:211], a[200:203], v[88:91]\n"
      "v_fmac_f32 v100, v68, v225\n"
      "v_fmac_f32 v101, v69, v225\n"
      "v_fmac_f32 v102, v70, v225\n"
      "v_fmac_f32 v103, v71, v225\n"
      "v_cvt_pk_bf16_f32 v244, v100, v101\n"
      "v_cvt_pk_bf16_f32 v245, v102, v103\n"
      "v_mfma_f32_16x16x32_bf16 v[88:91], v[212:215], a[216:219], v[88:91]\n"
      "v_fmac_f32 v104, v72, v226\n"
      "v_fmac_f32 v105, v73, v226\n"
      "v_fmac_f32 v106, v74, v226\n"
      "v_fmac_f32 v107, v75, v226\n"
      "v_cvt_pk_bf16_f32 v248, v104, v105\n"
      "v_cvt_pk_bf16_f32 v249, v106, v107\n"
      "v_mfma_f32_16x16x32_bf16 v[88:91], v[216:219], a[232:235], v[88:91]\n"
      "v_fmac_f32 v108, v76, v227\n"
      "v_fmac_f32 v109, v77, v227\n"
      "v_fmac_f32 v110, v78, v227\n"
      "v_fmac_f32 v111, v79, v227\n"
      "v_cvt_pk_bf16_f32 v252, v108, v109\n"
      "v_cvt_pk_bf16_f32 v253, v110, v111\n"
      "v_mfma_f32_16x16x32_bf16 v[88:91], v[220:223], a[248:251], v[88:91]\n"
      "s_waitcnt lgkmcnt(0)\n"
      "v_fmac_f32 v112, v80, v224\n"
      "v_fmac_f32 v113, v81, v224\n"
      "v_fmac_f32 v114, v82, v224\n"
      "v_fmac_f32 v115, v83, v224\n"
      "v_cvt_pk_bf16_f32 v242, v112, v113\n"
      "v_cvt_pk_bf16_f32 v243, v114, v115\n"
      "v_mfma_f32_16x16x32_bf16 v[92:95], v[208:211], a[204:207], v[92:95]\n"
      "v_fmac_f32 v116, v84, v225\n"
      "v_fmac_f32 v117, v85, v225\n"
      "v_fmac_f32 v118, v86, v225\n"
      "v_fmac_f32 v119, v87, v225\n"
      "v_cvt_pk_bf16_f32 v246, v116, v117\n"
      "v_cvt_pk_bf16_f32 v247, v118, v119\n"
      "v_mfma_f32_16x16x32_bf16 v[92:95], v[212:215], a[220:223], v[92:95]\n"
      "v_mfma_f32_16x16x32_bf16 v[92:95], v[216:219], a[236:239], v[92:95]\n"
      "v_fmac_f32 v120, v88, v226\n"
      "v_fmac_f32 v121, v89, v226\n"
      "v_fmac_f32 v122, v90, v226\n"
      "v_fmac_f32 v123, v91, v226\n"
      "v_cvt_pk_bf16_f32 v250, v120, v121\n"
      "v_cvt_pk_bf16_f32 v251, v122, v123\n"
      "v_mfma_f32_16x16x32_bf16 v[92:95], v[220:223], a[252:255], v[92:95]\n"
      "ds_write2_b64 v234, v[240:241], v[242:243] offset0:0 offset1:4\n"
      "ds_write2_b64 v234, v[244:245], v[246:247] offset0:160 offset1:164\n"
      "s_nop 1\n"
      "v_add_u32 v234, 0xa00, v234\n"
      "ds_write2_b64 v234, v[248:249], v[250:251] offset0:0 offset1:4\n"
      "s_nop 1\n"
      "v_fmac_f32 v124, v92, v227\n"
      "v_fmac_f32 v125, v93, v227\n"
      "v_fmac_f32 v126, v94, v227\n"
      "v_fmac_f32 v127, v95, v227\n"
      "v_cvt_pk_bf16_f32 v254, v124, v125\n"
      "v_cvt_pk_bf16_f32 v255, v126, v127\n"
      "ds_write2_b64 v234, v[252:253], v[254:255] offset0:160 offset1:164\n"
      "ds_read_b128 v[64:67], v235 offset:0\n"
      "ds_read_b128 v[68:71], v235 offset:1280\n"
      "ds_read_b128 v[72:75], v235 offset:2560\n"
      "ds_read_b128 v[76:79], v235 offset:3840\n"
      "s_waitcnt lgkmcnt(3)\n"
      "global_store_dwordx4 v236, v[64:67], s[66:67] offset:0 sc1\n"
      "s_waitcnt lgkmcnt(2)\n"
      "global_store_dwordx4 v236, v[68:71], s[80:81] offset:0 sc1\n"
      "s_waitcnt lgkmcnt(1)\n"
      "global_store_dwordx4 v236, v[72:75], s[82:83] offset:0 sc1\n"
      "s_waitcnt lgkmcnt(0)\n"
      "global_store_dwordx4 v236, v[76:79], s[84:85] offset:0 sc1\n"
      "s_or_b32 s90, s33, s4\n"
      "s_cbranch_scc1 .Lgm_end\n"
      "s_mov_b64 exec, 1\n"
      "global_store_dword v27, v26, s[30:31] offset:0 sc1\n"
      "s_mov_b64 exec, -1\n"
      ".Lgm_end:\n"
      "s_endpgm\n"
      ".Lgm_slow0:\n"
      "s_mov_b32 s95, 200000\n"
      ".Lgm_loop0:\n"
      "global_load_dwordx4 v[208:211], v37, s[54:55] offset:0 sc1\n"
      "global_load_dwordx4 v[192:195], v36, s[78:79] offset:0 sc1\n"
      "global_load_dwordx4 v[196:199], v36, s[80:81] offset:0 sc1\n"
      "global_load_dwordx4 v[200:203], v36, s[82:83] offset:0 sc1\n"
      "global_load_dwordx4 v[204:207], v36, s[84:85] offset:0 sc1\n"
      "s_waitcnt vmcnt(0)\n"
      "v_xor_b32 v192, s88, v192\n"
      "v_xor_b32 v194, s88, v194\n"
      "v_xor_b32 v196, s88, v196\n"
      "v_xor_b32 v198, s88, v198\n"
      "v_xor_b32 v200, s88, v200\n"
      "v_xor_b32 v202, s88, v202\n"
      "v_xor_b32 v204, s88, v204\n"
      "v_xor_b32 v206, s88, v206\n"
      "v_xor_b32 v208, s88, v208\n"
      "v_xor_b32 v209, s88, v209\n"
      "v_xor_b32 v210, s88, v210\n"
      "v_xor_b32 v211, s88, v211\n"
      "v_or_b32 v216, v192, v194\n"
      "v_or3_b32 v216, v216, v196, v198\n"
      "v_or3_b32 v216, v216, v200, v202\n"
      "v_or3_b32 v216, v216, v204, v206\n"
      "v_or3_b32 v216, v216, v208, v209\n"
      "v_or3_b32 v216, v216, v210, v211\n"
      "v_and_b32 v216, s86, v216\n"
      "v_cmp_ne_u32 vcc, 0, v216\n"
      "s_sub_u32 s95, s95, 1\n"
      "s_nop 1\n"
      "s_cbranch_vccz .Lgm_back0\n"
      "s_cbranch_scc0 .Lgm_loop0\n"
      "s_branch .Lgm_back0\n"
      ".Lgm_slow1:\n"
      "s_mov_b32 s95, 200000\n"
      ".Lgm_loop1:\n"
      "global_load_dwordx4 v[192:195], v24, s[56:57] offset:0 sc1\n"
      "global_load_dwordx4 v[196:199], v24, s[56:57] offset:1024 sc1\n"
      "global_load_dwordx4 v[200:203], v24, s[56:57] offset:2048 sc1\n"
      "global_load_dwordx4 v[204:207], v24, s[56:57] offset:3072 sc1\n"
      "global_load_dwordx4 v[208:211], v28, s[58:59] offset:0 sc1\n"
      "global_load_dwordx4 v[212:215], v28, s[58:59] offset:64 sc1\n"
      "s_waitcnt vmcnt(0)\n"
      "v_xor_b32 v216, v192, v26\n"
      "v_bitop3_b32 v216, v216, v193, v26 bitop3:0xf6\n"
      "v_bitop3_b32 v216, v216, v194, v26 bitop3:0xf6\n"
      "v_bitop3_b32 v216, v216, v195, v26 bitop3:0xf6\n"
      "v_bitop3_b32 v216, v216, v196, v26 bitop3:0xf6\n"
      "v_bitop3_b32 v216, v216, v197, v26 bitop3:0xf6\n"
      "v_bitop3_b32 v216, v216, v198, v26 bitop3:0xf6\n"
      "v_bitop3_b32 v216, v216, v199, v26 bitop3:0xf6\n"
      "v_bitop3_b32 v216, v216, v200, v26 bitop3:0xf6\n"
      "v_bitop3_b32 v216, v216, v201, v26 bitop3:0xf6\n"
      "v_bitop3_b32 v216, v216, v202, v26 bitop3:0xf6\n"
      "v_bitop3_b32 v216, v216, v203, v26 bitop3:0xf6\n"
      "v_bitop3_b32 v216, v216, v204, v26 bitop3:0xf6\n"
      "v_bitop3_b32 v216, v216, v205, v26 bitop3:0xf6\n"
      "v_bitop3_b32 v216, v216, v206, v26 bitop3:0xf6\n"
      "v_bitop3_b32 v216, v216, v207, v26 bitop3:0xf6\n"
      "v_bitop3_b32 v216, v216, v208, v26 bitop3:0xf6\n"
      "v_bitop3_b32 v216, v216, v209, v26 bitop3:0xf6\n"
      "v_bitop3_b32 v216, v216, v210, v26 bitop3:0xf6\n"
      "v_bitop3_b32 v216, v216, v211, v26 bitop3:0xf6\n"
      "v_bitop3_b32 v216, v216, v212, v26 bitop3:0xf6\n"
      "v_bitop3_b32 v216, v216, v213, v26 bitop3:0xf6\n"
      "v_bitop3_b32 v216, v216, v214, v26 bitop3:0xf6\n"
      "v_bitop3_b32 v216, v216, v215, v26 bitop3:0xf6\n"
      "v_cmp_ne_u16 vcc, 0, v216\n"
      "s_sub_u32 s95, s95, 1\n"
      "s_nop 1\n"
      "s_cbranch_vccz .Lgm_back1\n"
      "s_cbranch_scc0 .Lgm_loop1\n"
      "s_branch .Lgm_back1\n"
      ".Lgm_slow2:\n"
      "s_mov_b32 s95, 200000\n"
      ".Lgm_loop2:\n"
      "global_load_dword v238, v40, s[62:63] offset:0 sc1\n"
      "s_waitcnt vmcnt(0)\n"
      "v_cmp_ne_u32 vcc, s13, v238\n"
      "s_sub_u32 s95, s95, 1\n"
      "s_nop 1\n"
      "s_cbranch_vccz .Lgm_back2\n"
      "s_cbranch_scc0 .Lgm_loop2\n"
      "s_branch .Lgm_back2\n"
      ".Lgm_slow3:\n"
      "s_mov_b32 s95, 200000\n"
      ".Lgm_loop3:\n"
      "global_load_dword v238, v236, s[62:63] offset:0 sc1\n"
      "s_waitcnt vmcnt(0)\n"
      "v_cmp_ne_u32 vcc, s13, v238\n"
      "s_sub_u32 s95, s95, 1\n"
      "s_nop 1\n"
      "s_cbranch_vccz .Lgm_back3\n"
      "s_cbranch_scc0 .Lgm_loop3\n"
      "s_branch .Lgm_back3\n"
      ".Lgm_last:\n"
      :
      : "s"(__builtin_amdgcn_kernarg_segment_ptr()), "s"(blockIdx.x), "v"(threadIdx.x + (unsigned)(size_t)lds)
      : "v1","v2","v3","v4","v5","v6","v7","v8","v9","v10","v11","v12","v13","v14","v15","v16","v17","v18","v19","v20","v21","v22","v23","v24","v25","v26","v27","v28","v29","v30","v31","v32","v33","v34","v35","v36","v37","v38","v39","v40","v41","v42","v43","v44","v45","v46","v47","v48","v49","v50","v51","v52","v53","v54","v55","v56","v57","v58","v59","v60","v61","v62","v63","v64","v65","v66","v67","v68","v69","v70","v71","v72","v73","v74","v75","v76","v77","v78","v79","v80","v81","v82","v83","v84","v85","v86","v87","v88","v89","v90","v91","v92","v93","v94","v95","v96","v97","v98","v99","v100","v101","v102","v103","v104","v105","v106","v107","v108","v109","v110","v111","v112","v113","v114","v115","v116","v117","v118","v119","v120","v121","v122","v123","v124","v125","v126","v127","v128","v129","v130","v131","v132","v133","v134","v135","v136","v137","v138","v139","v140","v141","v142","v143","v144","v145","v146","v147","v148","v149","v150","v151","v152","v153","v154","v155","v156","v157","v158","v159","v160","v161","v162","v163","v164","v165","v166","v167","v168","v169","v170","v171","v172","v173","v174","v175","v176","v177","v178","v179","v180","v181","v182","v183","v184","v185","v186","v187","v188","v189","v190","v191","v192","v193","v194","v195","v196","v197","v198","v199","v200","v201","v202","v203","v204","v205","v206","v207","v208","v209","v210","v211","v212","v213","v214","v215","v216","v217","v218","v219","v220","v221","v222","v223","v224","v225","v226","v227","v228","v229","v230","v231","v232","v233","v234","v235","v236","v237","v238","v239","v240","v241","v242","v243","v244","v245","v246","v247","v248","v249","v250","v251","v252","v253","v254","v255","a0","a1","a2","a3","a4","a5","a6","a7","a8","a9","a10","a11","a12","a13","a14","a15","a16","a17","a18","a19","a20","a21","a22","a23","a24","a25","a26","a27","a28","a29","a30","a31","a32","a33","a34","a35","a36","a37","a38","a39","a40","a41","a42","a43","a44","a45","a46","a47","a48","a49","a50","a51","a52","a53","a54","a55","a56","a57","a58","a59","a60","a61","a62","a63","a64","a65","a66","a67","a68","a69","a70","a71","a72","a73","a74","a75","a76","a77","a78","a79","a80","a81","a82","a83","a84","a85","a86","a87","a88","a89","a90","a91","a92","a93","a94","a95","a96","a97","a98","a99","a100","a101","a102","a103","a104","a105","a106","a107","a108","a109","a110","a111","a112","a113","a114","a115","a116","a117","a118","a119","a120","a121","a122","a123","a124","a125","a126","a127","a128","a129","a130","a131","a132","a133","a134","a135","a136","a137","a138","a139","a140","a141","a142","a143","a144","a145","a146","a147","a148","a149","a150","a151","a152","a153","a154","a155","a156","a157","a158","a159","a160","a161","a162","a163","a164","a165","a166","a167","a168","a169","a170","a171","a172","a173","a174","a175","a176","a177","a178","a179","a180","a181","a182","a183","a184","a185","a186","a187","a188","a189","a190","a191","a192","a193","a194","a195","a196","a197","a198","a199","a200","a201","a202","a203","a204","a205","a206","a207","a208","a209","a210","a211","a212","a213","a214","a215","a216","a217","a218","a219","a220","a221","a222","a223","a224","a225","a226","a227","a228","a229","a230","a231","a232","a233","a234","a235","a236","a237","a238","a239","a240","a241","a242","a243","a244","a245","a246","a247","a248","a249","a250","a251","a252","a253","a254","a255","s3","s4","s5","s6","s7","s8","s9","s10","s11","s12","s13","s14","s15","s16","s17","s18","s19","s20","s21","s22","s23","s24","s25","s26","s27","s28","s29","s30","s31","s33","s34","s35","s36","s37","s38","s39","s40","s41","s42","s43","s44","s45","s46","s47","s48","s49","s50","s51","s52","s53","s54","s55","s56","s57","s58","s59","s60","s61","s62","s63","s64","s65","s66","s67","s68","s69","s70","s71","s72","s73","s74","s75","s76","s77","s78","s79","s80","s81","s82","s83","s84","s85","s86","s87","s88","s89","s90","s91","s92","s93","s94","s95","s96","s97","s98","s99","vcc","scc","memory");
}

extern "C" void kernel_launch(void* const* d_in, const int* in_sizes, int n_in, void* d_out, int out_size, void* d_ws, size_t ws_size,
                              hipStream_t stream) {
  gm_fused<<<256, 256, 0, stream>>>(d_in[0], d_in[1], d_in[2], d_in[4], d_in[5], d_in[6], d_out, d_ws);
}
```
